```python
import torch

def _softmax(x, axis=-1):
    return torch.softmax(x, dim=axis)
def _gelu(x):
    return torch.nn.functional.gelu(x, approximate="tanh")
def _silu(x):
    return torch.nn.functional.silu(x)
def _sigmoid(x):
    return torch.sigmoid(x)
def _layernorm(x, axis=-1, eps=1e-5):
    m = x.mean(dim=axis, keepdim=True)
    v = x.var(dim=axis, keepdim=True, unbiased=False)
    return (x - m) / torch.sqrt(v + eps)
def _scatter_add(tgt, idx, src, axis=0):
    return tgt.clone().index_add_(axis, idx, src)
def _segment_sum(src, seg, n):
    out = torch.zeros((n,) + src.shape[1:], dtype=src.dtype)
    return out.index_add_(0, seg, src)
def _pad(x, pw):
    flat = tuple(v for pair in reversed(pw) for v in pair)
    return torch.nn.functional.pad(x, flat)

kernel_name = 'interleave_layers__linear_attn_block__swiglu_ffn__prompt'

def setup_inputs():

    _w = (lambda a: (lambda t: t.to(torch.bfloat16) if t.is_floating_point() else t)(torch.as_tensor(a)))
    def _A_setup():
        B, S, D = 1, 2048, 640
        return {
            "x": _w((lambda *s: torch.randn(*s, dtype=torch.bfloat16) * 0.05)(B, S, D)),
            "wqkv": _w((lambda *s: torch.randn(*s, dtype=torch.bfloat16) * 0.05)(D, 3 * D)),
            "wo": _w((lambda *s: torch.randn(*s, dtype=torch.bfloat16) * 0.05)(D, D)),
        }
    _A_in = _A_setup()

    def _B_setup():
        B, S, D, F = 1, 2048, 640, 1728
        return {
            "x": _w((lambda *s: torch.randn(*s, dtype=torch.bfloat16) * 0.05)(B, S, D)),
            "w_gate": _w((lambda *s: torch.randn(*s, dtype=torch.bfloat16) * 0.05)(D, F)),
            "w_up": _w((lambda *s: torch.randn(*s, dtype=torch.bfloat16) * 0.05)(D, F)),
            "w_down": _w((lambda *s: torch.randn(*s, dtype=torch.bfloat16) * 0.05)(F, D)),
        }
    _B_in = _B_setup()

    return {
        "A_x": _A_in["x"],
        "A_wqkv": _A_in["wqkv"],
        "A_wo": _A_in["wo"],
        "B_x": _B_in["x"],
        "B_w_gate": _B_in["w_gate"],
        "B_w_up": _B_in["w_up"],
        "B_w_down": _B_in["w_down"],
    }

def reference(A_x, A_wqkv, A_wo, B_x, B_w_gate, B_w_up, B_w_down):
    def _A_ref(x, wqkv, wo):
        B, S, D = x.shape
        H = 32; Dh = D // H
        qkv = (lambda x, *d: x.permute(*d))((x @ wqkv).reshape(B, S, 3, H, Dh), 2, 0, 3, 1, 4)
        q, k, v = qkv[0], qkv[1], qkv[2]
        phi = lambda t: torch.where(t > 0, t + 1, torch.exp(t)) + 1e-6
        pq, pk = phi(q), phi(k)
        kv = torch.einsum("bhsd,bhse->bhde", pk, v)
        z = torch.einsum("bhsd->bhd", pk)
        num = torch.einsum("bhqd,bhde->bhqe", pq, kv)
        den = torch.einsum("bhqd,bhd->bhq", pq, z)[..., None] + 1e-6
        o = num / den
        return (lambda x, *d: x.permute(*d))(o, 0, 2, 1, 3).reshape(B, S, D) @ wo

    def _B_ref(x, w_gate, w_up, w_down):
        g = _silu(x @ w_gate)
        u = x @ w_up
        return (g * u) @ w_down

    x = A_x
    x = x + _A_ref(_layernorm(x, axis=-1), A_wqkv, A_wo)
    x = x + _B_ref(_layernorm(x, axis=-1), B_w_gate, B_w_up, B_w_down)
    x = x + _A_ref(_layernorm(x, axis=-1), A_wqkv, A_wo)
    x = x + _B_ref(_layernorm(x, axis=-1), B_w_gate, B_w_up, B_w_down)
    return x
```

```cpp
#include <hip/hip_runtime.h>
#include <hip/hip_bf16.h>
typedef unsigned short u16;
typedef short bf16x8 __attribute__((ext_vector_type(8)));
typedef float f32x4 __attribute__((ext_vector_type(4)));
#define S_ 2048
#define D_ 640
#define F_ 1728
#define H_ 32
#define DH 20
__device__ inline float bf2f(u16 b){ return __builtin_bit_cast(float, (unsigned)b << 16); }
__device__ inline u16 f2bf(float f){ return __builtin_bit_cast(u16, (__bf16)f); }

__global__ void k_ln(const u16* x, u16* h) {
    int row = blockIdx.x; int t = threadIdx.x;
    float v[10]; float s = 0;
    for (int i = 0; i < 10; ++i) { v[i] = bf2f(x[row * D_ + t + 64 * i]); s += v[i]; }
    for (int o = 32; o; o >>= 1) s += __shfl_xor(s, o);
    float m = s / D_; float q = 0;
    for (int i = 0; i < 10; ++i) { float d = v[i] - m; q += d * d; }
    for (int o = 32; o; o >>= 1) q += __shfl_xor(q, o);
    float r = rsqrtf(q / D_ + 1e-5f);
    for (int i = 0; i < 10; ++i) h[row * D_ + t + 64 * i] = f2bf((v[i] - m) * r);
}
template <int MODE>
__global__ void k_gemm(const u16* A, const u16* B, u16* C, const u16* res, int N, int K, int ldc) {
    __shared__ u16 As[64][40];
    __shared__ u16 Bs[64][40];
    int bm = blockIdx.y * 64, bn = blockIdx.x * 64;
    int t = threadIdx.x, lane = t & 63, w = t >> 6;
    f32x4 acc[4] = {};
    for (int k0 = 0; k0 < K; k0 += 32) {
        { int r = t >> 2, c = (t & 3) * 8; *(uint4*)&As[r][c] = *(const uint4*)&A[(size_t)(bm + r) * K + k0 + c]; }
        { int kk = t >> 3, c = (t & 7) * 8; uint4 v = *(const uint4*)&B[(size_t)(k0 + kk) * N + bn + c];
          u16* p = (u16*)&v; for (int j = 0; j < 8; ++j) Bs[c + j][kk] = p[j]; }
        __syncthreads();
        bf16x8 a = *(bf16x8*)&As[w * 16 + (lane & 15)][(lane >> 4) * 8];
        for (int n = 0; n < 4; ++n) {
            bf16x8 b = *(bf16x8*)&Bs[n * 16 + (lane & 15)][(lane >> 4) * 8];
            acc[n] = __builtin_amdgcn_mfma_f32_16x16x32_bf16(a, b, acc[n], 0, 0, 0);
        }
        __syncthreads();
    }
    for (int n = 0; n < 4; ++n) for (int j = 0; j < 4; ++j) {
        int r = bm + w * 16 + (lane >> 4) * 4 + j, c = bn + n * 16 + (lane & 15);
        float v = acc[n][j];
        if (MODE == 1) v = bf2f(f2bf(v)) + bf2f(res[(size_t)r * ldc + c]);
        C[(size_t)r * ldc + c] = f2bf(v);
    }
}
__device__ inline float phi(float t) { return (t > 0.f ? t + 1.f : __expf(t)) + 1e-6f; }
__global__ void k_kv(const u16* qkv, float* kv) {
    int h = blockIdx.x; int t = threadIdx.x;
    if (t >= 420) return;
    int d = t / 21, e = t % 21;
    float s = 0;
    for (int r = 0; r < S_; ++r) {
        float pk = phi(bf2f(qkv[(size_t)r * 1920 + 640 + h * DH + d]));
        float v = e < 20 ? bf2f(qkv[(size_t)r * 1920 + 1280 + h * DH + e]) : 1.f;
        s += pk * v;
    }
    kv[h * 420 + t] = s;
}
__global__ void k_o(const u16* qkv, const float* kv, u16* o) {
    int r = blockIdx.x; int c = threadIdx.x;
    int h = c / DH, e = c % DH;
    float num = 0, den = 0;
    for (int d = 0; d < DH; ++d) {
        float pq = phi(bf2f(qkv[(size_t)r * 1920 + h * DH + d]));
        num += pq * kv[h * 420 + d * 21 + e];
        den += pq * kv[h * 420 + d * 21 + 20];
    }
    o[(size_t)r * D_ + c] = f2bf(num / (den + 1e-6f));
}
__global__ void k_act(const u16* g, const u16* u, u16* a, int n) {
    int i = blockIdx.x * blockDim.x + threadIdx.x;
    if (i < n) { float x = bf2f(g[i]); a[i] = f2bf(x / (1.f + __expf(-x)) * bf2f(u[i])); }
}
__global__ void k_nop(int* p) { if (p == nullptr) __builtin_trap(); }
#define NOPS 0
extern "C" void kernel_launch(void* const* d_in, const int* in_sizes, int n_in,
                              void* d_out, int out_size, void* d_ws, size_t ws_size,
                              hipStream_t stream) {
    const u16* x0 = (const u16*)d_in[0]; const u16* wqkv = (const u16*)d_in[1]; const u16* wo = (const u16*)d_in[2];
    const u16* wg = (const u16*)d_in[4]; const u16* wu = (const u16*)d_in[5]; const u16* wd = (const u16*)d_in[6];
    char* ws = (char*)d_ws;
    u16* h = (u16*)ws; u16* qkv = (u16*)(ws + (4 << 20)); float* kv = (float*)(ws + (16 << 20));
    u16* o = (u16*)(ws + (20 << 20)); u16* x1 = (u16*)(ws + (24 << 20)); u16* g = (u16*)(ws + (28 << 20));
    u16* u = (u16*)(ws + (36 << 20)); u16* a = (u16*)(ws + (44 << 20)); u16* x2 = (u16*)(ws + (52 << 20));
    const u16* x = x0;
    for (int l = 0; l < 2; ++l) {
        k_ln<<<S_, 64, 0, stream>>>(x, h);
        k_gemm<0><<<dim3(30, 32), 256, 0, stream>>>(h, wqkv, qkv, nullptr, 1920, 640, 1920);
        k_kv<<<H_, 448, 0, stream>>>(qkv, kv);
        k_o<<<S_, 640, 0, stream>>>(qkv, kv, o);
        k_gemm<1><<<dim3(10, 32), 256, 0, stream>>>(o, wo, x1, x, 640, 640, 640);
        k_ln<<<S_, 64, 0, stream>>>(x1, h);
        k_gemm<0><<<dim3(27, 32), 256, 0, stream>>>(h, wg, g, nullptr, F_, 640, F_);
        k_gemm<0><<<dim3(27, 32), 256, 0, stream>>>(h, wu, u, nullptr, F_, 640, F_);
        k_act<<<S_ * F_ / 256, 256, 0, stream>>>(g, u, a, S_ * F_);
        u16* xo = l == 0 ? x2 : (u16*)d_out;
        k_gemm<1><<<dim3(10, 32), 256, 0, stream>>>(a, wd, xo, x1, 640, F_, 640);
        x = xo;
    }
    for (int i = 0; i < NOPS; ++i) k_nop<<<256, 256, 0, stream>>>((int*)d_ws);
}
```

```cpp
#include <hip/hip_runtime.h>
#include <stdint.h>
#define WQ_STRIDE 153600
#define W_QKV 0
#define W_O 2458624
#define W_GU 3277824
#define W_D 7865344
#define LBASE 10223616
#define L_FLAG 15990784
#define LSTR 16449536
#define L_TAG 26279936
typedef unsigned u4 __attribute__((ext_vector_type(4)));
typedef const unsigned short* W;

__device__ __forceinline__ u4 selector(int k, int q) {
  const unsigned one = (k >> 3) == q ? 0x3f80u << (16 * (k & 1)) : 0u;
  const int d = (k >> 1) & 3;
  return u4{d == 0 ? one : 0u, d == 1 ? one : 0u, d == 2 ? one : 0u, d == 3 ? one : 0u};
}
#define FETCH(T) asm volatile(T : "+v"(l), "+v"(n), "+v"(q) : [a] "v"(src[0]), [c] "v"(src[1]), [m] "s"(min(w * 40960, 118 * 1024)), [p] "s"(step), \
    [xa] "v"(xa), [xs] "s"(xs), [xr] "s"(u4{(unsigned)(uintptr_t)x, (unsigned)((uintptr_t)x >> 32), 2048 * 1280u, 0x20000u}), \
    [r] "s"(u4{(unsigned)(uintptr_t)mat, (unsigned)((uintptr_t)mat >> 32), 0x1000000u, 0x20000u}) : "s77", "scc", "memory")
#define KB (8 * ((n >> 2) & 1) + 4 * (n >> 3) + (n & 3))
#define DROP 0x7ff00000

__global__ void __launch_bounds__(256, 1) k_main(W x, W wqkv, W wo, W wg, W wu, W wd, char* ws, char* out) {
  __shared__ char lds[163840];
  const int tid = threadIdx.x, w = __builtin_amdgcn_readfirstlane(tid >> 6);
  int l = tid & 63, n = l & 15, q = l >> 4;
  const int b = blockIdx.x, xc = b & 7, t = (b >> 1 & 28) + (b >> 6), j = (xc & 1) * 8 + (t >> 2);
  asm volatile("s_getpc_b64 s[96:97]\nLkpc:\ns_mov_b32 s98, Lkend-Lkpc\ns_mov_b32 s99, 0x20000\ns_and_b32 s97, s97, 0xffff\n"
               "buffer_load_dwordx2 v[254:255], %0, s[96:99], 0 offen"
               : : "v"(t * 2048 + tid * 8) : "s96", "s97", "s98", "s99", "scc", "v254", "v255");
  W mat;
  int type, step, slot = DROP, k0, k1;
  int z = DROP;
  u4 src = {DROP, DROP, DROP, DROP}, dst = src;
  const int ln = l >> 2, lq = l & 3, od = xc & 1, pr = xc >> 1;
  const int xa = q * 1280 + ((q * 4 ^ n) << 4), xs = ((pr * 4 + (b >> 6)) * 8 + w * 2) * 20480;
  const int row = w * 160 + q * 8 + (ln & 3);
  if (t < 8 - od) {
    const int id = pr * 15 + od * 8 + t;
    type = 0; mat = wqkv; step = 3840;
    src[0] = row * 3840 + id * 64 + lq * 16;
    FETCH("s_nop 4\ns_add_u32 m0, %[m], 0\ns_mul_i32 s77, %[p], 0\nbuffer_load_dwordx4 %[a], %[r], s77 offen offset:0 lds\ns_add_u32 m0, %[m], 1024\ns_mul_i32 s77, %[p], 4\nbuffer_load_dwordx4 %[a], %[r], s77 offen offset:0 lds\ns_add_u32 m0, %[m], 2048\ns_mul_i32 s77, %[p], 32\nbuffer_load_dwordx4 %[a], %[r], s77 offen offset:0 lds\ns_add_u32 m0, %[m], 3072\ns_mul_i32 s77, %[p], 36\nbuffer_load_dwordx4 %[a], %[r], s77 offen offset:0 lds\ns_add_u32 m0, %[m], 4096\ns_mul_i32 s77, %[p], 64\nbuffer_load_dwordx4 %[a], %[r], s77 offen offset:0 lds\ns_add_u32 m0, %[m], 5120\ns_mul_i32 s77, %[p], 68\nbuffer_load_dwordx4 %[a], %[r], s77 offen offset:0 lds\ns_add_u32 m0, %[m], 6144\ns_mul_i32 s77, %[p], 96\nbuffer_load_dwordx4 %[a], %[r], s77 offen offset:0 lds\ns_add_u32 m0, %[m], 7168\ns_mul_i32 s77, %[p], 100\nbuffer_load_dwordx4 %[a], %[r], s77 offen offset:0 lds\ns_add_u32 m0, %[m], 8192\ns_mul_i32 s77, %[p], 128\nbuffer_load_dwordx4 %[a], %[r], s77 offen offset:0 lds\ns_add_u32 m0, %[m], 9216\ns_mul_i32 s77, %[p], 132\nbuffer_load_dwordx4 %[a], %[r], s77 offen offset:0 lds");
    const int part = id / 20, sp = id - part * 20, g = sp - sp / 5;
    const int a0 = part ? (part - 1) * 3072 : 102400, a1 = part ? 2048 - 256 + (part - 1) * 128 : 143360 - 256;
    const int q1 = part ? 256 : 128, s0 = part ? 5120 : 2048, s1 = part ? 5120 : 512;
    slot = L_TAG + id * 128; k0 = n; k1 = 16 + n;
#pragma unroll
    for (int k = 0; k < 2; ++k) {
      const int c = sp * 32 + k * 16 + n, h = c * 3277 >> 16, d = c - h * 20, hd = h & 1;
      const int aa = a0 + hd * 1024 + q * 256, ab = a1 + hd * 64 + q * q1, st = d < 16 ? s0 : s1;
      dst[k] = W_QKV + (h >> 1) * WQ_STRIDE + (d < 16 ? aa : ab) + d * 16 + w * 5 * st;
      dst[2 + k] = st;
    }
    const int za = 32768 + q * 32768 + g * 2048 + n * 64, zb = g * 512 + (w ? 4 + q : q) * 8192 + n * 16;
    const bool on = part < 2 && sp % 5 < 4 && q + (w == 2 ? part : 2) > 1;
    z = on ? LBASE + part * LSTR + L_FLAG + (w == 2 ? za : zb) : DROP;
  } else if (t < 22 - 2 * od) {
    const int id = pr * 27 + od * 15 + t - 8, up = id >= 54, sp = id - up * 54;
    type = 1; mat = up ? wu : wg; step = 3456;
    src[0] = row * 3456 + sp * 64 + lq * 16;
    FETCH("s_nop 4\ns_add_u32 m0, %[m], 0\ns_mul_i32 s77, %[p], 0\nbuffer_load_dwordx4 %[a], %[r], s77 offen offset:0 lds\ns_add_u32 m0, %[m], 1024\ns_mul_i32 s77, %[p], 4\nbuffer_load_dwordx4 %[a], %[r], s77 offen offset:0 lds\ns_add_u32 m0, %[m], 2048\ns_mul_i32 s77, %[p], 32\nbuffer_load_dwordx4 %[a], %[r], s77 offen offset:0 lds\ns_add_u32 m0, %[m], 3072\ns_mul_i32 s77, %[p], 36\nbuffer_load_dwordx4 %[a], %[r], s77 offen offset:0 lds\ns_add_u32 m0, %[m], 4096\ns_mul_i32 s77, %[p], 64\nbuffer_load_dwordx4 %[a], %[r], s77 offen offset:0 lds\ns_add_u32 m0, %[m], 5120\ns_mul_i32 s77, %[p], 68\nbuffer_load_dwordx4 %[a], %[r], s77 offen offset:0 lds\ns_add_u32 m0, %[m], 6144\ns_mul_i32 s77, %[p], 96\nbuffer_load_dwordx4 %[a], %[r], s77 offen offset:0 lds\ns_add_u32 m0, %[m], 7168\ns_mul_i32 s77, %[p], 100\nbuffer_load_dwordx4 %[a], %[r], s77 offen offset:0 lds\ns_add_u32 m0, %[m], 8192\ns_mul_i32 s77, %[p], 128\nbuffer_load_dwordx4 %[a], %[r], s77 offen offset:0 lds\ns_add_u32 m0, %[m], 9216\ns_mul_i32 s77, %[p], 132\nbuffer_load_dwordx4 %[a], %[r], s77 offen offset:0 lds\ns_add_u32 m0, %[m], 10240\ns_add_u32 s77, %[xs], 0\nbuffer_load_dwordx4 %[xa], %[xr], s77 offen offset:0 lds\ns_add_u32 m0, %[m], 11264\ns_add_u32 s77, %[xs], 5120\nbuffer_load_dwordx4 %[xa], %[xr], s77 offen offset:0 lds");
    k0 = KB; k1 = 16 + k0;
    dst[0] = W_GU + sp * 81920 + q * 512 + up * 256 + n * 16 + w * 10240;
    dst[1] = dst[0] + 40960;
    dst[2] = dst[3] = 2048;
  } else {
    const int id = pr * 19 + od * 12 + t - 22;
    const bool o = id < 20, live = t < 32 - 3 * od;
    type = 2; mat = o ? wo : wd; step = o ? 20 * 1280 : 4 * 1280;
    int f, d, st;
    if (o) {
      const int u = id / 5, ks = id - u * 5;
      f = ks < 4 ? 40 * (4 * u + ks) + 4 * q : 40 * (4 * u + q) + 16;
      d = W_O + u * 25600 + ks * 5120; st = 4 * 25600;
    } else {
      const int u = (id - 20) / 14, ks = id - 20 - u * 14;
      const int jj = ks < 12 ? 4 * u + (ks & 3) : 4 * u + 2 * (ks - 12) + (q >> 1);
      f = 112 * jj + 16 * (ks < 12 ? 2 * (ks >> 2) + (q >> 1) : 6) + 8 * (q & 1);
      d = W_D + u * 71680 + ks * 5120; st = 4 * 71680;
    }
    const int a = live && f < 1728 ? (f + (ln & 3)) * 1280 + w * 320 : DROP;
    src[0] = a + lq * 16;
    src[1] = a + 128 + (lq >> 1) * step + (lq & 1) * 16;
    FETCH("s_nop 4\ns_add_u32 m0, %[m], 0\ns_add_u32 s77, 0, 0\nbuffer_load_dwordx4 %[a], %[r], s77 offen offset:0 lds\ns_add_u32 m0, %[m], 1024\ns_add_u32 s77, %[p], 0\nbuffer_load_dwordx4 %[a], %[r], s77 offen offset:0 lds\ns_add_u32 m0, %[m], 2048\ns_add_u32 s77, 0, 64\nbuffer_load_dwordx4 %[a], %[r], s77 offen offset:0 lds\ns_add_u32 m0, %[m], 3072\ns_add_u32 s77, %[p], 64\nbuffer_load_dwordx4 %[a], %[r], s77 offen offset:0 lds\ns_add_u32 m0, %[m], 4096\ns_mov_b32 s77, 0\nbuffer_load_dwordx4 %[c], %[r], s77 offen offset:0 lds\ns_add_u32 m0, %[m], 5120\ns_add_u32 s77, 0, 160\nbuffer_load_dwordx4 %[a], %[r], s77 offen offset:0 lds\ns_add_u32 m0, %[m], 6144\ns_add_u32 s77, %[p], 160\nbuffer_load_dwordx4 %[a], %[r], s77 offen offset:0 lds\ns_add_u32 m0, %[m], 7168\ns_add_u32 s77, 0, 224\nbuffer_load_dwordx4 %[a], %[r], s77 offen offset:0 lds\ns_add_u32 m0, %[m], 8192\ns_add_u32 s77, %[p], 224\nbuffer_load_dwordx4 %[a], %[r], s77 offen offset:0 lds\ns_add_u32 m0, %[m], 9216\ns_mov_b32 s77, 160\nbuffer_load_dwordx4 %[c], %[r], s77 offen offset:0 lds\ns_add_u32 m0, %[m], 10240\ns_add_u32 s77, %[xs], 0\nbuffer_load_dwordx4 %[xa], %[xr], s77 offen offset:0 lds\ns_add_u32 m0, %[m], 11264\ns_add_u32 s77, %[xs], 5120\nbuffer_load_dwordx4 %[xa], %[xr], s77 offen offset:0 lds");
    k0 = 8 * (n >> 2) + (n & 3); k1 = k0 + 4;
    dst[0] = live ? d + w * 2 * st + l * 16 : DROP;
    dst[1] = dst[0] + st;
  }
  u4 e0 = selector(k0, q), e1 = selector(k1, q), e2 = selector(KB, q), e3 = selector(KB + 16, q);
  src[2] = L_TAG + (((l >> 1) & 3) * 20 + (5 * j >> 2) + (l & 1)) * 128;
  src[3] = w < 3 ? z : l < 40 ? W_GU + 108 * 40960 + (b * 40 + l) * 16 : DROP;
  u4 par = {(unsigned)type, (unsigned)step, (unsigned)slot, 0u};
  uintptr_t pw = (uintptr_t)ws, px = (uintptr_t)x, pm = (uintptr_t)mat;
  const unsigned lp = (unsigned)(uintptr_t)(__attribute__((address_space(3))) char*)lds;
  asm volatile(
      "v_mov_b32 v35, 0\n"
      "v_readfirstlane_b32 s84, v224\n"
      "v_readfirstlane_b32 s70, v225\n"
      "s_mov_b32 s38, 0x1000000\n"
      "s_mov_b32 s39, 0x20000\n"
      "s_mov_b32 s30, 2621440\n"
      "s_mov_b32 s31, 0x20000\n"
      "s_mov_b32 s22, 43122688\n"
      "s_mov_b32 s23, 0x20000\n"
      "s_mul_i32 s14, %[w], 40960\n"
      "s_min_u32 s14, s14, 120832\n"
      "s_cmp_lg_u32 s84, 0\n"
      "s_cbranch_scc1 LJg_%=\n"
      "buffer_load_dwordx2 v[238:239], v226, s[20:23], 0 offen sc1\n"
      "s_mov_b32 s40, %[w]\n"
      "s_and_b32 s75, %[b], 7\n"
      "s_lshr_b32 s76, %[b], 3\n"
      "s_lshr_b32 s77, s75, 1\n"
      "s_lshl_b32 s77, s77, 2\n"
      "s_lshr_b32 s78, s76, 3\n"
      "s_add_u32 s90, s77, s78\n"
      "s_and_b32 s77, s75, 1\n"
      "s_lshl_b32 s77, s77, 3\n"
      "s_and_b32 s78, s76, 7\n"
      "s_add_u32 s91, s77, s78\n"
      "v_mbcnt_lo_u32_b32 v11, -1, 0\n"
      "v_mbcnt_hi_u32_b32 v11, -1, v11\n"
      "v_lshlrev_b32 v0, 4, v11\n"
      "v_and_b32 v40, 15, v11\n"
      "v_mul_u32_u24 v36, 1280, v40\n"
      "v_and_b32 v37, 0x30, v11\n"
      "v_add_u32 v190, v36, v37\n"
      "v_lshrrev_b32 v46, 4, v11\n"
      "v_lshlrev_b32 v47, 2, v46\n"
      "v_xor_b32 v47, v47, v40\n"
      "v_mul_u32_u24 v45, 1280, v46\n"
      "v_lshl_add_u32 v192, v47, 4, v45\n"
      "s_lshl_b32 s77, s90, 3\n"
      "s_lshl_b32 s78, s40, 1\n"
      "s_add_u32 s93, s77, s78\n"
      "s_mul_i32 s42, s93, 20480\n"
      "s_lshr_b32 s75, %[b], 3\n"
      "s_lshl_b32 s75, s75, 2\n"
      "s_add_u32 s75, s75, s40\n"
      "v_lshlrev_b32 v248, 7, v11\n"
      "v_add_u32 v248, s75, v248\n"
      "v_lshlrev_b32 v249, 16, v248\n"
      "buffer_load_dword v250, v249, s[20:23], 0 offen sc1\n"
      "v_lshlrev_b32 v249, 16, v248\n"
      "s_mov_b64 s[96:97], %[o]\n"
      "s_mov_b32 s98, 2621440\n"
      "s_mov_b32 s99, 0x20000\n"
      "buffer_load_dword v252, v249, s[96:99], 0 offen sc1\n"
      "v_lshl_add_u32 v45, v40, 6, v37\n"
      "v_add_u32 v193, s14, v45\n"
      "v_and_b32 v45, 0xff, v45\n"
      "v_lshl_add_u32 v45, v40, 8, v45\n"
      "v_add_u32 v194, s14, v45\n"
      "v_xor_b32 v195, 64, v194\n"
      "v_xor_b32 v196, 128, v194\n"
      "v_xor_b32 v197, 192, v194\n"
      "s_add_u32 s24, s20, 10223616\n"
      "s_addc_u32 s25, s21, 0\n"
      "s_mov_b32 s26, 32899072\n"
      "s_mov_b32 s27, 0x20000\n"
      "s_mov_b32 s41, 0\n"
      "s_mul_i32 s71, s40, 13312\n"
      "s_mul_i32 s72, s40, 5120\n"
      "s_mul_i32 s77, s91, 153600\n"
      "s_add_u32 s65, s77, s72\n"
      "s_add_u32 s66, s77, s71\n"
      "s_and_b32 s92, s91, 7\n"
      "s_lshl_b32 s77, s90, 3\n"
      "s_lshr_b32 s78, s91, 3\n"
      "s_lshl_b32 s78, s78, 2\n"
      "s_add_u32 s75, s77, s78\n"
      "s_add_u32 s94, s75, s40\n"
      "s_lshl_b32 s43, s93, 10\n"
      "s_mul_i32 s44, s94, 20480\n"
      "s_mul_i32 s77, s92, 160\n"
      "s_add_u32 s46, s44, s77\n"
      "s_mul_i32 s77, s92, 2560\n"
      "s_add_u32 s44, s44, s77\n"
      "s_lshl_b32 s45, s94, 10\n"
      "s_lshl_b32 s77, s92, 7\n"
      "s_add_u32 s45, s45, s77\n"
      "s_mul_i32 s58, s40, 15360\n"
      "s_mul_i32 s76, s40, 14336\n"
      "s_add_u32 s77, s40, 1\n"
      "s_and_b32 s77, s77, 3\n"
      "s_mul_i32 s55, s77, 15360\n"
      "s_add_u32 s55, s55, 0\n"
      "s_add_u32 s77, s77, s75\n"
      "s_mul_i32 s47, s77, 20480\n"
      "s_add_u32 s47, s47, s72\n"
      "s_mul_i32 s51, s77, 57344\n"
      "s_add_u32 s51, s51, s76\n"
      "s_add_u32 s77, s40, 2\n"
      "s_and_b32 s77, s77, 3\n"
      "s_mul_i32 s56, s77, 15360\n"
      "s_add_u32 s56, s56, 5120\n"
      "s_add_u32 s77, s77, s75\n"
      "s_mul_i32 s48, s77, 20480\n"
      "s_add_u32 s48, s48, s72\n"
      "s_mul_i32 s52, s77, 57344\n"
      "s_add_u32 s52, s52, s76\n"
      "s_add_u32 s77, s40, 3\n"
      "s_and_b32 s77, s77, 3\n"
      "s_mul_i32 s57, s77, 15360\n"
      "s_add_u32 s57, s57, 10240\n"
      "s_add_u32 s77, s77, s75\n"
      "s_mul_i32 s49, s77, 20480\n"
      "s_add_u32 s49, s49, s72\n"
      "s_mul_i32 s53, s77, 57344\n"
      "s_add_u32 s53, s53, s76\n"
      "s_add_u32 s77, s40, 4\n"
      "s_and_b32 s77, s77, 3\n"
      "s_add_u32 s77, s77, s75\n"
      "s_mul_i32 s50, s77, 20480\n"
      "s_add_u32 s50, s50, s72\n"
      "s_mul_i32 s54, s77, 57344\n"
      "s_add_u32 s54, s54, s76\n"
      "s_lshr_b32 s75, s91, 2\n"
      "s_and_b32 s76, s91, 3\n"
      "s_mul_i32 s77, s75, 5120\n"
      "s_add_u32 s77, s77, s42\n"
      "s_lshl_b32 s78, s76, 10\n"
      "s_add_u32 s59, s77, s78\n"
      "s_lshl_b32 s78, s76, 8\n"
      "s_add_u32 s60, s77, s78\n"
      "s_mul_i32 s77, s93, 57344\n"
      "s_mul_i32 s78, s75, 14336\n"
      "s_add_u32 s77, s77, s78\n"
      "s_lshl_b32 s78, s76, 10\n"
      "s_add_u32 s61, s77, s78\n"
      "s_lshl_b32 s78, s76, 9\n"
      "s_add_u32 s62, s77, s78\n"
      "s_lshl_b32 s77, s91, 15\n"
      "s_lshl_b32 s78, s40, 13\n"
      "s_add_u32 s64, s77, s78\n"
      "s_lshl_b32 s73, s90, 9\n"
      "s_lshl_b32 s74, s91, 9\n"
      "s_add_u32 s63, s64, s73\n"
      "s_mul_i32 s67, s91, 286720\n"
      "s_add_u32 s67, s67, s72\n"
      "s_lshl_b32 s77, s92, 2\n"
      "s_add_u32 s77, s77, s40\n"
      "s_mul_i32 s68, s77, 25600\n"
      "s_mul_i32 s69, s77, 71680\n"
      "s_mov_b32 s86, 0x35c1a2a0\n"
      "s_mov_b32 s87, 0x35c1a2a0\n"
      "s_mov_b32 s88, 0x3f317218\n"
      "s_mov_b32 s89, 0x3f317218\n"
      "v_bfe_u32 v41, v11, 4, 1\n"
      "v_lshrrev_b32 v42, 5, v11\n"
      "v_lshlrev_b32 v43, 4, v40\n"
      "v_lshl_add_u32 v44, v42, 8, v43\n"
      "v_lshl_add_u32 v36, v41, 4, v36\n"
      "v_lshl_add_u32 v191, v42, 3, v36\n"
      "v_mov_b32 v45, 20480\n"
      "v_mad_u32_u24 v1, v41, v45, v44\n"
      "v_mov_b32 v45, 57344\n"
      "v_mad_u32_u24 v2, v41, v45, v44\n"
      "v_lshl_add_u32 v5, v41, 3, v44\n"
      "v_lshlrev_b32 v45, 2, v40\n"
      "v_lshl_add_u32 v34, v42, 6, v45\n"
      "v_lshl_add_u32 v3, v41, 10, v34\n"
      "v_lshl_add_u32 v45, v41, 8, v43\n"
      "v_lshl_add_u32 v4, v42, 3, v45\n"
      "v_and_b32 v45, 0x70, v0\n"
      "v_lshrrev_b32 v46, 4, v11\n"
      "v_lshl_add_u32 v45, v46, 7, v45\n"
      "v_add_u32 v6, 143360, v45\n"
      "v_add_u32 v7, 0x10000, v0\n"
      "v_add_u32 v8, 0x20000, v0\n"
      "v_mov_b32 v9, 0\n"
      "v_mov_b32 v10, 1\n"
      "v_mov_b32 v32, v0\n"
      "v_mov_b32 v33, v4\n"
      "s_add_u32 s81, s44, 16449536\n"
      "s_add_u32 s82, s44, 16450560\n"
      "s_add_u32 s83, s44, 16451584\n"
      "v_mov_b32 v176, 0x3f803f80\n"
      "v_mov_b32 v177, 0x3f803f80\n"
      "v_mov_b32 v178, 0x3f803f80\n"
      "v_mov_b32 v179, 0x3f803f80\n"
      "v_mov_b32 v240, 0\n"
      "v_mov_b32 v241, 0\n"
      "v_mov_b32 v242, 0\n"
      "v_mov_b32 v243, 0\n"
      "buffer_store_dwordx4 v[240:243], v219, s[20:23], 0 offen sc1\n"
      "s_add_u32 m0, s14, 10240\n"
      "s_add_u32 s80, s42, 0\n"
      "buffer_load_dwordx4 v192, s[28:31], s80 offen offset:0 lds\n"
      "s_add_u32 m0, s14, 11264\n"
      "s_add_u32 s80, s42, 5120\n"
      "buffer_load_dwordx4 v192, s[28:31], s80 offen offset:0 lds\n"
      "s_add_u32 m0, s14, 12288\n"
      "s_add_u32 s80, s42, 10240\n"
      "buffer_load_dwordx4 v192, s[28:31], s80 offen offset:0 lds\n"
      "s_add_u32 m0, s14, 13312\n"
      "s_add_u32 s80, s42, 15360\n"
      "buffer_load_dwordx4 v192, s[28:31], s80 offen offset:0 lds\n"
      "s_add_u32 m0, s14, 14336\n"
      "s_add_u32 s80, s42, 20480\n"
      "buffer_load_dwordx4 v192, s[28:31], s80 offen offset:0 lds\n"
      "s_add_u32 m0, s14, 15360\n"
      "s_add_u32 s80, s42, 25600\n"
      "buffer_load_dwordx4 v192, s[28:31], s80 offen offset:0 lds\n"
      "s_add_u32 m0, s14, 16384\n"
      "s_add_u32 s80, s42, 30720\n"
      "buffer_load_dwordx4 v192, s[28:31], s80 offen offset:0 lds\n"
      "s_add_u32 m0, s14, 17408\n"
      "s_add_u32 s80, s42, 35840\n"
      "buffer_load_dwordx4 v192, s[28:31], s80 offen offset:0 lds\n"
      "s_add_u32 m0, s14, 18432\n"
      "s_add_u32 s80, s42, 256\n"
      "buffer_load_dwordx4 v192, s[28:31], s80 offen offset:0 lds\n"
      "s_add_u32 m0, s14, 19456\n"
      "s_add_u32 s80, s42, 5376\n"
      "buffer_load_dwordx4 v192, s[28:31], s80 offen offset:0 lds\n"
      "s_add_u32 m0, s14, 20480\n"
      "s_add_u32 s80, s42, 10496\n"
      "buffer_load_dwordx4 v192, s[28:31], s80 offen offset:0 lds\n"
      "s_add_u32 m0, s14, 21504\n"
      "s_add_u32 s80, s42, 15616\n"
      "buffer_load_dwordx4 v192, s[28:31], s80 offen offset:0 lds\n"
      "s_add_u32 m0, s14, 22528\n"
      "s_add_u32 s80, s42, 20736\n"
      "buffer_load_dwordx4 v192, s[28:31], s80 offen offset:0 lds\n"
      "s_add_u32 m0, s14, 23552\n"
      "s_add_u32 s80, s42, 25856\n"
      "buffer_load_dwordx4 v192, s[28:31], s80 offen offset:0 lds\n"
      "s_waitcnt vmcnt(27)\n"
      "ds_read_b128 v[48:51], v193 offset:0\n"
      "s_waitcnt vmcnt(26)\n"
      "ds_read_b128 v[52:55], v193 offset:1024\n"
      "s_waitcnt vmcnt(25)\n"
      "ds_read_b128 v[56:59], v193 offset:2048\n"
      "s_waitcnt vmcnt(24)\n"
      "ds_read_b128 v[60:63], v193 offset:3072\n"
      "s_waitcnt vmcnt(23)\n"
      "ds_read_b128 v[64:67], v193 offset:4096\n"
      "s_waitcnt vmcnt(22)\n"
      "ds_read_b128 v[68:71], v193 offset:5120\n"
      "s_waitcnt vmcnt(21)\n"
      "ds_read_b128 v[72:75], v193 offset:6144\n"
      "s_waitcnt vmcnt(20)\n"
      "ds_read_b128 v[76:79], v193 offset:7168\n"
      "s_waitcnt vmcnt(19)\n"
      "ds_read_b128 v[80:83], v193 offset:8192\n"
      "s_waitcnt vmcnt(18)\n"
      "ds_read_b128 v[84:87], v193 offset:9216\n"
      "s_waitcnt lgkmcnt(9)\n"
      "v_mfma_f32_16x16x32_bf16 v[88:91], v[48:51], v[200:203], 0\n"
      "v_mfma_f32_16x16x32_bf16 v[92:95], v[48:51], v[204:207], 0\n"
      "s_waitcnt lgkmcnt(8)\n"
      "v_mfma_f32_16x16x32_bf16 v[88:91], v[52:55], v[200:203], v[88:91]\n"
      "v_mfma_f32_16x16x32_bf16 v[92:95], v[52:55], v[204:207], v[92:95]\n"
      "s_waitcnt lgkmcnt(7)\n"
      "v_mfma_f32_16x16x32_bf16 v[88:91], v[56:59], v[200:203], v[88:91]\n"
      "v_mfma_f32_16x16x32_bf16 v[92:95], v[56:59], v[204:207], v[92:95]\n"
      "s_waitcnt lgkmcnt(6)\n"
      "v_mfma_f32_16x16x32_bf16 v[88:91], v[60:63], v[200:203], v[88:91]\n"
      "v_mfma_f32_16x16x32_bf16 v[92:95], v[60:63], v[204:207], v[92:95]\n"
      "s_waitcnt lgkmcnt(5)\n"
      "v_mfma_f32_16x16x32_bf16 v[88:91], v[64:67], v[200:203], v[88:91]\n"
      "v_mfma_f32_16x16x32_bf16 v[92:95], v[64:67], v[204:207], v[92:95]\n"
      "s_waitcnt lgkmcnt(4)\n"
      "v_mfma_f32_16x16x32_bf16 v[88:91], v[68:71], v[200:203], v[88:91]\n"
      "v_mfma_f32_16x16x32_bf16 v[92:95], v[68:71], v[204:207], v[92:95]\n"
      "s_waitcnt lgkmcnt(3)\n"
      "v_mfma_f32_16x16x32_bf16 v[88:91], v[72:75], v[200:203], v[88:91]\n"
      "v_mfma_f32_16x16x32_bf16 v[92:95], v[72:75], v[204:207], v[92:95]\n"
      "s_waitcnt lgkmcnt(2)\n"
      "v_mfma_f32_16x16x32_bf16 v[88:91], v[76:79], v[200:203], v[88:91]\n"
      "v_mfma_f32_16x16x32_bf16 v[92:95], v[76:79], v[204:207], v[92:95]\n"
      "s_waitcnt lgkmcnt(1)\n"
      "v_mfma_f32_16x16x32_bf16 v[88:91], v[80:83], v[200:203], v[88:91]\n"
      "v_mfma_f32_16x16x32_bf16 v[92:95], v[80:83], v[204:207], v[92:95]\n"
      "s_waitcnt lgkmcnt(0)\n"
      "v_mfma_f32_16x16x32_bf16 v[88:91], v[84:87], v[200:203], v[88:91]\n"
      "v_mfma_f32_16x16x32_bf16 v[92:95], v[84:87], v[204:207], v[92:95]\n"
      "s_lshl_b32 s77, s40, 2\n"
      "s_add_u32 s77, s77, 160768\n"
      "v_add_u32 v45, s77, v44\n"
      "v_add_u32 v46, 160768, v43\n"
      "s_nop 2\n"
      "v_add_f32 v88, v88, v89\n"
      "v_add_f32 v90, v90, v91\n"
      "v_add_f32 v92, v92, v93\n"
      "v_add_f32 v94, v94, v95\n"
      "v_add_f32 v88, v88, v90\n"
      "v_add_f32 v92, v92, v94\n"
      "s_nop 1\n"
      "v_permlane32_swap_b32 v88, v92\n"
      "v_add_f32 v88, v88, v92\n"
      "v_mov_b32 v92, v88\n"
      "s_nop 1\n"
      "v_permlane16_swap_b32 v88, v92\n"
      "v_add_f32 v88, v88, v92\n"
      "ds_write_b32 v45, v88\n"
      "s_waitcnt lgkmcnt(0)\n"
      "s_barrier\n"
      "ds_read_b128 v[88:91], v46\n"
      "ds_read_b128 v[92:95], v46 offset:256\n"
      "s_waitcnt lgkmcnt(1)\n"
      "v_add_f32 v88, v88, v89\n"
      "v_add_f32 v90, v90, v91\n"
      "s_waitcnt lgkmcnt(0)\n"
      "v_add_f32 v92, v92, v93\n"
      "v_add_f32 v94, v94, v95\n"
      "v_add_f32 v88, v88, v90\n"
      "v_add_f32 v92, v92, v94\n"
      "v_mul_f32 v91, 0xbacccccd, v88\n"
      "v_mul_f32 v95, 0xbacccccd, v92\n"
      "v_mul_f32 v90, 0xbacccccd, v88\n"
      "v_mul_f32 v94, 0xbacccccd, v92\n"
      "v_mul_f32 v89, 0xbacccccd, v88\n"
      "v_mul_f32 v93, 0xbacccccd, v92\n"
      "v_mul_f32 v88, 0xbacccccd, v88\n"
      "v_mul_f32 v92, 0xbacccccd, v92\n"
      "s_nop 0\n"
      "v_mfma_f32_16x16x32_bf16 v[96:99], v[48:51], v[200:203], v[88:91]\n"
      "v_mfma_f32_16x16x32_bf16 v[100:103], v[52:55], v[200:203], v[88:91]\n"
      "v_mfma_f32_16x16x32_bf16 v[104:107], v[48:51], v[204:207], v[92:95]\n"
      "v_mfma_f32_16x16x32_bf16 v[108:111], v[52:55], v[204:207], v[92:95]\n"
      "v_mfma_f32_16x16x32_bf16 v[112:115], v[56:59], v[200:203], v[88:91]\n"
      "v_mfma_f32_16x16x32_bf16 v[116:119], v[60:63], v[200:203], v[88:91]\n"
      "s_nop 2\n"
      "v_cvt_pk_bf16_f32 v120, v96, v97\n"
      "v_cvt_pk_bf16_f32 v121, v98, v99\n"
      "v_cvt_pk_bf16_f32 v122, v100, v101\n"
      "v_cvt_pk_bf16_f32 v123, v102, v103\n"
      "v_mad_u32_u24 v228, v222, 0, v220\n"
      "buffer_store_dwordx4 v[120:123], v228, s[20:23], 0 offen sc1\n"
      "v_mfma_f32_16x16x32_bf16 v[96:99], v[56:59], v[204:207], v[92:95]\n"
      "v_mfma_f32_16x16x32_bf16 v[100:103], v[60:63], v[204:207], v[92:95]\n"
      "v_cvt_pk_bf16_f32 v124, v104, v105\n"
      "v_cvt_pk_bf16_f32 v125, v106, v107\n"
      "v_cvt_pk_bf16_f32 v126, v108, v109\n"
      "v_cvt_pk_bf16_f32 v127, v110, v111\n"
      "v_mad_u32_u24 v229, v223, 0, v221\n"
      "buffer_store_dwordx4 v[124:127], v229, s[20:23], 0 offen sc1\n"
      "v_mfma_f32_16x16x32_bf16 v[104:107], v[64:67], v[200:203], v[88:91]\n"
      "v_mfma_f32_16x16x32_bf16 v[108:111], v[68:71], v[200:203], v[88:91]\n"
      "v_cvt_pk_bf16_f32 v128, v112, v113\n"
      "v_cvt_pk_bf16_f32 v129, v114, v115\n"
      "v_cvt_pk_bf16_f32 v130, v116, v117\n"
      "v_cvt_pk_bf16_f32 v131, v118, v119\n"
      "v_mad_u32_u24 v230, v222, 1, v220\n"
      "buffer_store_dwordx4 v[128:131], v230, s[20:23], 0 offen sc1\n"
      "v_mfma_f32_16x16x32_bf16 v[112:115], v[64:67], v[204:207], v[92:95]\n"
      "v_mfma_f32_16x16x32_bf16 v[116:119], v[68:71], v[204:207], v[92:95]\n"
      "v_cvt_pk_bf16_f32 v132, v96, v97\n"
      "v_cvt_pk_bf16_f32 v133, v98, v99\n"
      "v_cvt_pk_bf16_f32 v134, v100, v101\n"
      "v_cvt_pk_bf16_f32 v135, v102, v103\n"
      "v_mad_u32_u24 v231, v223, 1, v221\n"
      "buffer_store_dwordx4 v[132:135], v231, s[20:23], 0 offen sc1\n"
      "v_mfma_f32_16x16x32_bf16 v[96:99], v[72:75], v[200:203], v[88:91]\n"
      "v_mfma_f32_16x16x32_bf16 v[100:103], v[76:79], v[200:203], v[88:91]\n"
      "v_cvt_pk_bf16_f32 v136, v104, v105\n"
      "v_cvt_pk_bf16_f32 v137, v106, v107\n"
      "v_cvt_pk_bf16_f32 v138, v108, v109\n"
      "v_cvt_pk_bf16_f32 v139, v110, v111\n"
      "v_mad_u32_u24 v232, v222, 2, v220\n"
      "buffer_store_dwordx4 v[136:139], v232, s[20:23], 0 offen sc1\n"
      "v_mfma_f32_16x16x32_bf16 v[104:107], v[72:75], v[204:207], v[92:95]\n"
      "v_mfma_f32_16x16x32_bf16 v[108:111], v[76:79], v[204:207], v[92:95]\n"
      "v_cvt_pk_bf16_f32 v140, v112, v113\n"
      "v_cvt_pk_bf16_f32 v141, v114, v115\n"
      "v_cvt_pk_bf16_f32 v142, v116, v117\n"
      "v_cvt_pk_bf16_f32 v143, v118, v119\n"
      "v_mad_u32_u24 v233, v223, 2, v221\n"
      "buffer_store_dwordx4 v[140:143], v233, s[20:23], 0 offen sc1\n"
      "v_mfma_f32_16x16x32_bf16 v[112:115], v[80:83], v[200:203], v[88:91]\n"
      "v_mfma_f32_16x16x32_bf16 v[116:119], v[84:87], v[200:203], v[88:91]\n"
      "v_cvt_pk_bf16_f32 v144, v96, v97\n"
      "v_cvt_pk_bf16_f32 v145, v98, v99\n"
      "v_cvt_pk_bf16_f32 v146, v100, v101\n"
      "v_cvt_pk_bf16_f32 v147, v102, v103\n"
      "v_mad_u32_u24 v234, v222, 3, v220\n"
      "buffer_store_dwordx4 v[144:147], v234, s[20:23], 0 offen sc1\n"
      "v_mfma_f32_16x16x32_bf16 v[96:99], v[80:83], v[204:207], v[92:95]\n"
      "v_mfma_f32_16x16x32_bf16 v[100:103], v[84:87], v[204:207], v[92:95]\n"
      "v_cvt_pk_bf16_f32 v148, v104, v105\n"
      "v_cvt_pk_bf16_f32 v149, v106, v107\n"
      "v_cvt_pk_bf16_f32 v150, v108, v109\n"
      "v_cvt_pk_bf16_f32 v151, v110, v111\n"
      "v_mad_u32_u24 v235, v223, 3, v221\n"
      "buffer_store_dwordx4 v[148:151], v235, s[20:23], 0 offen sc1\n"
      "v_cvt_pk_bf16_f32 v152, v112, v113\n"
      "v_cvt_pk_bf16_f32 v153, v114, v115\n"
      "v_cvt_pk_bf16_f32 v154, v116, v117\n"
      "v_cvt_pk_bf16_f32 v155, v118, v119\n"
      "v_mad_u32_u24 v236, v222, 4, v220\n"
      "buffer_store_dwordx4 v[152:155], v236, s[20:23], 0 offen sc1\n"
      "v_cvt_pk_bf16_f32 v156, v96, v97\n"
      "v_cvt_pk_bf16_f32 v157, v98, v99\n"
      "v_cvt_pk_bf16_f32 v158, v100, v101\n"
      "v_cvt_pk_bf16_f32 v159, v102, v103\n"
      "v_mad_u32_u24 v237, v223, 4, v221\n"
      "buffer_store_dwordx4 v[156:159], v237, s[20:23], 0 offen sc1\n"
      "s_add_u32 m0, s14, 24576\n"
      "s_add_u32 s80, s42, 30976\n"
      "buffer_load_dwordx4 v192, s[28:31], s80 offen offset:0 lds\n"
      "s_add_u32 m0, s14, 25600\n"
      "s_add_u32 s80, s42, 36096\n"
      "buffer_load_dwordx4 v192, s[28:31], s80 offen offset:0 lds\n"
      "s_add_u32 m0, s14, 26624\n"
      "s_add_u32 s80, s42, 512\n"
      "buffer_load_dwordx4 v192, s[28:31], s80 offen offset:0 lds\n"
      "s_add_u32 m0, s14, 27648\n"
      "s_add_u32 s80, s42, 5632\n"
      "buffer_load_dwordx4 v192, s[28:31], s80 offen offset:0 lds\n"
      "s_add_u32 m0, s14, 28672\n"
      "s_add_u32 s80, s42, 10752\n"
      "buffer_load_dwordx4 v192, s[28:31], s80 offen offset:0 lds\n"
      "s_add_u32 m0, s14, 29696\n"
      "s_add_u32 s80, s42, 15872\n"
      "buffer_load_dwordx4 v192, s[28:31], s80 offen offset:0 lds\n"
      "s_add_u32 m0, s14, 30720\n"
      "s_add_u32 s80, s42, 20992\n"
      "buffer_load_dwordx4 v192, s[28:31], s80 offen offset:0 lds\n"
      "s_add_u32 m0, s14, 31744\n"
      "s_add_u32 s80, s42, 26112\n"
      "buffer_load_dwordx4 v192, s[28:31], s80 offen offset:0 lds\n"
      "s_waitcnt vmcnt(35)\n"
      "v_add_u32 v238, 0x7f4a7c15, v238\n"
      "v_add_u32 v239, 0x9e3779b9, v239\n"
      "v_readfirstlane_b32 s34, v238\n"
      "v_readfirstlane_b32 s35, v239\n"
      "s_waitcnt vmcnt(8)\n"
      "s_barrier\n"
      "s_cmp_lg_u32 s40, 0\n"
      "s_cbranch_scc1 Ltag_%=\n"
      "s_mov_b64 exec, 1\n"
      "buffer_store_dwordx2 v[238:239], v226, s[20:23], 0 offen offset:8 sc1\n"
      "s_mov_b64 exec, -1\n"
      "Ltag_%=:\n"
      "s_add_u32 m0, s14, 32768\n"
      "s_add_u32 s80, s42, 31232\n"
      "buffer_load_dwordx4 v192, s[28:31], s80 offen offset:0 lds\n"
      "s_add_u32 m0, s14, 33792\n"
      "s_add_u32 s80, s42, 36352\n"
      "buffer_load_dwordx4 v192, s[28:31], s80 offen offset:0 lds\n"
      "s_add_u32 m0, s14, 34816\n"
      "s_add_u32 s80, s42, 768\n"
      "buffer_load_dwordx4 v192, s[28:31], s80 offen offset:0 lds\n"
      "s_add_u32 m0, s14, 35840\n"
      "s_add_u32 s80, s42, 5888\n"
      "buffer_load_dwordx4 v192, s[28:31], s80 offen offset:0 lds\n"
      "s_add_u32 m0, s14, 36864\n"
      "s_add_u32 s80, s42, 11008\n"
      "buffer_load_dwordx4 v192, s[28:31], s80 offen offset:0 lds\n"
      "s_add_u32 m0, s14, 37888\n"
      "s_add_u32 s80, s42, 16128\n"
      "buffer_load_dwordx4 v192, s[28:31], s80 offen offset:0 lds\n"
      "s_add_u32 m0, s14, 0\n"
      "s_add_u32 s80, s42, 21248\n"
      "buffer_load_dwordx4 v192, s[28:31], s80 offen offset:0 lds\n"
      "s_add_u32 m0, s14, 1024\n"
      "s_add_u32 s80, s42, 26368\n"
      "buffer_load_dwordx4 v192, s[28:31], s80 offen offset:0 lds\n"
      "s_add_u32 m0, s14, 2048\n"
      "s_add_u32 s80, s42, 31488\n"
      "buffer_load_dwordx4 v192, s[28:31], s80 offen offset:0 lds\n"
      "s_add_u32 m0, s14, 3072\n"
      "s_add_u32 s80, s42, 36608\n"
      "buffer_load_dwordx4 v192, s[28:31], s80 offen offset:0 lds\n"
      "s_add_u32 m0, s14, 4096\n"
      "s_add_u32 s80, s42, 1024\n"
      "buffer_load_dwordx4 v192, s[28:31], s80 offen offset:0 lds\n"
      "s_add_u32 m0, s14, 5120\n"
      "s_add_u32 s80, s42, 6144\n"
      "buffer_load_dwordx4 v192, s[28:31], s80 offen offset:0 lds\n"
      "ds_read_b128 a[0:3], v194 offset:10240\n"
      "ds_read_b128 a[4:7], v195 offset:10240\n"
      "ds_read_b128 a[8:11], v196 offset:10240\n"
      "ds_read_b128 a[12:15], v197 offset:10240\n"
      "s_add_u32 m0, s14, 6144\n"
      "s_add_u32 s80, s42, 11264\n"
      "buffer_load_dwordx4 v192, s[28:31], s80 offen offset:0 lds\n"
      "s_add_u32 m0, s14, 7168\n"
      "s_add_u32 s80, s42, 16384\n"
      "buffer_load_dwordx4 v192, s[28:31], s80 offen offset:0 lds\n"
      "s_add_u32 m0, s14, 8192\n"
      "s_add_u32 s80, s42, 21504\n"
      "buffer_load_dwordx4 v192, s[28:31], s80 offen offset:0 lds\n"
      "s_add_u32 m0, s14, 9216\n"
      "s_add_u32 s80, s42, 26624\n"
      "buffer_load_dwordx4 v192, s[28:31], s80 offen offset:0 lds\n"
      "ds_read_b128 a[80:83], v194 offset:14336\n"
      "ds_read_b128 a[84:87], v195 offset:14336\n"
      "ds_read_b128 a[88:91], v196 offset:14336\n"
      "ds_read_b128 a[92:95], v197 offset:14336\n"
      "s_waitcnt lgkmcnt(7)\n"
      "v_mfma_f32_16x16x32_bf16 v[160:163], a[0:3], v[176:179], 0\n"
      "v_mfma_f32_16x16x32_bf16 v[164:167], a[0:3], a[0:3], 0\n"
      "s_waitcnt lgkmcnt(6)\n"
      "v_mfma_f32_16x16x32_bf16 v[160:163], a[4:7], v[176:179], v[160:163]\n"
      "v_mfma_f32_16x16x32_bf16 v[164:167], a[4:7], a[4:7], v[164:167]\n"
      "s_waitcnt lgkmcnt(5)\n"
      "v_mfma_f32_16x16x32_bf16 v[160:163], a[8:11], v[176:179], v[160:163]\n"
      "v_mfma_f32_16x16x32_bf16 v[164:167], a[8:11], a[8:11], v[164:167]\n"
      "s_waitcnt lgkmcnt(4)\n"
      "v_mfma_f32_16x16x32_bf16 v[160:163], a[12:15], v[176:179], v[160:163]\n"
      "v_mfma_f32_16x16x32_bf16 v[164:167], a[12:15], a[12:15], v[164:167]\n"
      "s_add_u32 m0, s14, 10240\n"
      "s_add_u32 s80, s42, 31744\n"
      "buffer_load_dwordx4 v192, s[28:31], s80 offen offset:0 lds\n"
      "s_add_u32 m0, s14, 11264\n"
      "s_add_u32 s80, s42, 36864\n"
      "buffer_load_dwordx4 v192, s[28:31], s80 offen offset:0 lds\n"
      "buffer_load_dwordx4 v[180:183], v190, s[28:31], s46 offen\n"
      "buffer_load_dwordx4 v[184:187], v190, s[28:31], s46 offen offset:64\n"
      "buffer_load_dwordx2 v[188:189], v191, s[28:31], s46 offen offset:128\n"
      "s_cmp_lg_u32 s40, 0\n"
      "s_cbranch_scc1 Lw0_%=\n"
      "s_mov_b64 exec, 0x3f\n"
      "buffer_load_dwordx4 v[244:247], v218, s[20:23], 0 offen sc1\n"
      "s_mov_b64 exec, -1\n"
      "Lw0_%=:\n"
      "ds_read_b128 a[16:19], v194 offset:18432\n"
      "ds_read_b128 a[20:23], v195 offset:18432\n"
      "ds_read_b128 a[24:27], v196 offset:18432\n"
      "ds_read_b128 a[28:31], v197 offset:18432\n"
      "s_waitcnt lgkmcnt(7)\n"
      "v_mfma_f32_16x16x32_bf16 v[168:171], a[80:83], v[176:179], 0\n"
      "v_mfma_f32_16x16x32_bf16 v[172:175], a[80:83], a[80:83], 0\n"
      "s_waitcnt lgkmcnt(6)\n"
      "v_mfma_f32_16x16x32_bf16 v[168:171], a[84:87], v[176:179], v[168:171]\n"
      "v_mfma_f32_16x16x32_bf16 v[172:175], a[84:87], a[84:87], v[172:175]\n"
      "s_waitcnt lgkmcnt(5)\n"
      "v_mfma_f32_16x16x32_bf16 v[168:171], a[88:91], v[176:179], v[168:171]\n"
      "v_mfma_f32_16x16x32_bf16 v[172:175], a[88:91], a[88:91], v[172:175]\n"
      "s_waitcnt lgkmcnt(4)\n"
      "v_mfma_f32_16x16x32_bf16 v[168:171], a[92:95], v[176:179], v[168:171]\n"
      "v_mfma_f32_16x16x32_bf16 v[172:175], a[92:95], a[92:95], v[172:175]\n"
      "s_waitcnt vmcnt(27)\n"
      "ds_read_b128 a[96:99], v194 offset:22528\n"
      "ds_read_b128 a[100:103], v195 offset:22528\n"
      "ds_read_b128 a[104:107], v196 offset:22528\n"
      "ds_read_b128 a[108:111], v197 offset:22528\n"
      "s_waitcnt lgkmcnt(7)\n"
      "v_mfma_f32_16x16x32_bf16 v[160:163], a[16:19], v[176:179], v[160:163]\n"
      "v_mfma_f32_16x16x32_bf16 v[164:167], a[16:19], a[16:19], v[164:167]\n"
      "s_waitcnt lgkmcnt(6)\n"
      "v_mfma_f32_16x16x32_bf16 v[160:163], a[20:23], v[176:179], v[160:163]\n"
      "v_mfma_f32_16x16x32_bf16 v[164:167], a[20:23], a[20:23], v[164:167]\n"
      "s_waitcnt lgkmcnt(5)\n"
      "v_mfma_f32_16x16x32_bf16 v[160:163], a[24:27], v[176:179], v[160:163]\n"
      "v_mfma_f32_16x16x32_bf16 v[164:167], a[24:27], a[24:27], v[164:167]\n"
      "s_waitcnt lgkmcnt(4)\n"
      "v_mfma_f32_16x16x32_bf16 v[160:163], a[28:31], v[176:179], v[160:163]\n"
      "v_mfma_f32_16x16x32_bf16 v[164:167], a[28:31], a[28:31], v[164:167]\n"
      "s_waitcnt vmcnt(23)\n"
      "ds_read_b128 a[32:35], v194 offset:26624\n"
      "ds_read_b128 a[36:39], v195 offset:26624\n"
      "ds_read_b128 a[40:43], v196 offset:26624\n"
      "ds_read_b128 a[44:47], v197 offset:26624\n"
      "s_waitcnt lgkmcnt(7)\n"
      "v_mfma_f32_16x16x32_bf16 v[168:171], a[96:99], v[176:179], v[168:171]\n"
      "v_mfma_f32_16x16x32_bf16 v[172:175], a[96:99], a[96:99], v[172:175]\n"
      "s_waitcnt lgkmcnt(6)\n"
      "v_mfma_f32_16x16x32_bf16 v[168:171], a[100:103], v[176:179], v[168:171]\n"
      "v_mfma_f32_16x16x32_bf16 v[172:175], a[100:103], a[100:103], v[172:175]\n"
      "s_waitcnt lgkmcnt(5)\n"
      "v_mfma_f32_16x16x32_bf16 v[168:171], a[104:107], v[176:179], v[168:171]\n"
      "v_mfma_f32_16x16x32_bf16 v[172:175], a[104:107], a[104:107], v[172:175]\n"
      "s_waitcnt lgkmcnt(4)\n"
      "v_mfma_f32_16x16x32_bf16 v[168:171], a[108:111], v[176:179], v[168:171]\n"
      "v_mfma_f32_16x16x32_bf16 v[172:175], a[108:111], a[108:111], v[172:175]\n"
      "s_waitcnt vmcnt(19)\n"
      "ds_read_b128 a[112:115], v194 offset:30720\n"
      "ds_read_b128 a[116:119], v195 offset:30720\n"
      "ds_read_b128 a[120:123], v196 offset:30720\n"
      "ds_read_b128 a[124:127], v197 offset:30720\n"
      "s_waitcnt lgkmcnt(7)\n"
      "v_mfma_f32_16x16x32_bf16 v[160:163], a[32:35], v[176:179], v[160:163]\n"
      "v_mfma_f32_16x16x32_bf16 v[164:167], a[32:35], a[32:35], v[164:167]\n"
      "s_waitcnt lgkmcnt(6)\n"
      "v_mfma_f32_16x16x32_bf16 v[160:163], a[36:39], v[176:179], v[160:163]\n"
      "v_mfma_f32_16x16x32_bf16 v[164:167], a[36:39], a[36:39], v[164:167]\n"
      "s_waitcnt lgkmcnt(5)\n"
      "v_mfma_f32_16x16x32_bf16 v[160:163], a[40:43], v[176:179], v[160:163]\n"
      "v_mfma_f32_16x16x32_bf16 v[164:167], a[40:43], a[40:43], v[164:167]\n"
      "s_waitcnt lgkmcnt(4)\n"
      "v_mfma_f32_16x16x32_bf16 v[160:163], a[44:47], v[176:179], v[160:163]\n"
      "v_mfma_f32_16x16x32_bf16 v[164:167], a[44:47], a[44:47], v[164:167]\n"
      "s_waitcnt vmcnt(15)\n"
      "ds_read_b128 a[48:51], v194 offset:34816\n"
      "ds_read_b128 a[52:55], v195 offset:34816\n"
      "ds_read_b128 a[56:59], v196 offset:34816\n"
      "ds_read_b128 a[60:63], v197 offset:34816\n"
      "s_waitcnt lgkmcnt(7)\n"
      "v_mfma_f32_16x16x32_bf16 v[168:171], a[112:115], v[176:179], v[168:171]\n"
      "v_mfma_f32_16x16x32_bf16 v[172:175], a[112:115], a[112:115], v[172:175]\n"
      "s_waitcnt lgkmcnt(6)\n"
      "v_mfma_f32_16x16x32_bf16 v[168:171], a[116:119], v[176:179], v[168:171]\n"
      "v_mfma_f32_16x16x32_bf16 v[172:175], a[116:119], a[116:119], v[172:175]\n"
      "s_waitcnt lgkmcnt(5)\n"
      "v_mfma_f32_16x16x32_bf16 v[168:171], a[120:123], v[176:179], v[168:171]\n"
      "v_mfma_f32_16x16x32_bf16 v[172:175], a[120:123], a[120:123], v[172:175]\n"
      "s_waitcnt lgkmcnt(4)\n"
      "v_mfma_f32_16x16x32_bf16 v[168:171], a[124:127], v[176:179], v[168:171]\n"
      "v_mfma_f32_16x16x32_bf16 v[172:175], a[124:127], a[124:127], v[172:175]\n"
      "s_waitcnt vmcnt(11)\n"
      "ds_read_b128 a[128:131], v194 offset:0\n"
      "ds_read_b128 a[132:135], v195 offset:0\n"
      "ds_read_b128 a[136:139], v196 offset:0\n"
      "ds_read_b128 a[140:143], v197 offset:0\n"
      "s_waitcnt lgkmcnt(7)\n"
      "v_mfma_f32_16x16x32_bf16 v[160:163], a[48:51], v[176:179], v[160:163]\n"
      "v_mfma_f32_16x16x32_bf16 v[164:167], a[48:51], a[48:51], v[164:167]\n"
      "s_waitcnt lgkmcnt(6)\n"
      "v_mfma_f32_16x16x32_bf16 v[160:163], a[52:55], v[176:179], v[160:163]\n"
      "v_mfma_f32_16x16x32_bf16 v[164:167], a[52:55], a[52:55], v[164:167]\n"
      "s_waitcnt lgkmcnt(5)\n"
      "v_mfma_f32_16x16x32_bf16 v[160:163], a[56:59], v[176:179], v[160:163]\n"
      "v_mfma_f32_16x16x32_bf16 v[164:167], a[56:59], a[56:59], v[164:167]\n"
      "s_waitcnt lgkmcnt(4)\n"
      "v_mfma_f32_16x16x32_bf16 v[160:163], a[60:63], v[176:179], v[160:163]\n"
      "v_mfma_f32_16x16x32_bf16 v[164:167], a[60:63], a[60:63], v[164:167]\n"
      "s_waitcnt vmcnt(7)\n"
      "ds_read_b128 a[64:67], v194 offset:4096\n"
      "ds_read_b128 a[68:71], v195 offset:4096\n"
      "ds_read_b128 a[72:75], v196 offset:4096\n"
      "ds_read_b128 a[76:79], v197 offset:4096\n"
      "s_waitcnt lgkmcnt(7)\n"
      "v_mfma_f32_16x16x32_bf16 v[168:171], a[128:131], v[176:179], v[168:171]\n"
      "v_mfma_f32_16x16x32_bf16 v[172:175], a[128:131], a[128:131], v[172:175]\n"
      "s_waitcnt lgkmcnt(6)\n"
      "v_mfma_f32_16x16x32_bf16 v[168:171], a[132:135], v[176:179], v[168:171]\n"
      "v_mfma_f32_16x16x32_bf16 v[172:175], a[132:135], a[132:135], v[172:175]\n"
      "s_waitcnt lgkmcnt(5)\n"
      "v_mfma_f32_16x16x32_bf16 v[168:171], a[136:139], v[176:179], v[168:171]\n"
      "v_mfma_f32_16x16x32_bf16 v[172:175], a[136:139], a[136:139], v[172:175]\n"
      "s_waitcnt lgkmcnt(4)\n"
      "v_mfma_f32_16x16x32_bf16 v[168:171], a[140:143], v[176:179], v[168:171]\n"
      "v_mfma_f32_16x16x32_bf16 v[172:175], a[140:143], a[140:143], v[172:175]\n"
      "s_waitcnt vmcnt(3)\n"
      "ds_read_b128 a[144:147], v194 offset:8192\n"
      "ds_read_b128 a[148:151], v195 offset:8192\n"
      "ds_read_b128 a[152:155], v196 offset:8192\n"
      "ds_read_b128 a[156:159], v197 offset:8192\n"
      "s_waitcnt lgkmcnt(0)\n"
      "s_cmp_lg_u32 s40, 0\n"
      "s_cbranch_scc1 Lz0_%=\n"
      "s_mov_b32 s84, 400000\n"
      "s_mov_b64 exec, 0x3f\n"
      "s_waitcnt vmcnt(0)\n"
      "Ly0_%=:\n"
      "v_sub_u32 v246, v246, v244\n"
      "v_sub_u32 v247, v247, v245\n"
      "v_xor_b32 v246, 0x7f4a7c15, v246\n"
      "v_xor_b32 v247, 0x9e3779b9, v247\n"
      "v_or_b32 v246, v246, v247\n"
      "v_cmp_ne_u32 vcc, 0, v246\n"
      "s_cbranch_vccz Lx0_%=\n"
      "buffer_load_dwordx4 v[244:247], v218, s[20:23], 0 offen sc1\n"
      "s_waitcnt vmcnt(0)\n"
      "s_sub_u32 s84, s84, 1\n"
      "s_cmp_lg_u32 s84, 0\n"
      "s_cbranch_scc1 Ly0_%=\n"
      "Lx0_%=:\n"
      "s_mov_b64 exec, -1\n"
      "Lz0_%=:\n"
      "s_barrier\n"
      "s_add_u32 s79, s65, 0\n"
      "s_add_u32 m0, s72, 0\n"
      "s_nop 0\n"
      "buffer_load_dwordx4 v0, s[20:23], s79 offen offset:0 sc1 lds\n"
      "buffer_load_dwordx4 v0, s[20:23], s79 offen offset:1024 sc1 lds\n"
      "buffer_load_dwordx4 v0, s[20:23], s79 offen offset:2048 sc1 lds\n"
      "buffer_load_dwordx4 v0, s[20:23], s79 offen offset:3072 sc1 lds\n"
      "s_add_u32 s79, s65, 4096\n"
      "s_add_u32 m0, s72, 4096\n"
      "s_nop 0\n"
      "buffer_load_dwordx4 v0, s[20:23], s79 offen offset:0 sc1 lds\n"
      "s_add_u32 s79, s65, 20480\n"
      "s_add_u32 m0, s72, 20480\n"
      "s_nop 0\n"
      "buffer_load_dwordx4 v0, s[20:23], s79 offen offset:0 sc1 lds\n"
      "buffer_load_dwordx4 v0, s[20:23], s79 offen offset:1024 sc1 lds\n"
      "buffer_load_dwordx4 v0, s[20:23], s79 offen offset:2048 sc1 lds\n"
      "buffer_load_dwordx4 v0, s[20:23], s79 offen offset:3072 sc1 lds\n"
      "s_add_u32 s79, s65, 24576\n"
      "s_add_u32 m0, s72, 24576\n"
      "s_nop 0\n"
      "buffer_load_dwordx4 v0, s[20:23], s79 offen offset:0 sc1 lds\n"
      "v_mfma_f32_16x16x32_bf16 v[160:163], a[64:67], v[176:179], v[160:163]\n"
      "v_mfma_f32_16x16x32_bf16 v[164:167], a[64:67], a[64:67], v[164:167]\n"
      "v_mfma_f32_16x16x32_bf16 v[160:163], a[68:71], v[176:179], v[160:163]\n"
      "v_mfma_f32_16x16x32_bf16 v[164:167], a[68:71], a[68:71], v[164:167]\n"
      "v_mfma_f32_16x16x32_bf16 v[160:163], a[72:75], v[176:179], v[160:163]\n"
      "v_mfma_f32_16x16x32_bf16 v[164:167], a[72:75], a[72:75], v[164:167]\n"
      "v_mfma_f32_16x16x32_bf16 v[160:163], a[76:79], v[176:179], v[160:163]\n"
      "v_mfma_f32_16x16x32_bf16 v[164:167], a[76:79], a[76:79], v[164:167]\n"
      "v_mfma_f32_16x16x32_bf16 v[168:171], a[144:147], v[176:179], v[168:171]\n"
      "v_mfma_f32_16x16x32_bf16 v[172:175], a[144:147], a[144:147], v[172:175]\n"
      "v_mfma_f32_16x16x32_bf16 v[168:171], a[148:151], v[176:179], v[168:171]\n"
      "v_mfma_f32_16x16x32_bf16 v[172:175], a[148:151], a[148:151], v[172:175]\n"
      "v_mfma_f32_16x16x32_bf16 v[168:171], a[152:155], v[176:179], v[168:171]\n"
      "v_mfma_f32_16x16x32_bf16 v[172:175], a[152:155], a[152:155], v[172:175]\n"
      "v_mfma_f32_16x16x32_bf16 v[168:171], a[156:159], v[176:179], v[168:171]\n"
      "v_mfma_f32_16x16x32_bf16 v[172:175], a[156:159], a[156:159], v[172:175]\n"
      "s_waitcnt vmcnt(12)\n"
      "v_lshlrev_b32 v12, 16, v180\n"
      "v_and_b32 v13, 0xffff0000, v180\n"
      "v_lshlrev_b32 v14, 16, v181\n"
      "v_and_b32 v15, 0xffff0000, v181\n"
      "v_lshlrev_b32 v16, 16, v182\n"
      "v_and_b32 v17, 0xffff0000, v182\n"
      "v_lshlrev_b32 v18, 16, v183\n"
      "v_and_b32 v19, 0xffff0000, v183\n"
      "s_waitcnt vmcnt(11)\n"
      "v_lshlrev_b32 v20, 16, v184\n"
      "v_and_b32 v21, 0xffff0000, v184\n"
      "v_lshlrev_b32 v22, 16, v185\n"
      "v_and_b32 v23, 0xffff0000, v185\n"
      "v_lshlrev_b32 v24, 16, v186\n"
      "v_and_b32 v25, 0xffff0000, v186\n"
      "v_lshlrev_b32 v26, 16, v187\n"
      "v_and_b32 v27, 0xffff0000, v187\n"
      "s_waitcnt vmcnt(10)\n"
      "v_lshlrev_b32 v28, 16, v188\n"
      "v_and_b32 v29, 0xffff0000, v188\n"
      "v_lshlrev_b32 v30, 16, v189\n"
      "v_and_b32 v31, 0xffff0000, v189\n"
      "s_mov_b32 s90, 0x22222222\n"
      "s_mov_b32 s91, 0x22222222\n"
      "s_mov_b32 s92, 0x44444444\n"
      "s_mov_b32 s93, 0x44444444\n"
      "s_mov_b32 s94, 0x88888888\n"
      "s_mov_b32 s95, 0x88888888\n"
      "s_lshl_b32 s76, s40, 7\n"
      "s_add_u32 s76, s76, 159744\n"
      "v_lshrrev_b32 v38, 2, v0\n"
      "v_and_b32 v38, 0x3c, v38\n"
      "v_add_u32 v38, s76, v38\n"
      "v_lshrrev_b32 v39, 4, v0\n"
      "v_and_b32 v39, 0x30, v39\n"
      "v_add_u32 v39, s76, v39\n"
      "v_cndmask_b32 v160, v160, v161, s[90:91]\n"
      "v_cndmask_b32 v164, v164, v165, s[90:91]\n"
      "v_cndmask_b32 v168, v168, v169, s[90:91]\n"
      "v_cndmask_b32 v172, v172, v173, s[90:91]\n"
      "v_cndmask_b32 v160, v160, v162, s[92:93]\n"
      "v_cndmask_b32 v164, v164, v166, s[92:93]\n"
      "v_cndmask_b32 v168, v168, v170, s[92:93]\n"
      "v_cndmask_b32 v172, v172, v174, s[92:93]\n"
      "v_cndmask_b32 v160, v160, v163, s[94:95]\n"
      "v_cndmask_b32 v164, v164, v167, s[94:95]\n"
      "v_cndmask_b32 v168, v168, v171, s[94:95]\n"
      "v_cndmask_b32 v172, v172, v175, s[94:95]\n"
      "v_mul_f32 v160, 0x3acccccd, v160\n"
      "v_mul_f32 v164, 0x3acccccd, v164\n"
      "v_mul_f32 v168, 0x3acccccd, v168\n"
      "v_mul_f32 v172, 0x3acccccd, v172\n"
      "v_fma_f32 v164, -v160, v160, v164\n"
      "v_fma_f32 v172, -v168, v168, v172\n"
      "v_max_f32 v164, 0, v164\n"
      "v_max_f32 v172, 0, v172\n"
      "v_add_f32 v164, 0x3727c5ac, v164\n"
      "v_add_f32 v172, 0x3727c5ac, v172\n"
      "v_rsq_f32 v164, v164\n"
      "v_rsq_f32 v172, v172\n"
      "v_mul_f32 v164, 0x3fb8aa3b, v164\n"
      "v_mul_f32 v172, 0x3fb8aa3b, v172\n"
      "s_mov_b32 exec_lo, 0x00f0000f\n"
      "s_mov_b32 exec_hi, 0xf0000f00\n"
      "ds_write_b32 v38, v164\n"
      "ds_write_b32 v38, v172 offset:64\n"
      "s_mov_b64 exec, -1\n"
      "s_waitcnt vmcnt(5)\n"
      "s_waitcnt lgkmcnt(0)\n"
      "s_branch LJk_%=\n"
      "LJg_%=:\n"
      "s_mov_b32 s40, %[w]\n"
      "s_and_b32 s75, %[b], 7\n"
      "s_lshr_b32 s76, %[b], 3\n"
      "s_lshr_b32 s77, s75, 1\n"
      "s_lshl_b32 s77, s77, 2\n"
      "s_lshr_b32 s78, s76, 3\n"
      "s_add_u32 s90, s77, s78\n"
      "s_and_b32 s77, s75, 1\n"
      "s_lshl_b32 s77, s77, 3\n"
      "s_and_b32 s78, s76, 7\n"
      "s_add_u32 s91, s77, s78\n"
      "v_mbcnt_lo_u32_b32 v11, -1, 0\n"
      "v_mbcnt_hi_u32_b32 v11, -1, v11\n"
      "v_lshlrev_b32 v0, 4, v11\n"
      "v_and_b32 v40, 15, v11\n"
      "v_mul_u32_u24 v36, 1280, v40\n"
      "v_and_b32 v37, 0x30, v11\n"
      "v_add_u32 v190, v36, v37\n"
      "v_lshrrev_b32 v46, 4, v11\n"
      "v_lshlrev_b32 v47, 2, v46\n"
      "v_xor_b32 v47, v47, v40\n"
      "v_mul_u32_u24 v45, 1280, v46\n"
      "v_lshl_add_u32 v192, v47, 4, v45\n"
      "s_lshl_b32 s77, s90, 3\n"
      "s_lshl_b32 s78, s40, 1\n"
      "s_add_u32 s93, s77, s78\n"
      "s_mul_i32 s42, s93, 20480\n"
      "s_cmp_lg_u32 s84, 1\n"
      "s_cbranch_scc1 LJo_%=\n"
      "s_lshr_b32 s75, %[b], 3\n"
      "s_lshl_b32 s75, s75, 2\n"
      "s_add_u32 s75, s75, s40\n"
      "v_lshlrev_b32 v248, 7, v11\n"
      "v_add_u32 v248, s75, v248\n"
      "v_lshlrev_b32 v249, 16, v248\n"
      "buffer_load_dword v250, v249, s[20:23], 0 offen sc1\n"
      "v_lshlrev_b32 v249, 16, v248\n"
      "s_mov_b64 s[96:97], %[o]\n"
      "s_mov_b32 s98, 2621440\n"
      "s_mov_b32 s99, 0x20000\n"
      "buffer_load_dword v252, v249, s[96:99], 0 offen sc1\n"
      "v_lshl_add_u32 v45, v40, 6, v37\n"
      "v_add_u32 v193, s14, v45\n"
      "v_and_b32 v45, 0xff, v45\n"
      "v_lshl_add_u32 v45, v40, 8, v45\n"
      "v_add_u32 v194, s14, v45\n"
      "v_xor_b32 v195, 64, v194\n"
      "v_xor_b32 v196, 128, v194\n"
      "v_xor_b32 v197, 192, v194\n"
      "s_add_u32 s24, s20, 10223616\n"
      "s_addc_u32 s25, s21, 0\n"
      "s_mov_b32 s26, 32899072\n"
      "s_mov_b32 s27, 0x20000\n"
      "s_mov_b32 s41, 0\n"
      "s_mul_i32 s71, s40, 13312\n"
      "s_mul_i32 s72, s40, 5120\n"
      "s_mul_i32 s77, s91, 153600\n"
      "s_add_u32 s65, s77, s72\n"
      "s_add_u32 s66, s77, s71\n"
      "s_and_b32 s92, s91, 7\n"
      "s_lshl_b32 s77, s90, 3\n"
      "s_lshr_b32 s78, s91, 3\n"
      "s_lshl_b32 s78, s78, 2\n"
      "s_add_u32 s75, s77, s78\n"
      "s_add_u32 s94, s75, s40\n"
      "s_lshl_b32 s43, s93, 10\n"
      "s_mul_i32 s44, s94, 20480\n"
      "s_mul_i32 s77, s92, 160\n"
      "s_add_u32 s46, s44, s77\n"
      "s_mul_i32 s77, s92, 2560\n"
      "s_add_u32 s44, s44, s77\n"
      "s_lshl_b32 s45, s94, 10\n"
      "s_lshl_b32 s77, s92, 7\n"
      "s_add_u32 s45, s45, s77\n"
      "s_mul_i32 s58, s40, 15360\n"
      "s_mul_i32 s76, s40, 14336\n"
      "s_add_u32 s77, s40, 1\n"
      "s_and_b32 s77, s77, 3\n"
      "s_mul_i32 s55, s77, 15360\n"
      "s_add_u32 s55, s55, 0\n"
      "s_add_u32 s77, s77, s75\n"
      "s_mul_i32 s47, s77, 20480\n"
      "s_add_u32 s47, s47, s72\n"
      "s_mul_i32 s51, s77, 57344\n"
      "s_add_u32 s51, s51, s76\n"
      "s_add_u32 s77, s40, 2\n"
      "s_and_b32 s77, s77, 3\n"
      "s_mul_i32 s56, s77, 15360\n"
      "s_add_u32 s56, s56, 5120\n"
      "s_add_u32 s77, s77, s75\n"
      "s_mul_i32 s48, s77, 20480\n"
      "s_add_u32 s48, s48, s72\n"
      "s_mul_i32 s52, s77, 57344\n"
      "s_add_u32 s52, s52, s76\n"
      "s_add_u32 s77, s40, 3\n"
      "s_and_b32 s77, s77, 3\n"
      "s_mul_i32 s57, s77, 15360\n"
      "s_add_u32 s57, s57, 10240\n"
      "s_add_u32 s77, s77, s75\n"
      "s_mul_i32 s49, s77, 20480\n"
      "s_add_u32 s49, s49, s72\n"
      "s_mul_i32 s53, s77, 57344\n"
      "s_add_u32 s53, s53, s76\n"
      "s_add_u32 s77, s40, 4\n"
      "s_and_b32 s77, s77, 3\n"
      "s_add_u32 s77, s77, s75\n"
      "s_mul_i32 s50, s77, 20480\n"
      "s_add_u32 s50, s50, s72\n"
      "s_mul_i32 s54, s77, 57344\n"
      "s_add_u32 s54, s54, s76\n"
      "s_lshr_b32 s75, s91, 2\n"
      "s_and_b32 s76, s91, 3\n"
      "s_mul_i32 s77, s75, 5120\n"
      "s_add_u32 s77, s77, s42\n"
      "s_lshl_b32 s78, s76, 10\n"
      "s_add_u32 s59, s77, s78\n"
      "s_lshl_b32 s78, s76, 8\n"
      "s_add_u32 s60, s77, s78\n"
      "s_mul_i32 s77, s93, 57344\n"
      "s_mul_i32 s78, s75, 14336\n"
      "s_add_u32 s77, s77, s78\n"
      "s_lshl_b32 s78, s76, 10\n"
      "s_add_u32 s61, s77, s78\n"
      "s_lshl_b32 s78, s76, 9\n"
      "s_add_u32 s62, s77, s78\n"
      "s_lshl_b32 s77, s91, 15\n"
      "s_lshl_b32 s78, s40, 13\n"
      "s_add_u32 s64, s77, s78\n"
      "s_lshl_b32 s73, s90, 9\n"
      "s_lshl_b32 s74, s91, 9\n"
      "s_add_u32 s63, s64, s73\n"
      "s_mul_i32 s67, s91, 286720\n"
      "s_add_u32 s67, s67, s72\n"
      "s_lshl_b32 s77, s92, 2\n"
      "s_add_u32 s77, s77, s40\n"
      "s_mul_i32 s68, s77, 25600\n"
      "s_mul_i32 s69, s77, 71680\n"
      "s_mov_b32 s86, 0x35c1a2a0\n"
      "s_mov_b32 s87, 0x35c1a2a0\n"
      "s_mov_b32 s88, 0x3f317218\n"
      "s_mov_b32 s89, 0x3f317218\n"
      "v_bfe_u32 v41, v11, 4, 1\n"
      "v_lshrrev_b32 v42, 5, v11\n"
      "v_lshlrev_b32 v43, 4, v40\n"
      "v_lshl_add_u32 v44, v42, 8, v43\n"
      "v_lshl_add_u32 v36, v41, 4, v36\n"
      "v_lshl_add_u32 v191, v42, 3, v36\n"
      "v_mov_b32 v45, 20480\n"
      "v_mad_u32_u24 v1, v41, v45, v44\n"
      "v_mov_b32 v45, 57344\n"
      "v_mad_u32_u24 v2, v41, v45, v44\n"
      "v_lshl_add_u32 v5, v41, 3, v44\n"
      "v_lshlrev_b32 v45, 2, v40\n"
      "v_lshl_add_u32 v34, v42, 6, v45\n"
      "v_lshl_add_u32 v3, v41, 10, v34\n"
      "v_lshl_add_u32 v45, v41, 8, v43\n"
      "v_lshl_add_u32 v4, v42, 3, v45\n"
      "v_and_b32 v45, 0x70, v0\n"
      "v_lshrrev_b32 v46, 4, v11\n"
      "v_lshl_add_u32 v45, v46, 7, v45\n"
      "v_add_u32 v6, 143360, v45\n"
      "v_add_u32 v7, 0x10000, v0\n"
      "v_add_u32 v8, 0x20000, v0\n"
      "v_mov_b32 v9, 0\n"
      "v_mov_b32 v10, 1\n"
      "v_mov_b32 v32, v0\n"
      "v_mov_b32 v33, v4\n"
      "s_add_u32 s81, s44, 16449536\n"
      "s_add_u32 s82, s44, 16450560\n"
      "s_add_u32 s83, s44, 16451584\n"
      "v_mov_b32 v176, 0x3f803f80\n"
      "v_mov_b32 v177, 0x3f803f80\n"
      "v_mov_b32 v178, 0x3f803f80\n"
      "v_mov_b32 v179, 0x3f803f80\n"
      "v_mov_b32 v240, 0\n"
      "v_mov_b32 v241, 0\n"
      "v_mov_b32 v242, 0\n"
      "v_mov_b32 v243, 0\n"
      "buffer_store_dwordx4 v[240:243], v219, s[20:23], 0 offen sc1\n"
      "s_add_u32 m0, s14, 12288\n"
      "s_add_u32 s80, s42, 10240\n"
      "buffer_load_dwordx4 v192, s[28:31], s80 offen offset:0 lds\n"
      "s_add_u32 m0, s14, 13312\n"
      "s_add_u32 s80, s42, 15360\n"
      "buffer_load_dwordx4 v192, s[28:31], s80 offen offset:0 lds\n"
      "s_add_u32 m0, s14, 14336\n"
      "s_add_u32 s80, s42, 20480\n"
      "buffer_load_dwordx4 v192, s[28:31], s80 offen offset:0 lds\n"
      "s_add_u32 m0, s14, 15360\n"
      "s_add_u32 s80, s42, 25600\n"
      "buffer_load_dwordx4 v192, s[28:31], s80 offen offset:0 lds\n"
      "s_add_u32 m0, s14, 16384\n"
      "s_add_u32 s80, s42, 30720\n"
      "buffer_load_dwordx4 v192, s[28:31], s80 offen offset:0 lds\n"
      "s_add_u32 m0, s14, 17408\n"
      "s_add_u32 s80, s42, 35840\n"
      "buffer_load_dwordx4 v192, s[28:31], s80 offen offset:0 lds\n"
      "s_add_u32 m0, s14, 18432\n"
      "s_add_u32 s80, s42, 256\n"
      "buffer_load_dwordx4 v192, s[28:31], s80 offen offset:0 lds\n"
      "s_add_u32 m0, s14, 19456\n"
      "s_add_u32 s80, s42, 5376\n"
      "buffer_load_dwordx4 v192, s[28:31], s80 offen offset:0 lds\n"
      "s_add_u32 m0, s14, 20480\n"
      "s_add_u32 s80, s42, 10496\n"
      "buffer_load_dwordx4 v192, s[28:31], s80 offen offset:0 lds\n"
      "s_add_u32 m0, s14, 21504\n"
      "s_add_u32 s80, s42, 15616\n"
      "buffer_load_dwordx4 v192, s[28:31], s80 offen offset:0 lds\n"
      "s_add_u32 m0, s14, 22528\n"
      "s_add_u32 s80, s42, 20736\n"
      "buffer_load_dwordx4 v192, s[28:31], s80 offen offset:0 lds\n"
      "s_add_u32 m0, s14, 23552\n"
      "s_add_u32 s80, s42, 25856\n"
      "buffer_load_dwordx4 v192, s[28:31], s80 offen offset:0 lds\n"
      "s_add_u32 m0, s14, 24576\n"
      "s_add_u32 s80, s42, 30976\n"
      "buffer_load_dwordx4 v192, s[28:31], s80 offen offset:0 lds\n"
      "s_add_u32 m0, s14, 25600\n"
      "s_add_u32 s80, s42, 36096\n"
      "buffer_load_dwordx4 v192, s[28:31], s80 offen offset:0 lds\n"
      "s_add_u32 m0, s14, 26624\n"
      "s_add_u32 s80, s42, 512\n"
      "buffer_load_dwordx4 v192, s[28:31], s80 offen offset:0 lds\n"
      "s_add_u32 m0, s14, 27648\n"
      "s_add_u32 s80, s42, 5632\n"
      "buffer_load_dwordx4 v192, s[28:31], s80 offen offset:0 lds\n"
      "s_add_u32 m0, s14, 28672\n"
      "s_add_u32 s80, s42, 10752\n"
      "buffer_load_dwordx4 v192, s[28:31], s80 offen offset:0 lds\n"
      "s_add_u32 m0, s14, 29696\n"
      "s_add_u32 s80, s42, 15872\n"
      "buffer_load_dwordx4 v192, s[28:31], s80 offen offset:0 lds\n"
      "s_add_u32 m0, s14, 30720\n"
      "s_add_u32 s80, s42, 20992\n"
      "buffer_load_dwordx4 v192, s[28:31], s80 offen offset:0 lds\n"
      "s_add_u32 m0, s14, 31744\n"
      "s_add_u32 s80, s42, 26112\n"
      "buffer_load_dwordx4 v192, s[28:31], s80 offen offset:0 lds\n"
      "s_add_u32 m0, s14, 32768\n"
      "s_add_u32 s80, s42, 31232\n"
      "buffer_load_dwordx4 v192, s[28:31], s80 offen offset:0 lds\n"
      "s_add_u32 m0, s14, 33792\n"
      "s_add_u32 s80, s42, 36352\n"
      "buffer_load_dwordx4 v192, s[28:31], s80 offen offset:0 lds\n"
      "s_add_u32 m0, s14, 34816\n"
      "s_add_u32 s80, s42, 768\n"
      "buffer_load_dwordx4 v192, s[28:31], s80 offen offset:0 lds\n"
      "s_add_u32 m0, s14, 35840\n"
      "s_add_u32 s80, s42, 5888\n"
      "buffer_load_dwordx4 v192, s[28:31], s80 offen offset:0 lds\n"
      "s_add_u32 m0, s14, 36864\n"
      "s_add_u32 s80, s42, 11008\n"
      "buffer_load_dwordx4 v192, s[28:31], s80 offen offset:0 lds\n"
      "s_add_u32 m0, s14, 37888\n"
      "s_add_u32 s80, s42, 16128\n"
      "buffer_load_dwordx4 v192, s[28:31], s80 offen offset:0 lds\n"
      "s_waitcnt vmcnt(40)\n"
      "ds_read_b128 v[48:51], v193 offset:0\n"
      "s_waitcnt vmcnt(39)\n"
      "ds_read_b128 v[52:55], v193 offset:1024\n"
      "s_waitcnt vmcnt(38)\n"
      "ds_read_b128 v[56:59], v193 offset:2048\n"
      "s_waitcnt vmcnt(37)\n"
      "ds_read_b128 v[60:63], v193 offset:3072\n"
      "s_waitcnt vmcnt(36)\n"
      "ds_read_b128 v[64:67], v193 offset:4096\n"
      "s_waitcnt vmcnt(35)\n"
      "ds_read_b128 v[68:71], v193 offset:5120\n"
      "s_waitcnt vmcnt(34)\n"
      "ds_read_b128 v[72:75], v193 offset:6144\n"
      "s_waitcnt vmcnt(33)\n"
      "ds_read_b128 v[76:79], v193 offset:7168\n"
      "s_waitcnt vmcnt(32)\n"
      "ds_read_b128 v[80:83], v193 offset:8192\n"
      "s_waitcnt vmcnt(31)\n"
      "ds_read_b128 v[84:87], v193 offset:9216\n"
      "s_waitcnt lgkmcnt(9)\n"
      "v_mfma_f32_16x16x32_bf16 v[88:91], v[48:51], v[200:203], 0\n"
      "v_mfma_f32_16x16x32_bf16 v[92:95], v[48:51], v[204:207], 0\n"
      "s_waitcnt lgkmcnt(8)\n"
      "v_mfma_f32_16x16x32_bf16 v[88:91], v[52:55], v[200:203], v[88:91]\n"
      "v_mfma_f32_16x16x32_bf16 v[92:95], v[52:55], v[204:207], v[92:95]\n"
      "s_waitcnt lgkmcnt(7)\n"
      "v_mfma_f32_16x16x32_bf16 v[88:91], v[56:59], v[200:203], v[88:91]\n"
      "v_mfma_f32_16x16x32_bf16 v[92:95], v[56:59], v[204:207], v[92:95]\n"
      "s_waitcnt lgkmcnt(6)\n"
      "v_mfma_f32_16x16x32_bf16 v[88:91], v[60:63], v[200:203], v[88:91]\n"
      "v_mfma_f32_16x16x32_bf16 v[92:95], v[60:63], v[204:207], v[92:95]\n"
      "s_waitcnt lgkmcnt(5)\n"
      "v_mfma_f32_16x16x32_bf16 v[88:91], v[64:67], v[200:203], v[88:91]\n"
      "v_mfma_f32_16x16x32_bf16 v[92:95], v[64:67], v[204:207], v[92:95]\n"
      "s_waitcnt lgkmcnt(4)\n"
      "v_mfma_f32_16x16x32_bf16 v[88:91], v[68:71], v[200:203], v[88:91]\n"
      "v_mfma_f32_16x16x32_bf16 v[92:95], v[68:71], v[204:207], v[92:95]\n"
      "s_waitcnt lgkmcnt(3)\n"
      "v_mfma_f32_16x16x32_bf16 v[88:91], v[72:75], v[200:203], v[88:91]\n"
      "v_mfma_f32_16x16x32_bf16 v[92:95], v[72:75], v[204:207], v[92:95]\n"
      "s_waitcnt lgkmcnt(2)\n"
      "v_mfma_f32_16x16x32_bf16 v[88:91], v[76:79], v[200:203], v[88:91]\n"
      "v_mfma_f32_16x16x32_bf16 v[92:95], v[76:79], v[204:207], v[92:95]\n"
      "s_waitcnt lgkmcnt(1)\n"
      "v_mfma_f32_16x16x32_bf16 v[88:91], v[80:83], v[200:203], v[88:91]\n"
      "v_mfma_f32_16x16x32_bf16 v[92:95], v[80:83], v[204:207], v[92:95]\n"
      "s_waitcnt lgkmcnt(0)\n"
      "v_mfma_f32_16x16x32_bf16 v[88:91], v[84:87], v[200:203], v[88:91]\n"
      "v_mfma_f32_16x16x32_bf16 v[92:95], v[84:87], v[204:207], v[92:95]\n"
      "s_lshl_b32 s77, s40, 2\n"
      "s_add_u32 s77, s77, 160768\n"
      "v_add_u32 v45, s77, v44\n"
      "v_add_u32 v46, 160768, v43\n"
      "s_nop 2\n"
      "v_add_f32 v88, v88, v89\n"
      "v_add_f32 v90, v90, v91\n"
      "v_add_f32 v92, v92, v93\n"
      "v_add_f32 v94, v94, v95\n"
      "v_add_f32 v88, v88, v90\n"
      "v_add_f32 v92, v92, v94\n"
      "s_nop 1\n"
      "v_permlane32_swap_b32 v88, v92\n"
      "v_add_f32 v88, v88, v92\n"
      "v_mov_b32 v92, v88\n"
      "s_nop 1\n"
      "v_permlane16_swap_b32 v88, v92\n"
      "v_add_f32 v88, v88, v92\n"
      "ds_write_b32 v45, v88\n"
      "s_waitcnt lgkmcnt(0)\n"
      "s_barrier\n"
      "ds_read_b128 v[88:91], v46\n"
      "ds_read_b128 v[92:95], v46 offset:256\n"
      "s_waitcnt lgkmcnt(1)\n"
      "v_add_f32 v88, v88, v89\n"
      "v_add_f32 v90, v90, v91\n"
      "s_waitcnt lgkmcnt(0)\n"
      "v_add_f32 v92, v92, v93\n"
      "v_add_f32 v94, v94, v95\n"
      "v_add_f32 v88, v88, v90\n"
      "v_add_f32 v92, v92, v94\n"
      "v_mul_f32 v91, 0xbacccccd, v88\n"
      "v_mul_f32 v95, 0xbacccccd, v92\n"
      "v_mul_f32 v90, 0xbacccccd, v88\n"
      "v_mul_f32 v94, 0xbacccccd, v92\n"
      "v_mul_f32 v89, 0xbacccccd, v88\n"
      "v_mul_f32 v93, 0xbacccccd, v92\n"
      "v_mul_f32 v88, 0xbacccccd, v88\n"
      "v_mul_f32 v92, 0xbacccccd, v92\n"
      "s_nop 0\n"
      "v_mfma_f32_16x16x32_bf16 v[96:99], v[48:51], v[200:203], v[88:91]\n"
      "v_mfma_f32_16x16x32_bf16 v[100:103], v[52:55], v[200:203], v[88:91]\n"
      "v_mfma_f32_16x16x32_bf16 v[104:107], v[48:51], v[204:207], v[92:95]\n"
      "v_mfma_f32_16x16x32_bf16 v[108:111], v[52:55], v[204:207], v[92:95]\n"
      "v_mfma_f32_16x16x32_bf16 v[112:115], v[56:59], v[200:203], v[88:91]\n"
      "v_mfma_f32_16x16x32_bf16 v[116:119], v[60:63], v[200:203], v[88:91]\n"
      "s_nop 2\n"
      "v_cvt_pk_bf16_f32 v120, v96, v97\n"
      "v_cvt_pk_bf16_f32 v121, v98, v99\n"
      "v_cvt_pk_bf16_f32 v122, v100, v101\n"
      "v_cvt_pk_bf16_f32 v123, v102, v103\n"
      "v_mad_u32_u24 v228, v222, 0, v220\n"
      "buffer_store_dwordx4 v[120:123], v228, s[20:23], 0 offen sc1\n"
      "v_mfma_f32_16x16x32_bf16 v[96:99], v[56:59], v[204:207], v[92:95]\n"
      "v_mfma_f32_16x16x32_bf16 v[100:103], v[60:63], v[204:207], v[92:95]\n"
      "v_cvt_pk_bf16_f32 v124, v104, v105\n"
      "v_cvt_pk_bf16_f32 v125, v106, v107\n"
      "v_cvt_pk_bf16_f32 v126, v108, v109\n"
      "v_cvt_pk_bf16_f32 v127, v110, v111\n"
      "v_mad_u32_u24 v229, v223, 0, v221\n"
      "buffer_store_dwordx4 v[124:127], v229, s[20:23], 0 offen sc1\n"
      "v_mfma_f32_16x16x32_bf16 v[104:107], v[64:67], v[200:203], v[88:91]\n"
      "v_mfma_f32_16x16x32_bf16 v[108:111], v[68:71], v[200:203], v[88:91]\n"
      "v_cvt_pk_bf16_f32 v128, v112, v113\n"
      "v_cvt_pk_bf16_f32 v129, v114, v115\n"
      "v_cvt_pk_bf16_f32 v130, v116, v117\n"
      "v_cvt_pk_bf16_f32 v131, v118, v119\n"
      "v_mad_u32_u24 v230, v222, 1, v220\n"
      "buffer_store_dwordx4 v[128:131], v230, s[20:23], 0 offen sc1\n"
      "v_mfma_f32_16x16x32_bf16 v[112:115], v[64:67], v[204:207], v[92:95]\n"
      "v_mfma_f32_16x16x32_bf16 v[116:119], v[68:71], v[204:207], v[92:95]\n"
      "v_cvt_pk_bf16_f32 v132, v96, v97\n"
      "v_cvt_pk_bf16_f32 v133, v98, v99\n"
      "v_cvt_pk_bf16_f32 v134, v100, v101\n"
      "v_cvt_pk_bf16_f32 v135, v102, v103\n"
      "v_mad_u32_u24 v231, v223, 1, v221\n"
      "buffer_store_dwordx4 v[132:135], v231, s[20:23], 0 offen sc1\n"
      "v_mfma_f32_16x16x32_bf16 v[96:99], v[72:75], v[200:203], v[88:91]\n"
      "v_mfma_f32_16x16x32_bf16 v[100:103], v[76:79], v[200:203], v[88:91]\n"
      "v_cvt_pk_bf16_f32 v136, v104, v105\n"
      "v_cvt_pk_bf16_f32 v137, v106, v107\n"
      "v_cvt_pk_bf16_f32 v138, v108, v109\n"
      "v_cvt_pk_bf16_f32 v139, v110, v111\n"
      "v_mad_u32_u24 v232, v222, 2, v220\n"
      "buffer_store_dwordx4 v[136:139], v232, s[20:23], 0 offen sc1\n"
      "v_mfma_f32_16x16x32_bf16 v[104:107], v[72:75], v[204:207], v[92:95]\n"
      "v_mfma_f32_16x16x32_bf16 v[108:111], v[76:79], v[204:207], v[92:95]\n"
      "v_cvt_pk_bf16_f32 v140, v112, v113\n"
      "v_cvt_pk_bf16_f32 v141, v114, v115\n"
      "v_cvt_pk_bf16_f32 v142, v116, v117\n"
      "v_cvt_pk_bf16_f32 v143, v118, v119\n"
      "v_mad_u32_u24 v233, v223, 2, v221\n"
      "buffer_store_dwordx4 v[140:143], v233, s[20:23], 0 offen sc1\n"
      "v_mfma_f32_16x16x32_bf16 v[112:115], v[80:83], v[200:203], v[88:91]\n"
      "v_mfma_f32_16x16x32_bf16 v[116:119], v[84:87], v[200:203], v[88:91]\n"
      "v_cvt_pk_bf16_f32 v144, v96, v97\n"
      "v_cvt_pk_bf16_f32 v145, v98, v99\n"
      "v_cvt_pk_bf16_f32 v146, v100, v101\n"
      "v_cvt_pk_bf16_f32 v147, v102, v103\n"
      "v_mad_u32_u24 v234, v222, 3, v220\n"
      "buffer_store_dwordx4 v[144:147], v234, s[20:23], 0 offen sc1\n"
      "v_mfma_f32_16x16x32_bf16 v[96:99], v[80:83], v[204:207], v[92:95]\n"
      "v_mfma_f32_16x16x32_bf16 v[100:103], v[84:87], v[204:207], v[92:95]\n"
      "v_cvt_pk_bf16_f32 v148, v104, v105\n"
      "v_cvt_pk_bf16_f32 v149, v106, v107\n"
      "v_cvt_pk_bf16_f32 v150, v108, v109\n"
      "v_cvt_pk_bf16_f32 v151, v110, v111\n"
      "v_mad_u32_u24 v235, v223, 3, v221\n"
      "buffer_store_dwordx4 v[148:151], v235, s[20:23], 0 offen sc1\n"
      "v_cvt_pk_bf16_f32 v152, v112, v113\n"
      "v_cvt_pk_bf16_f32 v153, v114, v115\n"
      "v_cvt_pk_bf16_f32 v154, v116, v117\n"
      "v_cvt_pk_bf16_f32 v155, v118, v119\n"
      "v_mad_u32_u24 v236, v222, 4, v220\n"
      "buffer_store_dwordx4 v[152:155], v236, s[20:23], 0 offen sc1\n"
      "v_cvt_pk_bf16_f32 v156, v96, v97\n"
      "v_cvt_pk_bf16_f32 v157, v98, v99\n"
      "v_cvt_pk_bf16_f32 v158, v100, v101\n"
      "v_cvt_pk_bf16_f32 v159, v102, v103\n"
      "v_mad_u32_u24 v237, v223, 4, v221\n"
      "buffer_store_dwordx4 v[156:159], v237, s[20:23], 0 offen sc1\n"
      "s_add_u32 m0, s14, 0\n"
      "s_add_u32 s80, s42, 21248\n"
      "buffer_load_dwordx4 v192, s[28:31], s80 offen offset:0 lds\n"
      "s_add_u32 m0, s14, 1024\n"
      "s_add_u32 s80, s42, 26368\n"
      "buffer_load_dwordx4 v192, s[28:31], s80 offen offset:0 lds\n"
      "s_add_u32 m0, s14, 2048\n"
      "s_add_u32 s80, s42, 31488\n"
      "buffer_load_dwordx4 v192, s[28:31], s80 offen offset:0 lds\n"
      "s_add_u32 m0, s14, 3072\n"
      "s_add_u32 s80, s42, 36608\n"
      "buffer_load_dwordx4 v192, s[28:31], s80 offen offset:0 lds\n"
      "s_add_u32 m0, s14, 4096\n"
      "s_add_u32 s80, s42, 1024\n"
      "buffer_load_dwordx4 v192, s[28:31], s80 offen offset:0 lds\n"
      "s_add_u32 m0, s14, 5120\n"
      "s_add_u32 s80, s42, 6144\n"
      "buffer_load_dwordx4 v192, s[28:31], s80 offen offset:0 lds\n"
      "s_waitcnt vmcnt(40)\n"
      "ds_read_b128 a[0:3], v194 offset:10240\n"
      "ds_read_b128 a[4:7], v195 offset:10240\n"
      "ds_read_b128 a[8:11], v196 offset:10240\n"
      "ds_read_b128 a[12:15], v197 offset:10240\n"
      "s_add_u32 m0, s14, 6144\n"
      "s_add_u32 s80, s42, 11264\n"
      "buffer_load_dwordx4 v192, s[28:31], s80 offen offset:0 lds\n"
      "s_add_u32 m0, s14, 7168\n"
      "s_add_u32 s80, s42, 16384\n"
      "buffer_load_dwordx4 v192, s[28:31], s80 offen offset:0 lds\n"
      "s_add_u32 m0, s14, 8192\n"
      "s_add_u32 s80, s42, 21504\n"
      "buffer_load_dwordx4 v192, s[28:31], s80 offen offset:0 lds\n"
      "s_add_u32 m0, s14, 9216\n"
      "s_add_u32 s80, s42, 26624\n"
      "buffer_load_dwordx4 v192, s[28:31], s80 offen offset:0 lds\n"
      "s_waitcnt vmcnt(40)\n"
      "ds_read_b128 a[80:83], v194 offset:14336\n"
      "ds_read_b128 a[84:87], v195 offset:14336\n"
      "ds_read_b128 a[88:91], v196 offset:14336\n"
      "ds_read_b128 a[92:95], v197 offset:14336\n"
      "s_waitcnt lgkmcnt(7)\n"
      "v_mfma_f32_16x16x32_bf16 v[160:163], a[0:3], v[176:179], 0\n"
      "v_mfma_f32_16x16x32_bf16 v[164:167], a[0:3], a[0:3], 0\n"
      "s_waitcnt lgkmcnt(6)\n"
      "v_mfma_f32_16x16x32_bf16 v[160:163], a[4:7], v[176:179], v[160:163]\n"
      "v_mfma_f32_16x16x32_bf16 v[164:167], a[4:7], a[4:7], v[164:167]\n"
      "s_waitcnt lgkmcnt(5)\n"
      "v_mfma_f32_16x16x32_bf16 v[160:163], a[8:11], v[176:179], v[160:163]\n"
      "v_mfma_f32_16x16x32_bf16 v[164:167], a[8:11], a[8:11], v[164:167]\n"
      "s_waitcnt lgkmcnt(4)\n"
      "v_mfma_f32_16x16x32_bf16 v[160:163], a[12:15], v[176:179], v[160:163]\n"
      "v_mfma_f32_16x16x32_bf16 v[164:167], a[12:15], a[12:15], v[164:167]\n"
      "s_add_u32 m0, s14, 10240\n"
      "s_add_u32 s80, s42, 31744\n"
      "buffer_load_dwordx4 v192, s[28:31], s80 offen offset:0 lds\n"
      "s_add_u32 m0, s14, 11264\n"
      "s_add_u32 s80, s42, 36864\n"
      "buffer_load_dwordx4 v192, s[28:31], s80 offen offset:0 lds\n"
      "buffer_load_dwordx4 v[180:183], v190, s[28:31], s46 offen\n"
      "buffer_load_dwordx4 v[184:187], v190, s[28:31], s46 offen offset:64\n"
      "buffer_load_dwordx2 v[188:189], v191, s[28:31], s46 offen offset:128\n"
      "s_cmp_lg_u32 s40, 0\n"
      "s_cbranch_scc1 Lw1_%=\n"
      "s_mov_b64 exec, 0x3f\n"
      "buffer_load_dwordx4 v[244:247], v218, s[20:23], 0 offen sc1\n"
      "s_mov_b64 exec, -1\n"
      "Lw1_%=:\n"
      "s_waitcnt vmcnt(41)\n"
      "ds_read_b128 a[16:19], v194 offset:18432\n"
      "ds_read_b128 a[20:23], v195 offset:18432\n"
      "ds_read_b128 a[24:27], v196 offset:18432\n"
      "ds_read_b128 a[28:31], v197 offset:18432\n"
      "s_waitcnt lgkmcnt(7)\n"
      "v_mfma_f32_16x16x32_bf16 v[168:171], a[80:83], v[176:179], 0\n"
      "v_mfma_f32_16x16x32_bf16 v[172:175], a[80:83], a[80:83], 0\n"
      "s_waitcnt lgkmcnt(6)\n"
      "v_mfma_f32_16x16x32_bf16 v[168:171], a[84:87], v[176:179], v[168:171]\n"
      "v_mfma_f32_16x16x32_bf16 v[172:175], a[84:87], a[84:87], v[172:175]\n"
      "s_waitcnt lgkmcnt(5)\n"
      "v_mfma_f32_16x16x32_bf16 v[168:171], a[88:91], v[176:179], v[168:171]\n"
      "v_mfma_f32_16x16x32_bf16 v[172:175], a[88:91], a[88:91], v[172:175]\n"
      "s_waitcnt lgkmcnt(4)\n"
      "v_mfma_f32_16x16x32_bf16 v[168:171], a[92:95], v[176:179], v[168:171]\n"
      "v_mfma_f32_16x16x32_bf16 v[172:175], a[92:95], a[92:95], v[172:175]\n"
      "s_waitcnt vmcnt(37)\n"
      "ds_read_b128 a[96:99], v194 offset:22528\n"
      "ds_read_b128 a[100:103], v195 offset:22528\n"
      "ds_read_b128 a[104:107], v196 offset:22528\n"
      "ds_read_b128 a[108:111], v197 offset:22528\n"
      "s_waitcnt lgkmcnt(7)\n"
      "v_mfma_f32_16x16x32_bf16 v[160:163], a[16:19], v[176:179], v[160:163]\n"
      "v_mfma_f32_16x16x32_bf16 v[164:167], a[16:19], a[16:19], v[164:167]\n"
      "s_waitcnt lgkmcnt(6)\n"
      "v_mfma_f32_16x16x32_bf16 v[160:163], a[20:23], v[176:179], v[160:163]\n"
      "v_mfma_f32_16x16x32_bf16 v[164:167], a[20:23], a[20:23], v[164:167]\n"
      "s_waitcnt lgkmcnt(5)\n"
      "v_mfma_f32_16x16x32_bf16 v[160:163], a[24:27], v[176:179], v[160:163]\n"
      "v_mfma_f32_16x16x32_bf16 v[164:167], a[24:27], a[24:27], v[164:167]\n"
      "s_waitcnt lgkmcnt(4)\n"
      "v_mfma_f32_16x16x32_bf16 v[160:163], a[28:31], v[176:179], v[160:163]\n"
      "v_mfma_f32_16x16x32_bf16 v[164:167], a[28:31], a[28:31], v[164:167]\n"
      "s_waitcnt vmcnt(33)\n"
      "ds_read_b128 a[32:35], v194 offset:26624\n"
      "ds_read_b128 a[36:39], v195 offset:26624\n"
      "ds_read_b128 a[40:43], v196 offset:26624\n"
      "ds_read_b128 a[44:47], v197 offset:26624\n"
      "s_waitcnt lgkmcnt(7)\n"
      "v_mfma_f32_16x16x32_bf16 v[168:171], a[96:99], v[176:179], v[168:171]\n"
      "v_mfma_f32_16x16x32_bf16 v[172:175], a[96:99], a[96:99], v[172:175]\n"
      "s_waitcnt lgkmcnt(6)\n"
      "v_mfma_f32_16x16x32_bf16 v[168:171], a[100:103], v[176:179], v[168:171]\n"
      "v_mfma_f32_16x16x32_bf16 v[172:175], a[100:103], a[100:103], v[172:175]\n"
      "s_waitcnt lgkmcnt(5)\n"
      "v_mfma_f32_16x16x32_bf16 v[168:171], a[104:107], v[176:179], v[168:171]\n"
      "v_mfma_f32_16x16x32_bf16 v[172:175], a[104:107], a[104:107], v[172:175]\n"
      "s_waitcnt lgkmcnt(4)\n"
      "v_mfma_f32_16x16x32_bf16 v[168:171], a[108:111], v[176:179], v[168:171]\n"
      "v_mfma_f32_16x16x32_bf16 v[172:175], a[108:111], a[108:111], v[172:175]\n"
      "s_waitcnt vmcnt(29)\n"
      "ds_read_b128 a[112:115], v194 offset:30720\n"
      "ds_read_b128 a[116:119], v195 offset:30720\n"
      "ds_read_b128 a[120:123], v196 offset:30720\n"
      "ds_read_b128 a[124:127], v197 offset:30720\n"
      "s_waitcnt lgkmcnt(7)\n"
      "v_mfma_f32_16x16x32_bf16 v[160:163], a[32:35], v[176:179], v[160:163]\n"
      "v_mfma_f32_16x16x32_bf16 v[164:167], a[32:35], a[32:35], v[164:167]\n"
      "s_waitcnt lgkmcnt(6)\n"
      "v_mfma_f32_16x16x32_bf16 v[160:163], a[36:39], v[176:179], v[160:163]\n"
      "v_mfma_f32_16x16x32_bf16 v[164:167], a[36:39], a[36:39], v[164:167]\n"
      "s_waitcnt lgkmcnt(5)\n"
      "v_mfma_f32_16x16x32_bf16 v[160:163], a[40:43], v[176:179], v[160:163]\n"
      "v_mfma_f32_16x16x32_bf16 v[164:167], a[40:43], a[40:43], v[164:167]\n"
      "s_waitcnt lgkmcnt(4)\n"
      "v_mfma_f32_16x16x32_bf16 v[160:163], a[44:47], v[176:179], v[160:163]\n"
      "v_mfma_f32_16x16x32_bf16 v[164:167], a[44:47], a[44:47], v[164:167]\n"
      "s_waitcnt vmcnt(25)\n"
      "ds_read_b128 a[48:51], v194 offset:34816\n"
      "ds_read_b128 a[52:55], v195 offset:34816\n"
      "ds_read_b128 a[56:59], v196 offset:34816\n"
      "ds_read_b128 a[60:63], v197 offset:34816\n"
      "s_waitcnt lgkmcnt(7)\n"
      "v_mfma_f32_16x16x32_bf16 v[168:171], a[112:115], v[176:179], v[168:171]\n"
      "v_mfma_f32_16x16x32_bf16 v[172:175], a[112:115], a[112:115], v[172:175]\n"
      "s_waitcnt lgkmcnt(6)\n"
      "v_mfma_f32_16x16x32_bf16 v[168:171], a[116:119], v[176:179], v[168:171]\n"
      "v_mfma_f32_16x16x32_bf16 v[172:175], a[116:119], a[116:119], v[172:175]\n"
      "s_waitcnt lgkmcnt(5)\n"
      "v_mfma_f32_16x16x32_bf16 v[168:171], a[120:123], v[176:179], v[168:171]\n"
      "v_mfma_f32_16x16x32_bf16 v[172:175], a[120:123], a[120:123], v[172:175]\n"
      "s_waitcnt lgkmcnt(4)\n"
      "v_mfma_f32_16x16x32_bf16 v[168:171], a[124:127], v[176:179], v[168:171]\n"
      "v_mfma_f32_16x16x32_bf16 v[172:175], a[124:127], a[124:127], v[172:175]\n"
      "s_waitcnt vmcnt(11)\n"
      "ds_read_b128 a[128:131], v194 offset:0\n"
      "ds_read_b128 a[132:135], v195 offset:0\n"
      "ds_read_b128 a[136:139], v196 offset:0\n"
      "ds_read_b128 a[140:143], v197 offset:0\n"
      "s_waitcnt lgkmcnt(7)\n"
      "v_mfma_f32_16x16x32_bf16 v[160:163], a[48:51], v[176:179], v[160:163]\n"
      "v_mfma_f32_16x16x32_bf16 v[164:167], a[48:51], a[48:51], v[164:167]\n"
      "s_waitcnt lgkmcnt(6)\n"
      "v_mfma_f32_16x16x32_bf16 v[160:163], a[52:55], v[176:179], v[160:163]\n"
      "v_mfma_f32_16x16x32_bf16 v[164:167], a[52:55], a[52:55], v[164:167]\n"
      "s_waitcnt lgkmcnt(5)\n"
      "v_mfma_f32_16x16x32_bf16 v[160:163], a[56:59], v[176:179], v[160:163]\n"
      "v_mfma_f32_16x16x32_bf16 v[164:167], a[56:59], a[56:59], v[164:167]\n"
      "s_waitcnt lgkmcnt(4)\n"
      "v_mfma_f32_16x16x32_bf16 v[160:163], a[60:63], v[176:179], v[160:163]\n"
      "v_mfma_f32_16x16x32_bf16 v[164:167], a[60:63], a[60:63], v[164:167]\n"
      "s_waitcnt vmcnt(7)\n"
      "ds_read_b128 a[64:67], v194 offset:4096\n"
      "ds_read_b128 a[68:71], v195 offset:4096\n"
      "ds_read_b128 a[72:75], v196 offset:4096\n"
      "ds_read_b128 a[76:79], v197 offset:4096\n"
      "s_waitcnt lgkmcnt(7)\n"
      "v_mfma_f32_16x16x32_bf16 v[168:171], a[128:131], v[176:179], v[168:171]\n"
      "v_mfma_f32_16x16x32_bf16 v[172:175], a[128:131], a[128:131], v[172:175]\n"
      "s_waitcnt lgkmcnt(6)\n"
      "v_mfma_f32_16x16x32_bf16 v[168:171], a[132:135], v[176:179], v[168:171]\n"
      "v_mfma_f32_16x16x32_bf16 v[172:175], a[132:135], a[132:135], v[172:175]\n"
      "s_waitcnt lgkmcnt(5)\n"
      "v_mfma_f32_16x16x32_bf16 v[168:171], a[136:139], v[176:179], v[168:171]\n"
      "v_mfma_f32_16x16x32_bf16 v[172:175], a[136:139], a[136:139], v[172:175]\n"
      "s_waitcnt lgkmcnt(4)\n"
      "v_mfma_f32_16x16x32_bf16 v[168:171], a[140:143], v[176:179], v[168:171]\n"
      "v_mfma_f32_16x16x32_bf16 v[172:175], a[140:143], a[140:143], v[172:175]\n"
      "s_waitcnt vmcnt(3)\n"
      "ds_read_b128 a[144:147], v194 offset:8192\n"
      "ds_read_b128 a[148:151], v195 offset:8192\n"
      "ds_read_b128 a[152:155], v196 offset:8192\n"
      "ds_read_b128 a[156:159], v197 offset:8192\n"
      "s_waitcnt lgkmcnt(0)\n"
      "s_cmp_lg_u32 s40, 0\n"
      "s_cbranch_scc1 Lz1_%=\n"
      "s_mov_b32 s84, 400000\n"
      "s_mov_b64 exec, 0x3f\n"
      "s_waitcnt vmcnt(0)\n"
      "Ly1_%=:\n"
      "v_sub_u32 v246, v246, v244\n"
      "v_sub_u32 v247, v247, v245\n"
      "v_xor_b32 v246, 0x7f4a7c15, v246\n"
      "v_xor_b32 v247, 0x9e3779b9, v247\n"
      "v_or_b32 v246, v246, v247\n"
      "v_cmp_ne_u32 vcc, 0, v246\n"
      "s_cbranch_vccz Lx1_%=\n"
      "buffer_load_dwordx4 v[244:247], v218, s[20:23], 0 offen sc1\n"
      "s_waitcnt vmcnt(0)\n"
      "s_sub_u32 s84, s84, 1\n"
      "s_cmp_lg_u32 s84, 0\n"
      "s_cbranch_scc1 Ly1_%=\n"
      "Lx1_%=:\n"
      "s_mov_b64 exec, -1\n"
      "Lz1_%=:\n"
      "s_barrier\n"
      "s_add_u32 s79, s65, 0\n"
      "s_add_u32 m0, s72, 0\n"
      "s_nop 0\n"
      "buffer_load_dwordx4 v0, s[20:23], s79 offen offset:0 sc1 lds\n"
      "buffer_load_dwordx4 v0, s[20:23], s79 offen offset:1024 sc1 lds\n"
      "buffer_load_dwordx4 v0, s[20:23], s79 offen offset:2048 sc1 lds\n"
      "buffer_load_dwordx4 v0, s[20:23], s79 offen offset:3072 sc1 lds\n"
      "s_add_u32 s79, s65, 4096\n"
      "s_add_u32 m0, s72, 4096\n"
      "s_nop 0\n"
      "buffer_load_dwordx4 v0, s[20:23], s79 offen offset:0 sc1 lds\n"
      "s_add_u32 s79, s65, 20480\n"
      "s_add_u32 m0, s72, 20480\n"
      "s_nop 0\n"
      "buffer_load_dwordx4 v0, s[20:23], s79 offen offset:0 sc1 lds\n"
      "buffer_load_dwordx4 v0, s[20:23], s79 offen offset:1024 sc1 lds\n"
      "buffer_load_dwordx4 v0, s[20:23], s79 offen offset:2048 sc1 lds\n"
      "buffer_load_dwordx4 v0, s[20:23], s79 offen offset:3072 sc1 lds\n"
      "s_add_u32 s79, s65, 24576\n"
      "s_add_u32 m0, s72, 24576\n"
      "s_nop 0\n"
      "buffer_load_dwordx4 v0, s[20:23], s79 offen offset:0 sc1 lds\n"
      "v_mfma_f32_16x16x32_bf16 v[160:163], a[64:67], v[176:179], v[160:163]\n"
      "v_mfma_f32_16x16x32_bf16 v[164:167], a[64:67], a[64:67], v[164:167]\n"
      "v_mfma_f32_16x16x32_bf16 v[160:163], a[68:71], v[176:179], v[160:163]\n"
      "v_mfma_f32_16x16x32_bf16 v[164:167], a[68:71], a[68:71], v[164:167]\n"
      "v_mfma_f32_16x16x32_bf16 v[160:163], a[72:75], v[176:179], v[160:163]\n"
      "v_mfma_f32_16x16x32_bf16 v[164:167], a[72:75], a[72:75], v[164:167]\n"
      "v_mfma_f32_16x16x32_bf16 v[160:163], a[76:79], v[176:179], v[160:163]\n"
      "v_mfma_f32_16x16x32_bf16 v[164:167], a[76:79], a[76:79], v[164:167]\n"
      "v_mfma_f32_16x16x32_bf16 v[168:171], a[144:147], v[176:179], v[168:171]\n"
      "v_mfma_f32_16x16x32_bf16 v[172:175], a[144:147], a[144:147], v[172:175]\n"
      "v_mfma_f32_16x16x32_bf16 v[168:171], a[148:151], v[176:179], v[168:171]\n"
      "v_mfma_f32_16x16x32_bf16 v[172:175], a[148:151], a[148:151], v[172:175]\n"
      "v_mfma_f32_16x16x32_bf16 v[168:171], a[152:155], v[176:179], v[168:171]\n"
      "v_mfma_f32_16x16x32_bf16 v[172:175], a[152:155], a[152:155], v[172:175]\n"
      "v_mfma_f32_16x16x32_bf16 v[168:171], a[156:159], v[176:179], v[168:171]\n"
      "v_mfma_f32_16x16x32_bf16 v[172:175], a[156:159], a[156:159], v[172:175]\n"
      "s_waitcnt vmcnt(12)\n"
      "v_lshlrev_b32 v12, 16, v180\n"
      "v_and_b32 v13, 0xffff0000, v180\n"
      "v_lshlrev_b32 v14, 16, v181\n"
      "v_and_b32 v15, 0xffff0000, v181\n"
      "v_lshlrev_b32 v16, 16, v182\n"
      "v_and_b32 v17, 0xffff0000, v182\n"
      "v_lshlrev_b32 v18, 16, v183\n"
      "v_and_b32 v19, 0xffff0000, v183\n"
      "s_waitcnt vmcnt(11)\n"
      "v_lshlrev_b32 v20, 16, v184\n"
      "v_and_b32 v21, 0xffff0000, v184\n"
      "v_lshlrev_b32 v22, 16, v185\n"
      "v_and_b32 v23, 0xffff0000, v185\n"
      "v_lshlrev_b32 v24, 16, v186\n"
      "v_and_b32 v25, 0xffff0000, v186\n"
      "v_lshlrev_b32 v26, 16, v187\n"
      "v_and_b32 v27, 0xffff0000, v187\n"
      "s_waitcnt vmcnt(10)\n"
      "v_lshlrev_b32 v28, 16, v188\n"
      "v_and_b32 v29, 0xffff0000, v188\n"
      "v_lshlrev_b32 v30, 16, v189\n"
      "v_and_b32 v31, 0xffff0000, v189\n"
      "s_mov_b32 s90, 0x22222222\n"
      "s_mov_b32 s91, 0x22222222\n"
      "s_mov_b32 s92, 0x44444444\n"
      "s_mov_b32 s93, 0x44444444\n"
      "s_mov_b32 s94, 0x88888888\n"
      "s_mov_b32 s95, 0x88888888\n"
      "s_lshl_b32 s76, s40, 7\n"
      "s_add_u32 s76, s76, 159744\n"
      "v_lshrrev_b32 v38, 2, v0\n"
      "v_and_b32 v38, 0x3c, v38\n"
      "v_add_u32 v38, s76, v38\n"
      "v_lshrrev_b32 v39, 4, v0\n"
      "v_and_b32 v39, 0x30, v39\n"
      "v_add_u32 v39, s76, v39\n"
      "v_cndmask_b32 v160, v160, v161, s[90:91]\n"
      "v_cndmask_b32 v164, v164, v165, s[90:91]\n"
      "v_cndmask_b32 v168, v168, v169, s[90:91]\n"
      "v_cndmask_b32 v172, v172, v173, s[90:91]\n"
      "v_cndmask_b32 v160, v160, v162, s[92:93]\n"
      "v_cndmask_b32 v164, v164, v166, s[92:93]\n"
      "v_cndmask_b32 v168, v168, v170, s[92:93]\n"
      "v_cndmask_b32 v172, v172, v174, s[92:93]\n"
      "v_cndmask_b32 v160, v160, v163, s[94:95]\n"
      "v_cndmask_b32 v164, v164, v167, s[94:95]\n"
      "v_cndmask_b32 v168, v168, v171, s[94:95]\n"
      "v_cndmask_b32 v172, v172, v175, s[94:95]\n"
      "v_mul_f32 v160, 0x3acccccd, v160\n"
      "v_mul_f32 v164, 0x3acccccd, v164\n"
      "v_mul_f32 v168, 0x3acccccd, v168\n"
      "v_mul_f32 v172, 0x3acccccd, v172\n"
      "v_fma_f32 v164, -v160, v160, v164\n"
      "v_fma_f32 v172, -v168, v168, v172\n"
      "v_max_f32 v164, 0, v164\n"
      "v_max_f32 v172, 0, v172\n"
      "v_add_f32 v164, 0x3727c5ac, v164\n"
      "v_add_f32 v172, 0x3727c5ac, v172\n"
      "v_rsq_f32 v164, v164\n"
      "v_rsq_f32 v172, v172\n"
      "v_mul_f32 v164, 0x3fb8aa3b, v164\n"
      "v_mul_f32 v172, 0x3fb8aa3b, v172\n"
      "s_mov_b32 exec_lo, 0x00f0000f\n"
      "s_mov_b32 exec_hi, 0xf0000f00\n"
      "ds_write_b32 v38, v164\n"
      "ds_write_b32 v38, v172 offset:64\n"
      "s_mov_b64 exec, -1\n"
      "s_waitcnt vmcnt(5)\n"
      "s_waitcnt lgkmcnt(0)\n"
      "s_branch LJk_%=\n"
      "LJo_%=:\n"
      "s_lshr_b32 s75, %[b], 3\n"
      "s_lshl_b32 s75, s75, 2\n"
      "s_add_u32 s75, s75, s40\n"
      "v_lshlrev_b32 v248, 7, v11\n"
      "v_add_u32 v248, s75, v248\n"
      "v_lshlrev_b32 v249, 16, v248\n"
      "buffer_load_dword v250, v249, s[20:23], 0 offen sc1\n"
      "v_lshlrev_b32 v249, 16, v248\n"
      "s_mov_b64 s[96:97], %[o]\n"
      "s_mov_b32 s98, 2621440\n"
      "s_mov_b32 s99, 0x20000\n"
      "buffer_load_dword v252, v249, s[96:99], 0 offen sc1\n"
      "v_lshl_add_u32 v45, v40, 6, v37\n"
      "v_add_u32 v193, s14, v45\n"
      "v_and_b32 v45, 0xff, v45\n"
      "v_lshl_add_u32 v45, v40, 8, v45\n"
      "v_add_u32 v194, s14, v45\n"
      "v_xor_b32 v195, 64, v194\n"
      "v_xor_b32 v196, 128, v194\n"
      "v_xor_b32 v197, 192, v194\n"
      "s_add_u32 s24, s20, 10223616\n"
      "s_addc_u32 s25, s21, 0\n"
      "s_mov_b32 s26, 32899072\n"
      "s_mov_b32 s27, 0x20000\n"
      "s_mov_b32 s41, 0\n"
      "s_mul_i32 s71, s40, 13312\n"
      "s_mul_i32 s72, s40, 5120\n"
      "s_mul_i32 s77, s91, 153600\n"
      "s_add_u32 s65, s77, s72\n"
      "s_add_u32 s66, s77, s71\n"
      "s_and_b32 s92, s91, 7\n"
      "s_lshl_b32 s77, s90, 3\n"
      "s_lshr_b32 s78, s91, 3\n"
      "s_lshl_b32 s78, s78, 2\n"
      "s_add_u32 s75, s77, s78\n"
      "s_add_u32 s94, s75, s40\n"
      "s_lshl_b32 s43, s93, 10\n"
      "s_mul_i32 s44, s94, 20480\n"
      "s_mul_i32 s77, s92, 160\n"
      "s_add_u32 s46, s44, s77\n"
      "s_mul_i32 s77, s92, 2560\n"
      "s_add_u32 s44, s44, s77\n"
      "s_lshl_b32 s45, s94, 10\n"
      "s_lshl_b32 s77, s92, 7\n"
      "s_add_u32 s45, s45, s77\n"
      "s_mul_i32 s58, s40, 15360\n"
      "s_mul_i32 s76, s40, 14336\n"
      "s_add_u32 s77, s40, 1\n"
      "s_and_b32 s77, s77, 3\n"
      "s_mul_i32 s55, s77, 15360\n"
      "s_add_u32 s55, s55, 0\n"
      "s_add_u32 s77, s77, s75\n"
      "s_mul_i32 s47, s77, 20480\n"
      "s_add_u32 s47, s47, s72\n"
      "s_mul_i32 s51, s77, 57344\n"
      "s_add_u32 s51, s51, s76\n"
      "s_add_u32 s77, s40, 2\n"
      "s_and_b32 s77, s77, 3\n"
      "s_mul_i32 s56, s77, 15360\n"
      "s_add_u32 s56, s56, 5120\n"
      "s_add_u32 s77, s77, s75\n"
      "s_mul_i32 s48, s77, 20480\n"
      "s_add_u32 s48, s48, s72\n"
      "s_mul_i32 s52, s77, 57344\n"
      "s_add_u32 s52, s52, s76\n"
      "s_add_u32 s77, s40, 3\n"
      "s_and_b32 s77, s77, 3\n"
      "s_mul_i32 s57, s77, 15360\n"
      "s_add_u32 s57, s57, 10240\n"
      "s_add_u32 s77, s77, s75\n"
      "s_mul_i32 s49, s77, 20480\n"
      "s_add_u32 s49, s49, s72\n"
      "s_mul_i32 s53, s77, 57344\n"
      "s_add_u32 s53, s53, s76\n"
      "s_add_u32 s77, s40, 4\n"
      "s_and_b32 s77, s77, 3\n"
      "s_add_u32 s77, s77, s75\n"
      "s_mul_i32 s50, s77, 20480\n"
      "s_add_u32 s50, s50, s72\n"
      "s_mul_i32 s54, s77, 57344\n"
      "s_add_u32 s54, s54, s76\n"
      "s_lshr_b32 s75, s91, 2\n"
      "s_and_b32 s76, s91, 3\n"
      "s_mul_i32 s77, s75, 5120\n"
      "s_add_u32 s77, s77, s42\n"
      "s_lshl_b32 s78, s76, 10\n"
      "s_add_u32 s59, s77, s78\n"
      "s_lshl_b32 s78, s76, 8\n"
      "s_add_u32 s60, s77, s78\n"
      "s_mul_i32 s77, s93, 57344\n"
      "s_mul_i32 s78, s75, 14336\n"
      "s_add_u32 s77, s77, s78\n"
      "s_lshl_b32 s78, s76, 10\n"
      "s_add_u32 s61, s77, s78\n"
      "s_lshl_b32 s78, s76, 9\n"
      "s_add_u32 s62, s77, s78\n"
      "s_lshl_b32 s77, s91, 15\n"
      "s_lshl_b32 s78, s40, 13\n"
      "s_add_u32 s64, s77, s78\n"
      "s_lshl_b32 s73, s90, 9\n"
      "s_lshl_b32 s74, s91, 9\n"
      "s_add_u32 s63, s64, s73\n"
      "s_mul_i32 s67, s91, 286720\n"
      "s_add_u32 s67, s67, s72\n"
      "s_lshl_b32 s77, s92, 2\n"
      "s_add_u32 s77, s77, s40\n"
      "s_mul_i32 s68, s77, 25600\n"
      "s_mul_i32 s69, s77, 71680\n"
      "s_mov_b32 s86, 0x35c1a2a0\n"
      "s_mov_b32 s87, 0x35c1a2a0\n"
      "s_mov_b32 s88, 0x3f317218\n"
      "s_mov_b32 s89, 0x3f317218\n"
      "v_bfe_u32 v41, v11, 4, 1\n"
      "v_lshrrev_b32 v42, 5, v11\n"
      "v_lshlrev_b32 v43, 4, v40\n"
      "v_lshl_add_u32 v44, v42, 8, v43\n"
      "v_lshl_add_u32 v36, v41, 4, v36\n"
      "v_lshl_add_u32 v191, v42, 3, v36\n"
      "v_mov_b32 v45, 20480\n"
      "v_mad_u32_u24 v1, v41, v45, v44\n"
      "v_mov_b32 v45, 57344\n"
      "v_mad_u32_u24 v2, v41, v45, v44\n"
      "v_lshl_add_u32 v5, v41, 3, v44\n"
      "v_lshlrev_b32 v45, 2, v40\n"
      "v_lshl_add_u32 v34, v42, 6, v45\n"
      "v_lshl_add_u32 v3, v41, 10, v34\n"
      "v_lshl_add_u32 v45, v41, 8, v43\n"
      "v_lshl_add_u32 v4, v42, 3, v45\n"
      "v_and_b32 v45, 0x70, v0\n"
      "v_lshrrev_b32 v46, 4, v11\n"
      "v_lshl_add_u32 v45, v46, 7, v45\n"
      "v_add_u32 v6, 143360, v45\n"
      "v_add_u32 v7, 0x10000, v0\n"
      "v_add_u32 v8, 0x20000, v0\n"
      "v_mov_b32 v9, 0\n"
      "v_mov_b32 v10, 1\n"
      "v_mov_b32 v32, v0\n"
      "v_mov_b32 v33, v4\n"
      "s_add_u32 s81, s44, 16449536\n"
      "s_add_u32 s82, s44, 16450560\n"
      "s_add_u32 s83, s44, 16451584\n"
      "v_mov_b32 v176, 0x3f803f80\n"
      "v_mov_b32 v177, 0x3f803f80\n"
      "v_mov_b32 v178, 0x3f803f80\n"
      "v_mov_b32 v179, 0x3f803f80\n"
      "v_mov_b32 v240, 0\n"
      "v_mov_b32 v241, 0\n"
      "v_mov_b32 v242, 0\n"
      "v_mov_b32 v243, 0\n"
      "buffer_store_dwordx4 v[240:243], v219, s[20:23], 0 offen sc1\n"
      "s_add_u32 m0, s14, 12288\n"
      "s_add_u32 s80, s42, 10240\n"
      "buffer_load_dwordx4 v192, s[28:31], s80 offen offset:0 lds\n"
      "s_add_u32 m0, s14, 13312\n"
      "s_add_u32 s80, s42, 15360\n"
      "buffer_load_dwordx4 v192, s[28:31], s80 offen offset:0 lds\n"
      "s_add_u32 m0, s14, 14336\n"
      "s_add_u32 s80, s42, 20480\n"
      "buffer_load_dwordx4 v192, s[28:31], s80 offen offset:0 lds\n"
      "s_add_u32 m0, s14, 15360\n"
      "s_add_u32 s80, s42, 25600\n"
      "buffer_load_dwordx4 v192, s[28:31], s80 offen offset:0 lds\n"
      "s_add_u32 m0, s14, 16384\n"
      "s_add_u32 s80, s42, 30720\n"
      "buffer_load_dwordx4 v192, s[28:31], s80 offen offset:0 lds\n"
      "s_add_u32 m0, s14, 17408\n"
      "s_add_u32 s80, s42, 35840\n"
      "buffer_load_dwordx4 v192, s[28:31], s80 offen offset:0 lds\n"
      "s_add_u32 m0, s14, 18432\n"
      "s_add_u32 s80, s42, 256\n"
      "buffer_load_dwordx4 v192, s[28:31], s80 offen offset:0 lds\n"
      "s_add_u32 m0, s14, 19456\n"
      "s_add_u32 s80, s42, 5376\n"
      "buffer_load_dwordx4 v192, s[28:31], s80 offen offset:0 lds\n"
      "s_add_u32 m0, s14, 20480\n"
      "s_add_u32 s80, s42, 10496\n"
      "buffer_load_dwordx4 v192, s[28:31], s80 offen offset:0 lds\n"
      "s_add_u32 m0, s14, 21504\n"
      "s_add_u32 s80, s42, 15616\n"
      "buffer_load_dwordx4 v192, s[28:31], s80 offen offset:0 lds\n"
      "s_add_u32 m0, s14, 22528\n"
      "s_add_u32 s80, s42, 20736\n"
      "buffer_load_dwordx4 v192, s[28:31], s80 offen offset:0 lds\n"
      "s_add_u32 m0, s14, 23552\n"
      "s_add_u32 s80, s42, 25856\n"
      "buffer_load_dwordx4 v192, s[28:31], s80 offen offset:0 lds\n"
      "s_add_u32 m0, s14, 24576\n"
      "s_add_u32 s80, s42, 30976\n"
      "buffer_load_dwordx4 v192, s[28:31], s80 offen offset:0 lds\n"
      "s_add_u32 m0, s14, 25600\n"
      "s_add_u32 s80, s42, 36096\n"
      "buffer_load_dwordx4 v192, s[28:31], s80 offen offset:0 lds\n"
      "s_add_u32 m0, s14, 26624\n"
      "s_add_u32 s80, s42, 512\n"
      "buffer_load_dwordx4 v192, s[28:31], s80 offen offset:0 lds\n"
      "s_add_u32 m0, s14, 27648\n"
      "s_add_u32 s80, s42, 5632\n"
      "buffer_load_dwordx4 v192, s[28:31], s80 offen offset:0 lds\n"
      "s_add_u32 m0, s14, 28672\n"
      "s_add_u32 s80, s42, 10752\n"
      "buffer_load_dwordx4 v192, s[28:31], s80 offen offset:0 lds\n"
      "s_add_u32 m0, s14, 29696\n"
      "s_add_u32 s80, s42, 15872\n"
      "buffer_load_dwordx4 v192, s[28:31], s80 offen offset:0 lds\n"
      "s_add_u32 m0, s14, 30720\n"
      "s_add_u32 s80, s42, 20992\n"
      "buffer_load_dwordx4 v192, s[28:31], s80 offen offset:0 lds\n"
      "s_add_u32 m0, s14, 31744\n"
      "s_add_u32 s80, s42, 26112\n"
      "buffer_load_dwordx4 v192, s[28:31], s80 offen offset:0 lds\n"
      "s_add_u32 m0, s14, 32768\n"
      "s_add_u32 s80, s42, 31232\n"
      "buffer_load_dwordx4 v192, s[28:31], s80 offen offset:0 lds\n"
      "s_add_u32 m0, s14, 33792\n"
      "s_add_u32 s80, s42, 36352\n"
      "buffer_load_dwordx4 v192, s[28:31], s80 offen offset:0 lds\n"
      "s_add_u32 m0, s14, 34816\n"
      "s_add_u32 s80, s42, 768\n"
      "buffer_load_dwordx4 v192, s[28:31], s80 offen offset:0 lds\n"
      "s_add_u32 m0, s14, 35840\n"
      "s_add_u32 s80, s42, 5888\n"
      "buffer_load_dwordx4 v192, s[28:31], s80 offen offset:0 lds\n"
      "s_add_u32 m0, s14, 36864\n"
      "s_add_u32 s80, s42, 11008\n"
      "buffer_load_dwordx4 v192, s[28:31], s80 offen offset:0 lds\n"
      "s_add_u32 m0, s14, 37888\n"
      "s_add_u32 s80, s42, 16128\n"
      "buffer_load_dwordx4 v192, s[28:31], s80 offen offset:0 lds\n"
      "s_waitcnt vmcnt(40)\n"
      "ds_read_b128 v[48:51], v193 offset:0\n"
      "s_waitcnt vmcnt(39)\n"
      "ds_read_b128 v[52:55], v193 offset:1024\n"
      "s_waitcnt vmcnt(38)\n"
      "ds_read_b128 v[56:59], v193 offset:2048\n"
      "s_waitcnt vmcnt(37)\n"
      "ds_read_b128 v[60:63], v193 offset:3072\n"
      "s_waitcnt vmcnt(36)\n"
      "ds_read_b128 v[64:67], v193 offset:4096\n"
      "s_waitcnt vmcnt(35)\n"
      "ds_read_b128 v[68:71], v193 offset:5120\n"
      "s_waitcnt vmcnt(34)\n"
      "ds_read_b128 v[72:75], v193 offset:6144\n"
      "s_waitcnt vmcnt(33)\n"
      "ds_read_b128 v[76:79], v193 offset:7168\n"
      "s_waitcnt vmcnt(32)\n"
      "ds_read_b128 v[80:83], v193 offset:8192\n"
      "s_waitcnt vmcnt(31)\n"
      "ds_read_b128 v[84:87], v193 offset:9216\n"
      "s_waitcnt lgkmcnt(0)\n"
      "v_mfma_f32_16x16x32_bf16 v[96:99], v[48:51], v[200:203], 0\n"
      "v_mfma_f32_16x16x32_bf16 v[100:103], v[52:55], v[200:203], 0\n"
      "v_mfma_f32_16x16x32_bf16 v[104:107], v[48:51], v[204:207], 0\n"
      "v_mfma_f32_16x16x32_bf16 v[108:111], v[52:55], v[204:207], 0\n"
      "v_mfma_f32_16x16x32_bf16 v[112:115], v[56:59], v[200:203], 0\n"
      "v_mfma_f32_16x16x32_bf16 v[116:119], v[60:63], v[200:203], 0\n"
      "s_nop 2\n"
      "v_cvt_pk_bf16_f32 v120, v96, v97\n"
      "v_cvt_pk_bf16_f32 v121, v98, v99\n"
      "v_cvt_pk_bf16_f32 v122, v100, v101\n"
      "v_cvt_pk_bf16_f32 v123, v102, v103\n"
      "v_add_u32 v228, 0, v220\n"
      "buffer_store_dwordx4 v[120:123], v228, s[20:23], 0 offen sc1\n"
      "v_mfma_f32_16x16x32_bf16 v[96:99], v[56:59], v[204:207], 0\n"
      "v_mfma_f32_16x16x32_bf16 v[100:103], v[60:63], v[204:207], 0\n"
      "v_cvt_pk_bf16_f32 v124, v104, v105\n"
      "v_cvt_pk_bf16_f32 v125, v106, v107\n"
      "v_cvt_pk_bf16_f32 v126, v108, v109\n"
      "v_cvt_pk_bf16_f32 v127, v110, v111\n"
      "v_add_u32 v229, 1024, v220\n"
      "buffer_store_dwordx4 v[124:127], v229, s[20:23], 0 offen sc1\n"
      "v_mfma_f32_16x16x32_bf16 v[104:107], v[64:67], v[208:211], 0\n"
      "v_mfma_f32_16x16x32_bf16 v[108:111], v[64:67], v[212:215], 0\n"
      "v_cvt_pk_bf16_f32 v128, v112, v113\n"
      "v_cvt_pk_bf16_f32 v129, v114, v115\n"
      "v_cvt_pk_bf16_f32 v130, v116, v117\n"
      "v_cvt_pk_bf16_f32 v131, v118, v119\n"
      "v_add_u32 v230, 2048, v220\n"
      "buffer_store_dwordx4 v[128:131], v230, s[20:23], 0 offen sc1\n"
      "v_mfma_f32_16x16x32_bf16 v[112:115], v[68:71], v[200:203], 0\n"
      "v_mfma_f32_16x16x32_bf16 v[116:119], v[72:75], v[200:203], 0\n"
      "v_cvt_pk_bf16_f32 v132, v96, v97\n"
      "v_cvt_pk_bf16_f32 v133, v98, v99\n"
      "v_cvt_pk_bf16_f32 v134, v100, v101\n"
      "v_cvt_pk_bf16_f32 v135, v102, v103\n"
      "v_add_u32 v231, 3072, v220\n"
      "buffer_store_dwordx4 v[132:135], v231, s[20:23], 0 offen sc1\n"
      "v_mfma_f32_16x16x32_bf16 v[96:99], v[68:71], v[204:207], 0\n"
      "v_mfma_f32_16x16x32_bf16 v[100:103], v[72:75], v[204:207], 0\n"
      "v_cvt_pk_bf16_f32 v136, v104, v105\n"
      "v_cvt_pk_bf16_f32 v137, v106, v107\n"
      "v_cvt_pk_bf16_f32 v138, v108, v109\n"
      "v_cvt_pk_bf16_f32 v139, v110, v111\n"
      "v_add_u32 v232, 4096, v220\n"
      "buffer_store_dwordx4 v[136:139], v232, s[20:23], 0 offen sc1\n"
      "v_mfma_f32_16x16x32_bf16 v[104:107], v[76:79], v[200:203], 0\n"
      "v_mfma_f32_16x16x32_bf16 v[108:111], v[80:83], v[200:203], 0\n"
      "v_cvt_pk_bf16_f32 v140, v112, v113\n"
      "v_cvt_pk_bf16_f32 v141, v114, v115\n"
      "v_cvt_pk_bf16_f32 v142, v116, v117\n"
      "v_cvt_pk_bf16_f32 v143, v118, v119\n"
      "v_add_u32 v233, 0, v221\n"
      "buffer_store_dwordx4 v[140:143], v233, s[20:23], 0 offen sc1\n"
      "v_mfma_f32_16x16x32_bf16 v[112:115], v[76:79], v[204:207], 0\n"
      "v_mfma_f32_16x16x32_bf16 v[116:119], v[80:83], v[204:207], 0\n"
      "v_cvt_pk_bf16_f32 v144, v96, v97\n"
      "v_cvt_pk_bf16_f32 v145, v98, v99\n"
      "v_cvt_pk_bf16_f32 v146, v100, v101\n"
      "v_cvt_pk_bf16_f32 v147, v102, v103\n"
      "v_add_u32 v234, 1024, v221\n"
      "buffer_store_dwordx4 v[144:147], v234, s[20:23], 0 offen sc1\n"
      "v_mfma_f32_16x16x32_bf16 v[96:99], v[84:87], v[208:211], 0\n"
      "v_mfma_f32_16x16x32_bf16 v[100:103], v[84:87], v[212:215], 0\n"
      "v_cvt_pk_bf16_f32 v148, v104, v105\n"
      "v_cvt_pk_bf16_f32 v149, v106, v107\n"
      "v_cvt_pk_bf16_f32 v150, v108, v109\n"
      "v_cvt_pk_bf16_f32 v151, v110, v111\n"
      "v_add_u32 v235, 2048, v221\n"
      "buffer_store_dwordx4 v[148:151], v235, s[20:23], 0 offen sc1\n"
      "v_cvt_pk_bf16_f32 v152, v112, v113\n"
      "v_cvt_pk_bf16_f32 v153, v114, v115\n"
      "v_cvt_pk_bf16_f32 v154, v116, v117\n"
      "v_cvt_pk_bf16_f32 v155, v118, v119\n"
      "v_add_u32 v236, 3072, v221\n"
      "buffer_store_dwordx4 v[152:155], v236, s[20:23], 0 offen sc1\n"
      "v_cvt_pk_bf16_f32 v156, v96, v97\n"
      "v_cvt_pk_bf16_f32 v157, v98, v99\n"
      "v_cvt_pk_bf16_f32 v158, v100, v101\n"
      "v_cvt_pk_bf16_f32 v159, v102, v103\n"
      "v_add_u32 v237, 4096, v221\n"
      "buffer_store_dwordx4 v[156:159], v237, s[20:23], 0 offen sc1\n"
      "s_add_u32 m0, s14, 0\n"
      "s_add_u32 s80, s42, 21248\n"
      "buffer_load_dwordx4 v192, s[28:31], s80 offen offset:0 lds\n"
      "s_add_u32 m0, s14, 1024\n"
      "s_add_u32 s80, s42, 26368\n"
      "buffer_load_dwordx4 v192, s[28:31], s80 offen offset:0 lds\n"
      "s_add_u32 m0, s14, 2048\n"
      "s_add_u32 s80, s42, 31488\n"
      "buffer_load_dwordx4 v192, s[28:31], s80 offen offset:0 lds\n"
      "s_add_u32 m0, s14, 3072\n"
      "s_add_u32 s80, s42, 36608\n"
      "buffer_load_dwordx4 v192, s[28:31], s80 offen offset:0 lds\n"
      "s_add_u32 m0, s14, 4096\n"
      "s_add_u32 s80, s42, 1024\n"
      "buffer_load_dwordx4 v192, s[28:31], s80 offen offset:0 lds\n"
      "s_add_u32 m0, s14, 5120\n"
      "s_add_u32 s80, s42, 6144\n"
      "buffer_load_dwordx4 v192, s[28:31], s80 offen offset:0 lds\n"
      "s_waitcnt vmcnt(40)\n"
      "ds_read_b128 a[0:3], v194 offset:10240\n"
      "ds_read_b128 a[4:7], v195 offset:10240\n"
      "ds_read_b128 a[8:11], v196 offset:10240\n"
      "ds_read_b128 a[12:15], v197 offset:10240\n"
      "s_add_u32 m0, s14, 6144\n"
      "s_add_u32 s80, s42, 11264\n"
      "buffer_load_dwordx4 v192, s[28:31], s80 offen offset:0 lds\n"
      "s_add_u32 m0, s14, 7168\n"
      "s_add_u32 s80, s42, 16384\n"
      "buffer_load_dwordx4 v192, s[28:31], s80 offen offset:0 lds\n"
      "s_add_u32 m0, s14, 8192\n"
      "s_add_u32 s80, s42, 21504\n"
      "buffer_load_dwordx4 v192, s[28:31], s80 offen offset:0 lds\n"
      "s_add_u32 m0, s14, 9216\n"
      "s_add_u32 s80, s42, 26624\n"
      "buffer_load_dwordx4 v192, s[28:31], s80 offen offset:0 lds\n"
      "s_waitcnt vmcnt(40)\n"
      "ds_read_b128 a[80:83], v194 offset:14336\n"
      "ds_read_b128 a[84:87], v195 offset:14336\n"
      "ds_read_b128 a[88:91], v196 offset:14336\n"
      "ds_read_b128 a[92:95], v197 offset:14336\n"
      "s_waitcnt lgkmcnt(7)\n"
      "v_mfma_f32_16x16x32_bf16 v[160:163], a[0:3], v[176:179], 0\n"
      "v_mfma_f32_16x16x32_bf16 v[164:167], a[0:3], a[0:3], 0\n"
      "s_waitcnt lgkmcnt(6)\n"
      "v_mfma_f32_16x16x32_bf16 v[160:163], a[4:7], v[176:179], v[160:163]\n"
      "v_mfma_f32_16x16x32_bf16 v[164:167], a[4:7], a[4:7], v[164:167]\n"
      "s_waitcnt lgkmcnt(5)\n"
      "v_mfma_f32_16x16x32_bf16 v[160:163], a[8:11], v[176:179], v[160:163]\n"
      "v_mfma_f32_16x16x32_bf16 v[164:167], a[8:11], a[8:11], v[164:167]\n"
      "s_waitcnt lgkmcnt(4)\n"
      "v_mfma_f32_16x16x32_bf16 v[160:163], a[12:15], v[176:179], v[160:163]\n"
      "v_mfma_f32_16x16x32_bf16 v[164:167], a[12:15], a[12:15], v[164:167]\n"
      "s_add_u32 m0, s14, 10240\n"
      "s_add_u32 s80, s42, 31744\n"
      "buffer_load_dwordx4 v192, s[28:31], s80 offen offset:0 lds\n"
      "s_add_u32 m0, s14, 11264\n"
      "s_add_u32 s80, s42, 36864\n"
      "buffer_load_dwordx4 v192, s[28:31], s80 offen offset:0 lds\n"
      "buffer_load_dwordx4 v[180:183], v190, s[28:31], s46 offen\n"
      "buffer_load_dwordx4 v[184:187], v190, s[28:31], s46 offen offset:64\n"
      "buffer_load_dwordx2 v[188:189], v191, s[28:31], s46 offen offset:128\n"
      "s_cmp_lg_u32 s40, 0\n"
      "s_cbranch_scc1 Lw2_%=\n"
      "s_mov_b64 exec, 0x3f\n"
      "buffer_load_dwordx4 v[244:247], v218, s[20:23], 0 offen sc1\n"
      "s_mov_b64 exec, -1\n"
      "Lw2_%=:\n"
      "s_waitcnt vmcnt(41)\n"
      "ds_read_b128 a[16:19], v194 offset:18432\n"
      "ds_read_b128 a[20:23], v195 offset:18432\n"
      "ds_read_b128 a[24:27], v196 offset:18432\n"
      "ds_read_b128 a[28:31], v197 offset:18432\n"
      "s_waitcnt lgkmcnt(7)\n"
      "v_mfma_f32_16x16x32_bf16 v[168:171], a[80:83], v[176:179], 0\n"
      "v_mfma_f32_16x16x32_bf16 v[172:175], a[80:83], a[80:83], 0\n"
      "s_waitcnt lgkmcnt(6)\n"
      "v_mfma_f32_16x16x32_bf16 v[168:171], a[84:87], v[176:179], v[168:171]\n"
      "v_mfma_f32_16x16x32_bf16 v[172:175], a[84:87], a[84:87], v[172:175]\n"
      "s_waitcnt lgkmcnt(5)\n"
      "v_mfma_f32_16x16x32_bf16 v[168:171], a[88:91], v[176:179], v[168:171]\n"
      "v_mfma_f32_16x16x32_bf16 v[172:175], a[88:91], a[88:91], v[172:175]\n"
      "s_waitcnt lgkmcnt(4)\n"
      "v_mfma_f32_16x16x32_bf16 v[168:171], a[92:95], v[176:179], v[168:171]\n"
      "v_mfma_f32_16x16x32_bf16 v[172:175], a[92:95], a[92:95], v[172:175]\n"
      "s_waitcnt vmcnt(37)\n"
      "ds_read_b128 a[96:99], v194 offset:22528\n"
      "ds_read_b128 a[100:103], v195 offset:22528\n"
      "ds_read_b128 a[104:107], v196 offset:22528\n"
      "ds_read_b128 a[108:111], v197 offset:22528\n"
      "s_waitcnt lgkmcnt(7)\n"
      "v_mfma_f32_16x16x32_bf16 v[160:163], a[16:19], v[176:179], v[160:163]\n"
      "v_mfma_f32_16x16x32_bf16 v[164:167], a[16:19], a[16:19], v[164:167]\n"
      "s_waitcnt lgkmcnt(6)\n"
      "v_mfma_f32_16x16x32_bf16 v[160:163], a[20:23], v[176:179], v[160:163]\n"
      "v_mfma_f32_16x16x32_bf16 v[164:167], a[20:23], a[20:23], v[164:167]\n"
      "s_waitcnt lgkmcnt(5)\n"
      "v_mfma_f32_16x16x32_bf16 v[160:163], a[24:27], v[176:179], v[160:163]\n"
      "v_mfma_f32_16x16x32_bf16 v[164:167], a[24:27], a[24:27], v[164:167]\n"
      "s_waitcnt lgkmcnt(4)\n"
      "v_mfma_f32_16x16x32_bf16 v[160:163], a[28:31], v[176:179], v[160:163]\n"
      "v_mfma_f32_16x16x32_bf16 v[164:167], a[28:31], a[28:31], v[164:167]\n"
      "s_waitcnt vmcnt(33)\n"
      "ds_read_b128 a[32:35], v194 offset:26624\n"
      "ds_read_b128 a[36:39], v195 offset:26624\n"
      "ds_read_b128 a[40:43], v196 offset:26624\n"
      "ds_read_b128 a[44:47], v197 offset:26624\n"
      "s_waitcnt lgkmcnt(7)\n"
      "v_mfma_f32_16x16x32_bf16 v[168:171], a[96:99], v[176:179], v[168:171]\n"
      "v_mfma_f32_16x16x32_bf16 v[172:175], a[96:99], a[96:99], v[172:175]\n"
      "s_waitcnt lgkmcnt(6)\n"
      "v_mfma_f32_16x16x32_bf16 v[168:171], a[100:103], v[176:179], v[168:171]\n"
      "v_mfma_f32_16x16x32_bf16 v[172:175], a[100:103], a[100:103], v[172:175]\n"
      "s_waitcnt lgkmcnt(5)\n"
      "v_mfma_f32_16x16x32_bf16 v[168:171], a[104:107], v[176:179], v[168:171]\n"
      "v_mfma_f32_16x16x32_bf16 v[172:175], a[104:107], a[104:107], v[172:175]\n"
      "s_waitcnt lgkmcnt(4)\n"
      "v_mfma_f32_16x16x32_bf16 v[168:171], a[108:111], v[176:179], v[168:171]\n"
      "v_mfma_f32_16x16x32_bf16 v[172:175], a[108:111], a[108:111], v[172:175]\n"
      "s_waitcnt vmcnt(29)\n"
      "ds_read_b128 a[112:115], v194 offset:30720\n"
      "ds_read_b128 a[116:119], v195 offset:30720\n"
      "ds_read_b128 a[120:123], v196 offset:30720\n"
      "ds_read_b128 a[124:127], v197 offset:30720\n"
      "s_waitcnt lgkmcnt(7)\n"
      "v_mfma_f32_16x16x32_bf16 v[160:163], a[32:35], v[176:179], v[160:163]\n"
      "v_mfma_f32_16x16x32_bf16 v[164:167], a[32:35], a[32:35], v[164:167]\n"
      "s_waitcnt lgkmcnt(6)\n"
      "v_mfma_f32_16x16x32_bf16 v[160:163], a[36:39], v[176:179], v[160:163]\n"
      "v_mfma_f32_16x16x32_bf16 v[164:167], a[36:39], a[36:39], v[164:167]\n"
      "s_waitcnt lgkmcnt(5)\n"
      "v_mfma_f32_16x16x32_bf16 v[160:163], a[40:43], v[176:179], v[160:163]\n"
      "v_mfma_f32_16x16x32_bf16 v[164:167], a[40:43], a[40:43], v[164:167]\n"
      "s_waitcnt lgkmcnt(4)\n"
      "v_mfma_f32_16x16x32_bf16 v[160:163], a[44:47], v[176:179], v[160:163]\n"
      "v_mfma_f32_16x16x32_bf16 v[164:167], a[44:47], a[44:47], v[164:167]\n"
      "s_waitcnt vmcnt(25)\n"
      "ds_read_b128 a[48:51], v194 offset:34816\n"
      "ds_read_b128 a[52:55], v195 offset:34816\n"
      "ds_read_b128 a[56:59], v196 offset:34816\n"
      "ds_read_b128 a[60:63], v197 offset:34816\n"
      "s_waitcnt lgkmcnt(7)\n"
      "v_mfma_f32_16x16x32_bf16 v[168:171], a[112:115], v[176:179], v[168:171]\n"
      "v_mfma_f32_16x16x32_bf16 v[172:175], a[112:115], a[112:115], v[172:175]\n"
      "s_waitcnt lgkmcnt(6)\n"
      "v_mfma_f32_16x16x32_bf16 v[168:171], a[116:119], v[176:179], v[168:171]\n"
      "v_mfma_f32_16x16x32_bf16 v[172:175], a[116:119], a[116:119], v[172:175]\n"
      "s_waitcnt lgkmcnt(5)\n"
      "v_mfma_f32_16x16x32_bf16 v[168:171], a[120:123], v[176:179], v[168:171]\n"
      "v_mfma_f32_16x16x32_bf16 v[172:175], a[120:123], a[120:123], v[172:175]\n"
      "s_waitcnt lgkmcnt(4)\n"
      "v_mfma_f32_16x16x32_bf16 v[168:171], a[124:127], v[176:179], v[168:171]\n"
      "v_mfma_f32_16x16x32_bf16 v[172:175], a[124:127], a[124:127], v[172:175]\n"
      "s_waitcnt vmcnt(11)\n"
      "ds_read_b128 a[128:131], v194 offset:0\n"
      "ds_read_b128 a[132:135], v195 offset:0\n"
      "ds_read_b128 a[136:139], v196 offset:0\n"
      "ds_read_b128 a[140:143], v197 offset:0\n"
      "s_waitcnt lgkmcnt(7)\n"
      "v_mfma_f32_16x16x32_bf16 v[160:163], a[48:51], v[176:179], v[160:163]\n"
      "v_mfma_f32_16x16x32_bf16 v[164:167], a[48:51], a[48:51], v[164:167]\n"
      "s_waitcnt lgkmcnt(6)\n"
      "v_mfma_f32_16x16x32_bf16 v[160:163], a[52:55], v[176:179], v[160:163]\n"
      "v_mfma_f32_16x16x32_bf16 v[164:167], a[52:55], a[52:55], v[164:167]\n"
      "s_waitcnt lgkmcnt(5)\n"
      "v_mfma_f32_16x16x32_bf16 v[160:163], a[56:59], v[176:179], v[160:163]\n"
      "v_mfma_f32_16x16x32_bf16 v[164:167], a[56:59], a[56:59], v[164:167]\n"
      "s_waitcnt lgkmcnt(4)\n"
      "v_mfma_f32_16x16x32_bf16 v[160:163], a[60:63], v[176:179], v[160:163]\n"
      "v_mfma_f32_16x16x32_bf16 v[164:167], a[60:63], a[60:63], v[164:167]\n"
      "s_waitcnt vmcnt(7)\n"
      "ds_read_b128 a[64:67], v194 offset:4096\n"
      "ds_read_b128 a[68:71], v195 offset:4096\n"
      "ds_read_b128 a[72:75], v196 offset:4096\n"
      "ds_read_b128 a[76:79], v197 offset:4096\n"
      "s_waitcnt lgkmcnt(7)\n"
      "v_mfma_f32_16x16x32_bf16 v[168:171], a[128:131], v[176:179], v[168:171]\n"
      "v_mfma_f32_16x16x32_bf16 v[172:175], a[128:131], a[128:131], v[172:175]\n"
      "s_waitcnt lgkmcnt(6)\n"
      "v_mfma_f32_16x16x32_bf16 v[168:171], a[132:135], v[176:179], v[168:171]\n"
      "v_mfma_f32_16x16x32_bf16 v[172:175], a[132:135], a[132:135], v[172:175]\n"
      "s_waitcnt lgkmcnt(5)\n"
      "v_mfma_f32_16x16x32_bf16 v[168:171], a[136:139], v[176:179], v[168:171]\n"
      "v_mfma_f32_16x16x32_bf16 v[172:175], a[136:139], a[136:139], v[172:175]\n"
      "s_waitcnt lgkmcnt(4)\n"
      "v_mfma_f32_16x16x32_bf16 v[168:171], a[140:143], v[176:179], v[168:171]\n"
      "v_mfma_f32_16x16x32_bf16 v[172:175], a[140:143], a[140:143], v[172:175]\n"
      "s_waitcnt vmcnt(3)\n"
      "ds_read_b128 a[144:147], v194 offset:8192\n"
      "ds_read_b128 a[148:151], v195 offset:8192\n"
      "ds_read_b128 a[152:155], v196 offset:8192\n"
      "ds_read_b128 a[156:159], v197 offset:8192\n"
      "s_waitcnt lgkmcnt(0)\n"
      "s_cmp_lg_u32 s40, 0\n"
      "s_cbranch_scc1 Lz2_%=\n"
      "s_mov_b32 s84, 400000\n"
      "s_mov_b64 exec, 0x3f\n"
      "s_waitcnt vmcnt(0)\n"
      "Ly2_%=:\n"
      "v_sub_u32 v246, v246, v244\n"
      "v_sub_u32 v247, v247, v245\n"
      "v_xor_b32 v246, 0x7f4a7c15, v246\n"
      "v_xor_b32 v247, 0x9e3779b9, v247\n"
      "v_or_b32 v246, v246, v247\n"
      "v_cmp_ne_u32 vcc, 0, v246\n"
      "s_cbranch_vccz Lx2_%=\n"
      "buffer_load_dwordx4 v[244:247], v218, s[20:23], 0 offen sc1\n"
      "s_waitcnt vmcnt(0)\n"
      "s_sub_u32 s84, s84, 1\n"
      "s_cmp_lg_u32 s84, 0\n"
      "s_cbranch_scc1 Ly2_%=\n"
      "Lx2_%=:\n"
      "s_mov_b64 exec, -1\n"
      "Lz2_%=:\n"
      "s_barrier\n"
      "s_add_u32 s79, s65, 0\n"
      "s_add_u32 m0, s72, 0\n"
      "s_nop 0\n"
      "buffer_load_dwordx4 v0, s[20:23], s79 offen offset:0 sc1 lds\n"
      "buffer_load_dwordx4 v0, s[20:23], s79 offen offset:1024 sc1 lds\n"
      "buffer_load_dwordx4 v0, s[20:23], s79 offen offset:2048 sc1 lds\n"
      "buffer_load_dwordx4 v0, s[20:23], s79 offen offset:3072 sc1 lds\n"
      "s_add_u32 s79, s65, 4096\n"
      "s_add_u32 m0, s72, 4096\n"
      "s_nop 0\n"
      "buffer_load_dwordx4 v0, s[20:23], s79 offen offset:0 sc1 lds\n"
      "s_add_u32 s79, s65, 20480\n"
      "s_add_u32 m0, s72, 20480\n"
      "s_nop 0\n"
      "buffer_load_dwordx4 v0, s[20:23], s79 offen offset:0 sc1 lds\n"
      "buffer_load_dwordx4 v0, s[20:23], s79 offen offset:1024 sc1 lds\n"
      "buffer_load_dwordx4 v0, s[20:23], s79 offen offset:2048 sc1 lds\n"
      "buffer_load_dwordx4 v0, s[20:23], s79 offen offset:3072 sc1 lds\n"
      "s_add_u32 s79, s65, 24576\n"
      "s_add_u32 m0, s72, 24576\n"
      "s_nop 0\n"
      "buffer_load_dwordx4 v0, s[20:23], s79 offen offset:0 sc1 lds\n"
      "v_mfma_f32_16x16x32_bf16 v[160:163], a[64:67], v[176:179], v[160:163]\n"
      "v_mfma_f32_16x16x32_bf16 v[164:167], a[64:67], a[64:67], v[164:167]\n"
      "v_mfma_f32_16x16x32_bf16 v[160:163], a[68:71], v[176:179], v[160:163]\n"
      "v_mfma_f32_16x16x32_bf16 v[164:167], a[68:71], a[68:71], v[164:167]\n"
      "v_mfma_f32_16x16x32_bf16 v[160:163], a[72:75], v[176:179], v[160:163]\n"
      "v_mfma_f32_16x16x32_bf16 v[164:167], a[72:75], a[72:75], v[164:167]\n"
      "v_mfma_f32_16x16x32_bf16 v[160:163], a[76:79], v[176:179], v[160:163]\n"
      "v_mfma_f32_16x16x32_bf16 v[164:167], a[76:79], a[76:79], v[164:167]\n"
      "v_mfma_f32_16x16x32_bf16 v[168:171], a[144:147], v[176:179], v[168:171]\n"
      "v_mfma_f32_16x16x32_bf16 v[172:175], a[144:147], a[144:147], v[172:175]\n"
      "v_mfma_f32_16x16x32_bf16 v[168:171], a[148:151], v[176:179], v[168:171]\n"
      "v_mfma_f32_16x16x32_bf16 v[172:175], a[148:151], a[148:151], v[172:175]\n"
      "v_mfma_f32_16x16x32_bf16 v[168:171], a[152:155], v[176:179], v[168:171]\n"
      "v_mfma_f32_16x16x32_bf16 v[172:175], a[152:155], a[152:155], v[172:175]\n"
      "v_mfma_f32_16x16x32_bf16 v[168:171], a[156:159], v[176:179], v[168:171]\n"
      "v_mfma_f32_16x16x32_bf16 v[172:175], a[156:159], a[156:159], v[172:175]\n"
      "s_waitcnt vmcnt(12)\n"
      "v_lshlrev_b32 v12, 16, v180\n"
      "v_and_b32 v13, 0xffff0000, v180\n"
      "v_lshlrev_b32 v14, 16, v181\n"
      "v_and_b32 v15, 0xffff0000, v181\n"
      "v_lshlrev_b32 v16, 16, v182\n"
      "v_and_b32 v17, 0xffff0000, v182\n"
      "v_lshlrev_b32 v18, 16, v183\n"
      "v_and_b32 v19, 0xffff0000, v183\n"
      "s_waitcnt vmcnt(11)\n"
      "v_lshlrev_b32 v20, 16, v184\n"
      "v_and_b32 v21, 0xffff0000, v184\n"
      "v_lshlrev_b32 v22, 16, v185\n"
      "v_and_b32 v23, 0xffff0000, v185\n"
      "v_lshlrev_b32 v24, 16, v186\n"
      "v_and_b32 v25, 0xffff0000, v186\n"
      "v_lshlrev_b32 v26, 16, v187\n"
      "v_and_b32 v27, 0xffff0000, v187\n"
      "s_waitcnt vmcnt(10)\n"
      "v_lshlrev_b32 v28, 16, v188\n"
      "v_and_b32 v29, 0xffff0000, v188\n"
      "v_lshlrev_b32 v30, 16, v189\n"
      "v_and_b32 v31, 0xffff0000, v189\n"
      "s_mov_b32 s90, 0x22222222\n"
      "s_mov_b32 s91, 0x22222222\n"
      "s_mov_b32 s92, 0x44444444\n"
      "s_mov_b32 s93, 0x44444444\n"
      "s_mov_b32 s94, 0x88888888\n"
      "s_mov_b32 s95, 0x88888888\n"
      "s_lshl_b32 s76, s40, 7\n"
      "s_add_u32 s76, s76, 159744\n"
      "v_lshrrev_b32 v38, 2, v0\n"
      "v_and_b32 v38, 0x3c, v38\n"
      "v_add_u32 v38, s76, v38\n"
      "v_lshrrev_b32 v39, 4, v0\n"
      "v_and_b32 v39, 0x30, v39\n"
      "v_add_u32 v39, s76, v39\n"
      "v_cndmask_b32 v160, v160, v161, s[90:91]\n"
      "v_cndmask_b32 v164, v164, v165, s[90:91]\n"
      "v_cndmask_b32 v168, v168, v169, s[90:91]\n"
      "v_cndmask_b32 v172, v172, v173, s[90:91]\n"
      "v_cndmask_b32 v160, v160, v162, s[92:93]\n"
      "v_cndmask_b32 v164, v164, v166, s[92:93]\n"
      "v_cndmask_b32 v168, v168, v170, s[92:93]\n"
      "v_cndmask_b32 v172, v172, v174, s[92:93]\n"
      "v_cndmask_b32 v160, v160, v163, s[94:95]\n"
      "v_cndmask_b32 v164, v164, v167, s[94:95]\n"
      "v_cndmask_b32 v168, v168, v171, s[94:95]\n"
      "v_cndmask_b32 v172, v172, v175, s[94:95]\n"
      "v_mul_f32 v160, 0x3acccccd, v160\n"
      "v_mul_f32 v164, 0x3acccccd, v164\n"
      "v_mul_f32 v168, 0x3acccccd, v168\n"
      "v_mul_f32 v172, 0x3acccccd, v172\n"
      "v_fma_f32 v164, -v160, v160, v164\n"
      "v_fma_f32 v172, -v168, v168, v172\n"
      "v_max_f32 v164, 0, v164\n"
      "v_max_f32 v172, 0, v172\n"
      "v_add_f32 v164, 0x3727c5ac, v164\n"
      "v_add_f32 v172, 0x3727c5ac, v172\n"
      "v_rsq_f32 v164, v164\n"
      "v_rsq_f32 v172, v172\n"
      "v_mul_f32 v164, 0x3fb8aa3b, v164\n"
      "v_mul_f32 v172, 0x3fb8aa3b, v172\n"
      "s_mov_b32 exec_lo, 0x00f0000f\n"
      "s_mov_b32 exec_hi, 0xf0000f00\n"
      "ds_write_b32 v38, v164\n"
      "ds_write_b32 v38, v172 offset:64\n"
      "s_mov_b64 exec, -1\n"
      "s_waitcnt vmcnt(5)\n"
      "s_waitcnt lgkmcnt(0)\n"
      "LJk_%=:\n"
      "s_barrier\n"
      "s_cmp_lg_u32 s40, 0\n"
      "s_cbranch_scc1 Lsg1_%=\n"
      "s_add_u32 s80, s74, 15990784\n"
      "s_mov_b64 exec, 1\n"
      "buffer_atomic_add v10, v9, s[24:27], s80 offen\n"
      "s_mov_b64 exec, -1\n"
      "Lsg1_%=:\n"
      "ds_read_b128 v[108:111], v39\n"
      "ds_read_b128 v[112:115], v39 offset:64\n"
      "ds_read_b32 v116, v38\n"
      "ds_read_b32 v118, v38 offset:64\n"
      "ds_read_b128 v[84:87], v0 offset:0\n"
      "ds_read_b128 v[88:91], v0 offset:1024\n"
      "ds_read_b128 v[92:95], v0 offset:2048\n"
      "ds_read_b128 v[96:99], v0 offset:3072\n"
      "ds_read_b128 v[100:103], v0 offset:4096\n"
      "s_mov_b64 s[28:29], s[24:25]\n"
      "s_mov_b64 s[30:31], s[26:27]\n"
      "s_mov_b64 s[36:37], s[20:21]\n"
      "s_mov_b64 s[38:39], s[22:23]\n"
      "s_add_u32 s70, s45, 16449536\n"
      "s_waitcnt lgkmcnt(3)\n"
      "v_mfma_f32_16x16x32_bf16 v[44:47], a[0:3], v[84:87], 0\n"
      "ds_read_b128 v[104:107], v0 offset:5120\n"
      "v_mfma_f32_16x16x32_bf16 v[48:51], a[80:83], v[84:87], 0\n"
      "v_mfma_f32_16x16x32_bf16 v[52:55], a[0:3], v[88:91], 0\n"
      "ds_read_b128 v[84:87], v0 offset:6144\n"
      "v_mfma_f32_16x16x32_bf16 v[56:59], a[80:83], v[88:91], 0\n"
      "s_waitcnt lgkmcnt(3)\n"
      "v_mfma_f32_16x16x32_bf16 v[60:63], a[0:3], v[92:95], 0\n"
      "ds_read_b128 v[88:91], v0 offset:7168\n"
      "s_add_u32 s79, s65, 40960\n"
      "s_add_u32 m0, s72, 40960\n"
      "s_nop 0\n"
      "buffer_load_dwordx4 v0, s[20:23], s79 offen offset:0 sc1 lds\n"
      "v_mfma_f32_16x16x32_bf16 v[64:67], a[80:83], v[92:95], 0\n"
      "v_mfma_f32_16x16x32_bf16 v[68:71], a[0:3], v[96:99], 0\n"
      "ds_read_b128 v[92:95], v0 offset:8192\n"
      "v_mfma_f32_16x16x32_bf16 v[72:75], a[80:83], v[96:99], 0\n"
      "s_waitcnt lgkmcnt(3)\n"
      "v_mfma_f32_16x16x32_bf16 v[76:79], a[0:3], v[100:103], 0\n"
      "ds_read_b128 v[96:99], v0 offset:9216\n"
      "v_mfma_f32_16x16x32_bf16 v[80:83], a[80:83], v[100:103], 0\n"
      "buffer_load_dwordx4 v0, s[20:23], s79 offen offset:1024 sc1 lds\n"
      "v_mfma_f32_16x16x32_bf16 v[44:47], a[4:7], v[104:107], v[44:47]\n"
      "ds_read_b128 v[100:103], v0 offset:10240\n"
      "v_mfma_f32_16x16x32_bf16 v[48:51], a[84:87], v[104:107], v[48:51]\n"
      "s_waitcnt lgkmcnt(3)\n"
      "v_mfma_f32_16x16x32_bf16 v[52:55], a[4:7], v[84:87], v[52:55]\n"
      "ds_read_b128 v[104:107], v0 offset:11264\n"
      "v_mfma_f32_16x16x32_bf16 v[56:59], a[84:87], v[84:87], v[56:59]\n"
      "buffer_load_dwordx4 v0, s[20:23], s79 offen offset:2048 sc1 lds\n"
      "v_mfma_f32_16x16x32_bf16 v[60:63], a[4:7], v[88:91], v[60:63]\n"
      "ds_read_b128 v[84:87], v0 offset:12288\n"
      "v_mfma_f32_16x16x32_bf16 v[64:67], a[84:87], v[88:91], v[64:67]\n"
      "s_waitcnt lgkmcnt(3)\n"
      "v_mfma_f32_16x16x32_bf16 v[68:71], a[4:7], v[92:95], v[68:71]\n"
      "ds_read_b128 v[88:91], v0 offset:13312\n"
      "v_mfma_f32_16x16x32_bf16 v[72:75], a[84:87], v[92:95], v[72:75]\n"
      "v_mfma_f32_16x16x32_bf16 v[76:79], a[4:7], v[96:99], v[76:79]\n"
      "ds_read_b128 v[92:95], v0 offset:14336\n"
      "buffer_load_dwordx4 v0, s[20:23], s79 offen offset:3072 sc1 lds\n"
      "v_mfma_f32_16x16x32_bf16 v[80:83], a[84:87], v[96:99], v[80:83]\n"
      "s_waitcnt lgkmcnt(3)\n"
      "v_mfma_f32_16x16x32_bf16 v[44:47], a[8:11], v[100:103], v[44:47]\n"
      "ds_read_b128 v[96:99], v0 offset:15360\n"
      "v_mfma_f32_16x16x32_bf16 v[48:51], a[88:91], v[100:103], v[48:51]\n"
      "v_mfma_f32_16x16x32_bf16 v[52:55], a[8:11], v[104:107], v[52:55]\n"
      "ds_read_b128 v[100:103], v0 offset:16384\n"
      "s_add_u32 s79, s65, 45056\n"
      "s_add_u32 m0, s72, 45056\n"
      "s_nop 0\n"
      "buffer_load_dwordx4 v0, s[20:23], s79 offen offset:0 sc1 lds\n"
      "v_mfma_f32_16x16x32_bf16 v[56:59], a[88:91], v[104:107], v[56:59]\n"
      "s_waitcnt lgkmcnt(3)\n"
      "v_mfma_f32_16x16x32_bf16 v[60:63], a[8:11], v[84:87], v[60:63]\n"
      "ds_read_b128 v[104:107], v0 offset:17408\n"
      "v_mfma_f32_16x16x32_bf16 v[64:67], a[88:91], v[84:87], v[64:67]\n"
      "v_mfma_f32_16x16x32_bf16 v[68:71], a[8:11], v[88:91], v[68:71]\n"
      "ds_read_b128 v[84:87], v0 offset:18432\n"
      "v_mfma_f32_16x16x32_bf16 v[72:75], a[88:91], v[88:91], v[72:75]\n"
      "s_add_u32 s79, s65, 61440\n"
      "s_add_u32 m0, s72, 61440\n"
      "s_nop 0\n"
      "buffer_load_dwordx4 v0, s[20:23], s79 offen offset:0 sc1 lds\n"
      "s_waitcnt lgkmcnt(3)\n"
      "v_mfma_f32_16x16x32_bf16 v[76:79], a[8:11], v[92:95], v[76:79]\n"
      "ds_read_b128 v[88:91], v0 offset:19456\n"
      "s_waitcnt vmcnt(6)\n"
      "s_barrier\n"
      "v_mfma_f32_16x16x32_bf16 v[80:83], a[88:91], v[92:95], v[80:83]\n"
      "v_mfma_f32_16x16x32_bf16 v[44:47], a[12:15], v[96:99], v[44:47]\n"
      "ds_read_b128 v[92:95], v0 offset:20480\n"
      "v_mfma_f32_16x16x32_bf16 v[48:51], a[92:95], v[96:99], v[48:51]\n"
      "buffer_load_dwordx4 v0, s[20:23], s79 offen offset:1024 sc1 lds\n"
      "s_waitcnt lgkmcnt(3)\n"
      "v_mfma_f32_16x16x32_bf16 v[52:55], a[12:15], v[100:103], v[52:55]\n"
      "ds_read_b128 v[96:99], v0 offset:21504\n"
      "v_mfma_f32_16x16x32_bf16 v[56:59], a[92:95], v[100:103], v[56:59]\n"
      "v_mfma_f32_16x16x32_bf16 v[60:63], a[12:15], v[104:107], v[60:63]\n"
      "ds_read_b128 v[100:103], v0 offset:22528\n"
      "v_mfma_f32_16x16x32_bf16 v[64:67], a[92:95], v[104:107], v[64:67]\n"
      "s_waitcnt lgkmcnt(3)\n"
      "v_mfma_f32_16x16x32_bf16 v[68:71], a[12:15], v[84:87], v[68:71]\n"
      "ds_read_b128 v[104:107], v0 offset:23552\n"
      "buffer_load_dwordx4 v0, s[20:23], s79 offen offset:2048 sc1 lds\n"
      "v_mfma_f32_16x16x32_bf16 v[72:75], a[92:95], v[84:87], v[72:75]\n"
      "v_mfma_f32_16x16x32_bf16 v[76:79], a[12:15], v[88:91], v[76:79]\n"
      "ds_read_b128 v[84:87], v0 offset:24576\n"
      "v_mfma_f32_16x16x32_bf16 v[80:83], a[92:95], v[88:91], v[80:83]\n"
      "s_waitcnt lgkmcnt(3)\n"
      "v_mfma_f32_16x16x32_bf16 v[44:47], a[16:19], v[92:95], v[44:47]\n"
      "ds_read_b128 v[88:91], v0 offset:25600\n"
      "buffer_load_dwordx4 v0, s[20:23], s79 offen offset:3072 sc1 lds\n"
      "v_mfma_f32_16x16x32_bf16 v[48:51], a[96:99], v[92:95], v[48:51]\n"
      "v_mfma_f32_16x16x32_bf16 v[52:55], a[16:19], v[96:99], v[52:55]\n"
      "ds_read_b128 v[92:95], v0 offset:26624\n"
      "v_mfma_f32_16x16x32_bf16 v[56:59], a[96:99], v[96:99], v[56:59]\n"
      "s_waitcnt lgkmcnt(3)\n"
      "v_mfma_f32_16x16x32_bf16 v[60:63], a[16:19], v[100:103], v[60:63]\n"
      "ds_read_b128 v[96:99], v0 offset:27648\n"
      "v_mfma_f32_16x16x32_bf16 v[64:67], a[96:99], v[100:103], v[64:67]\n"
      "s_add_u32 s79, s65, 65536\n"
      "s_add_u32 m0, s72, 65536\n"
      "s_nop 0\n"
      "buffer_load_dwordx4 v0, s[20:23], s79 offen offset:0 sc1 lds\n"
      "v_mfma_f32_16x16x32_bf16 v[68:71], a[16:19], v[104:107], v[68:71]\n"
      "ds_read_b128 v[100:103], v0 offset:28672\n"
      "v_mfma_f32_16x16x32_bf16 v[72:75], a[96:99], v[104:107], v[72:75]\n"
      "s_waitcnt lgkmcnt(3)\n"
      "v_mfma_f32_16x16x32_bf16 v[76:79], a[16:19], v[84:87], v[76:79]\n"
      "ds_read_b128 v[104:107], v0 offset:29696\n"
      "v_mfma_f32_16x16x32_bf16 v[80:83], a[96:99], v[84:87], v[80:83]\n"
      "s_add_u32 s79, s65, 81920\n"
      "s_add_u32 m0, s72, 81920\n"
      "s_nop 0\n"
      "buffer_load_dwordx4 v0, s[20:23], s79 offen offset:0 sc1 lds\n"
      "v_mfma_f32_16x16x32_bf16 v[44:47], a[20:23], v[88:91], v[44:47]\n"
      "ds_read_b128 v[84:87], v0 offset:30720\n"
      "v_mfma_f32_16x16x32_bf16 v[48:51], a[100:103], v[88:91], v[48:51]\n"
      "s_waitcnt lgkmcnt(3)\n"
      "v_mfma_f32_16x16x32_bf16 v[52:55], a[20:23], v[92:95], v[52:55]\n"
      "ds_read_b128 v[88:91], v0 offset:31744\n"
      "v_mfma_f32_16x16x32_bf16 v[56:59], a[100:103], v[92:95], v[56:59]\n"
      "v_mfma_f32_16x16x32_bf16 v[60:63], a[20:23], v[96:99], v[60:63]\n"
      "ds_read_b128 v[92:95], v0 offset:32768\n"
      "buffer_load_dwordx4 v0, s[20:23], s79 offen offset:1024 sc1 lds\n"
      "v_mfma_f32_16x16x32_bf16 v[64:67], a[100:103], v[96:99], v[64:67]\n"
      "s_waitcnt lgkmcnt(3)\n"
      "v_mfma_f32_16x16x32_bf16 v[68:71], a[20:23], v[100:103], v[68:71]\n"
      "ds_read_b128 v[96:99], v0 offset:33792\n"
      "v_mfma_f32_16x16x32_bf16 v[72:75], a[100:103], v[100:103], v[72:75]\n"
      "v_mfma_f32_16x16x32_bf16 v[76:79], a[20:23], v[104:107], v[76:79]\n"
      "ds_read_b128 v[100:103], v0 offset:34816\n"
      "v_mfma_f32_16x16x32_bf16 v[80:83], a[100:103], v[104:107], v[80:83]\n"
      "buffer_load_dwordx4 v0, s[20:23], s79 offen offset:2048 sc1 lds\n"
      "s_waitcnt lgkmcnt(3)\n"
      "v_mfma_f32_16x16x32_bf16 v[44:47], a[24:27], v[84:87], v[44:47]\n"
      "ds_read_b128 v[104:107], v0 offset:35840\n"
      "v_mfma_f32_16x16x32_bf16 v[48:51], a[104:107], v[84:87], v[48:51]\n"
      "v_mfma_f32_16x16x32_bf16 v[52:55], a[24:27], v[88:91], v[52:55]\n"
      "ds_read_b128 v[84:87], v0 offset:36864\n"
      "v_mfma_f32_16x16x32_bf16 v[56:59], a[104:107], v[88:91], v[56:59]\n"
      "buffer_load_dwordx4 v0, s[20:23], s79 offen offset:3072 sc1 lds\n"
      "s_waitcnt lgkmcnt(3)\n"
      "v_mfma_f32_16x16x32_bf16 v[60:63], a[24:27], v[92:95], v[60:63]\n"
      "ds_read_b128 v[88:91], v0 offset:37888\n"
      "v_mfma_f32_16x16x32_bf16 v[64:67], a[104:107], v[92:95], v[64:67]\n"
      "v_mfma_f32_16x16x32_bf16 v[68:71], a[24:27], v[96:99], v[68:71]\n"
      "ds_read_b128 v[92:95], v0 offset:38912\n"
      "v_mfma_f32_16x16x32_bf16 v[72:75], a[104:107], v[96:99], v[72:75]\n"
      "s_waitcnt lgkmcnt(3)\n"
      "v_mfma_f32_16x16x32_bf16 v[76:79], a[24:27], v[100:103], v[76:79]\n"
      "ds_read_b128 v[96:99], v0 offset:39936\n"
      "s_add_u32 s79, s65, 86016\n"
      "s_add_u32 m0, s72, 86016\n"
      "s_nop 0\n"
      "buffer_load_dwordx4 v0, s[20:23], s79 offen offset:0 sc1 lds\n"
      "s_waitcnt vmcnt(10)\n"
      "s_barrier\n"
      "v_mfma_f32_16x16x32_bf16 v[80:83], a[104:107], v[100:103], v[80:83]\n"
      "v_mfma_f32_16x16x32_bf16 v[44:47], a[28:31], v[104:107], v[44:47]\n"
      "ds_read_b128 v[100:103], v0 offset:40960\n"
      "v_mfma_f32_16x16x32_bf16 v[48:51], a[108:111], v[104:107], v[48:51]\n"
      "s_waitcnt lgkmcnt(3)\n"
      "v_mfma_f32_16x16x32_bf16 v[52:55], a[28:31], v[84:87], v[52:55]\n"
      "ds_read_b128 v[104:107], v0 offset:41984\n"
      "s_add_u32 s79, s66, 102400\n"
      "s_add_u32 m0, s71, 102400\n"
      "s_nop 0\n"
      "buffer_load_dwordx4 v0, s[20:23], s79 offen offset:0 sc1 lds\n"
      "v_mfma_f32_16x16x32_bf16 v[56:59], a[108:111], v[84:87], v[56:59]\n"
      "v_mfma_f32_16x16x32_bf16 v[60:63], a[28:31], v[88:91], v[60:63]\n"
      "ds_read_b128 v[84:87], v0 offset:43008\n"
      "v_mfma_f32_16x16x32_bf16 v[64:67], a[108:111], v[88:91], v[64:67]\n"
      "s_waitcnt lgkmcnt(3)\n"
      "v_mfma_f32_16x16x32_bf16 v[68:71], a[28:31], v[92:95], v[68:71]\n"
      "ds_read_b128 v[88:91], v0 offset:44032\n"
      "v_mfma_f32_16x16x32_bf16 v[72:75], a[108:111], v[92:95], v[72:75]\n"
      "buffer_load_dwordx4 v0, s[20:23], s79 offen offset:1024 sc1 lds\n"
      "v_mfma_f32_16x16x32_bf16 v[76:79], a[28:31], v[96:99], v[76:79]\n"
      "ds_read_b128 v[92:95], v0 offset:45056\n"
      "v_mfma_f32_16x16x32_bf16 v[80:83], a[108:111], v[96:99], v[80:83]\n"
      "s_waitcnt lgkmcnt(3)\n"
      "v_mfma_f32_16x16x32_bf16 v[44:47], a[32:35], v[100:103], v[44:47]\n"
      "ds_read_b128 v[96:99], v0 offset:46080\n"
      "v_mfma_f32_16x16x32_bf16 v[48:51], a[112:115], v[100:103], v[48:51]\n"
      "buffer_load_dwordx4 v0, s[20:23], s79 offen offset:2048 sc1 lds\n"
      "v_mfma_f32_16x16x32_bf16 v[52:55], a[32:35], v[104:107], v[52:55]\n"
      "ds_read_b128 v[100:103], v0 offset:47104\n"
      "v_mfma_f32_16x16x32_bf16 v[56:59], a[112:115], v[104:107], v[56:59]\n"
      "s_waitcnt lgkmcnt(3)\n"
      "v_mfma_f32_16x16x32_bf16 v[60:63], a[32:35], v[84:87], v[60:63]\n"
      "ds_read_b128 v[104:107], v0 offset:48128\n"
      "v_mfma_f32_16x16x32_bf16 v[64:67], a[112:115], v[84:87], v[64:67]\n"
      "v_mfma_f32_16x16x32_bf16 v[68:71], a[32:35], v[88:91], v[68:71]\n"
      "ds_read_b128 v[84:87], v0 offset:49152\n"
      "buffer_load_dwordx4 v0, s[20:23], s79 offen offset:3072 sc1 lds\n"
      "v_mfma_f32_16x16x32_bf16 v[72:75], a[112:115], v[88:91], v[72:75]\n"
      "s_waitcnt lgkmcnt(3)\n"
      "v_mfma_f32_16x16x32_bf16 v[76:79], a[32:35], v[92:95], v[76:79]\n"
      "ds_read_b128 v[88:91], v0 offset:50176\n"
      "v_mfma_f32_16x16x32_bf16 v[80:83], a[112:115], v[92:95], v[80:83]\n"
      "v_mfma_f32_16x16x32_bf16 v[44:47], a[36:39], v[96:99], v[44:47]\n"
      "ds_read_b128 v[92:95], v0 offset:51200\n"
      "s_add_u32 s79, s66, 106496\n"
      "s_add_u32 m0, s71, 106496\n"
      "s_nop 0\n"
      "buffer_load_dwordx4 v0, s[20:23], s79 offen offset:0 sc1 lds\n"
      "v_mfma_f32_16x16x32_bf16 v[48:51], a[116:119], v[96:99], v[48:51]\n"
      "s_waitcnt lgkmcnt(3)\n"
      "v_mfma_f32_16x16x32_bf16 v[52:55], a[36:39], v[100:103], v[52:55]\n"
      "ds_read_b128 v[96:99], v0 offset:52224\n"
      "v_mfma_f32_16x16x32_bf16 v[56:59], a[116:119], v[100:103], v[56:59]\n"
      "v_mfma_f32_16x16x32_bf16 v[60:63], a[36:39], v[104:107], v[60:63]\n"
      "ds_read_b128 v[100:103], v0 offset:53248\n"
      "v_mfma_f32_16x16x32_bf16 v[64:67], a[116:119], v[104:107], v[64:67]\n"
      "buffer_load_dwordx4 v0, s[20:23], s79 offen offset:1024 sc1 lds\n"
      "s_waitcnt lgkmcnt(3)\n"
      "v_mfma_f32_16x16x32_bf16 v[68:71], a[36:39], v[84:87], v[68:71]\n"
      "ds_read_b128 v[104:107], v0 offset:54272\n"
      "v_mfma_f32_16x16x32_bf16 v[72:75], a[116:119], v[84:87], v[72:75]\n"
      "v_mfma_f32_16x16x32_bf16 v[76:79], a[36:39], v[88:91], v[76:79]\n"
      "ds_read_b128 v[84:87], v0 offset:55296\n"
      "v_mfma_f32_16x16x32_bf16 v[80:83], a[116:119], v[88:91], v[80:83]\n"
      "buffer_load_dwordx4 v0, s[20:23], s79 offen offset:2048 sc1 lds\n"
      "s_waitcnt lgkmcnt(3)\n"
      "v_mfma_f32_16x16x32_bf16 v[44:47], a[40:43], v[92:95], v[44:47]\n"
      "ds_read_b128 v[88:91], v0 offset:56320\n"
      "v_mfma_f32_16x16x32_bf16 v[48:51], a[120:123], v[92:95], v[48:51]\n"
      "v_mfma_f32_16x16x32_bf16 v[52:55], a[40:43], v[96:99], v[52:55]\n"
      "ds_read_b128 v[92:95], v0 offset:57344\n"
      "v_mfma_f32_16x16x32_bf16 v[56:59], a[120:123], v[96:99], v[56:59]\n"
      "s_waitcnt lgkmcnt(3)\n"
      "v_mfma_f32_16x16x32_bf16 v[60:63], a[40:43], v[100:103], v[60:63]\n"
      "ds_read_b128 v[96:99], v0 offset:58368\n"
      "buffer_load_dwordx4 v0, s[20:23], s79 offen offset:3072 sc1 lds\n"
      "v_mfma_f32_16x16x32_bf16 v[64:67], a[120:123], v[100:103], v[64:67]\n"
      "v_mfma_f32_16x16x32_bf16 v[68:71], a[40:43], v[104:107], v[68:71]\n"
      "ds_read_b128 v[100:103], v0 offset:59392\n"
      "v_mfma_f32_16x16x32_bf16 v[72:75], a[120:123], v[104:107], v[72:75]\n"
      "s_waitcnt lgkmcnt(3)\n"
      "v_mfma_f32_16x16x32_bf16 v[76:79], a[40:43], v[84:87], v[76:79]\n"
      "ds_read_b128 v[104:107], v0 offset:60416\n"
      "s_waitcnt vmcnt(8)\n"
      "s_barrier\n"
      "v_mfma_f32_16x16x32_bf16 v[80:83], a[120:123], v[84:87], v[80:83]\n"
      "s_add_u32 s79, s66, 110592\n"
      "s_add_u32 m0, s71, 110592\n"
      "s_nop 0\n"
      "buffer_load_dwordx4 v0, s[20:23], s79 offen offset:0 sc1 lds\n"
      "v_mfma_f32_16x16x32_bf16 v[44:47], a[44:47], v[88:91], v[44:47]\n"
      "ds_read_b128 v[84:87], v0 offset:61440\n"
      "v_mfma_f32_16x16x32_bf16 v[48:51], a[124:127], v[88:91], v[48:51]\n"
      "s_waitcnt lgkmcnt(3)\n"
      "v_mfma_f32_16x16x32_bf16 v[52:55], a[44:47], v[92:95], v[52:55]\n"
      "ds_read_b128 v[88:91], v0 offset:62464\n"
      "v_mfma_f32_16x16x32_bf16 v[56:59], a[124:127], v[92:95], v[56:59]\n"
      "buffer_load_dwordx4 v0, s[20:23], s79 offen offset:1024 sc1 lds\n"
      "v_mfma_f32_16x16x32_bf16 v[60:63], a[44:47], v[96:99], v[60:63]\n"
      "ds_read_b128 v[92:95], v0 offset:63488\n"
      "s_or_b32 s98, s40, s41\n"
      "s_cbranch_scc1 Lc2t_%=\n"
      "v_bfe_u32 v193, v0, 4, 4\n"
      "v_bfe_u32 v194, v0, 6, 2\n"
      "v_add_lshl_u32 v193, v193, v194, 7\n"
      "s_mov_b32 s98, 26279936\n"
      "buffer_load_dwordx4 v[194:197], v193, s[20:23], s98 offen sc1\n"
      "Lc2t_%=:\n"
      "v_mfma_f32_16x16x32_bf16 v[64:67], a[124:127], v[96:99], v[64:67]\n"
      "s_waitcnt lgkmcnt(3)\n"
      "v_mfma_f32_16x16x32_bf16 v[68:71], a[44:47], v[100:103], v[68:71]\n"
      "ds_read_b128 v[96:99], v0 offset:64512\n"
      "v_mfma_f32_16x16x32_bf16 v[72:75], a[124:127], v[100:103], v[72:75]\n"
      "v_mfma_f32_16x16x32_bf16 v[76:79], a[44:47], v[104:107], v[76:79]\n"
      "ds_read_b128 v[100:103], v7 offset:0\n"
      "buffer_load_dwordx4 v0, s[20:23], s79 offen offset:2048 sc1 lds\n"
      "v_mfma_f32_16x16x32_bf16 v[80:83], a[124:127], v[104:107], v[80:83]\n"
      "s_waitcnt lgkmcnt(3)\n"
      "v_mfma_f32_16x16x32_bf16 v[44:47], a[48:51], v[84:87], v[44:47]\n"
      "ds_read_b128 v[104:107], v7 offset:1024\n"
      "v_mfma_f32_16x16x32_bf16 v[48:51], a[128:131], v[84:87], v[48:51]\n"
      "v_mfma_f32_16x16x32_bf16 v[52:55], a[48:51], v[88:91], v[52:55]\n"
      "ds_read_b128 v[84:87], v7 offset:2048\n"
      "buffer_load_dwordx4 v0, s[20:23], s79 offen offset:3072 sc1 lds\n"
      "v_mfma_f32_16x16x32_bf16 v[56:59], a[128:131], v[88:91], v[56:59]\n"
      "s_waitcnt lgkmcnt(3)\n"
      "v_mfma_f32_16x16x32_bf16 v[60:63], a[48:51], v[92:95], v[60:63]\n"
      "ds_read_b128 v[88:91], v7 offset:3072\n"
      "v_mfma_f32_16x16x32_bf16 v[64:67], a[128:131], v[92:95], v[64:67]\n"
      "v_mfma_f32_16x16x32_bf16 v[68:71], a[48:51], v[96:99], v[68:71]\n"
      "ds_read_b128 v[92:95], v7 offset:4096\n"
      "v_mfma_f32_16x16x32_bf16 v[72:75], a[128:131], v[96:99], v[72:75]\n"
      "s_add_u32 s79, s66, 114688\n"
      "s_add_u32 m0, s71, 114688\n"
      "s_nop 0\n"
      "buffer_load_dwordx4 v0, s[20:23], s79 offen offset:0 sc1 lds\n"
      "s_waitcnt lgkmcnt(3)\n"
      "v_mfma_f32_16x16x32_bf16 v[76:79], a[48:51], v[100:103], v[76:79]\n"
      "ds_read_b128 v[96:99], v7 offset:5120\n"
      "v_mfma_f32_16x16x32_bf16 v[80:83], a[128:131], v[100:103], v[80:83]\n"
      "v_mfma_f32_16x16x32_bf16 v[44:47], a[52:55], v[104:107], v[44:47]\n"
      "ds_read_b128 v[100:103], v7 offset:6144\n"
      "v_mfma_f32_16x16x32_bf16 v[48:51], a[132:135], v[104:107], v[48:51]\n"
      "s_waitcnt lgkmcnt(3)\n"
      "v_mfma_f32_16x16x32_bf16 v[52:55], a[52:55], v[84:87], v[52:55]\n"
      "ds_read_b128 v[104:107], v7 offset:7168\n"
      "v_mfma_f32_16x16x32_bf16 v[56:59], a[132:135], v[84:87], v[56:59]\n"
      "v_mfma_f32_16x16x32_bf16 v[60:63], a[52:55], v[88:91], v[60:63]\n"
      "ds_read_b128 v[84:87], v7 offset:8192\n"
      "v_mfma_f32_16x16x32_bf16 v[64:67], a[132:135], v[88:91], v[64:67]\n"
      "s_waitcnt lgkmcnt(3)\n"
      "v_mfma_f32_16x16x32_bf16 v[68:71], a[52:55], v[92:95], v[68:71]\n"
      "ds_read_b128 v[88:91], v7 offset:9216\n"
      "v_mfma_f32_16x16x32_bf16 v[72:75], a[132:135], v[92:95], v[72:75]\n"
      "v_mfma_f32_16x16x32_bf16 v[76:79], a[52:55], v[96:99], v[76:79]\n"
      "ds_read_b128 v[92:95], v7 offset:10240\n"
      "v_mfma_f32_16x16x32_bf16 v[80:83], a[132:135], v[96:99], v[80:83]\n"
      "s_waitcnt lgkmcnt(3)\n"
      "v_mfma_f32_16x16x32_bf16 v[44:47], a[56:59], v[100:103], v[44:47]\n"
      "ds_read_b128 v[96:99], v7 offset:11264\n"
      "v_mfma_f32_16x16x32_bf16 v[48:51], a[136:139], v[100:103], v[48:51]\n"
      "v_mfma_f32_16x16x32_bf16 v[52:55], a[56:59], v[104:107], v[52:55]\n"
      "ds_read_b128 v[100:103], v7 offset:12288\n"
      "v_mfma_f32_16x16x32_bf16 v[56:59], a[136:139], v[104:107], v[56:59]\n"
      "s_waitcnt lgkmcnt(3)\n"
      "v_mfma_f32_16x16x32_bf16 v[60:63], a[56:59], v[84:87], v[60:63]\n"
      "ds_read_b128 v[104:107], v7 offset:13312\n"
      "v_mfma_f32_16x16x32_bf16 v[64:67], a[136:139], v[84:87], v[64:67]\n"
      "v_mfma_f32_16x16x32_bf16 v[68:71], a[56:59], v[88:91], v[68:71]\n"
      "ds_read_b128 v[84:87], v7 offset:14336\n"
      "v_mfma_f32_16x16x32_bf16 v[72:75], a[136:139], v[88:91], v[72:75]\n"
      "s_waitcnt lgkmcnt(3)\n"
      "v_mfma_f32_16x16x32_bf16 v[76:79], a[56:59], v[92:95], v[76:79]\n"
      "ds_read_b128 v[88:91], v7 offset:15360\n"
      "v_mfma_f32_16x16x32_bf16 v[80:83], a[136:139], v[92:95], v[80:83]\n"
      "v_mfma_f32_16x16x32_bf16 v[44:47], a[60:63], v[96:99], v[44:47]\n"
      "ds_read_b128 v[92:95], v7 offset:16384\n"
      "v_mfma_f32_16x16x32_bf16 v[48:51], a[140:143], v[96:99], v[48:51]\n"
      "s_waitcnt lgkmcnt(3)\n"
      "v_mfma_f32_16x16x32_bf16 v[52:55], a[60:63], v[100:103], v[52:55]\n"
      "ds_read_b128 v[96:99], v7 offset:17408\n"
      "v_mfma_f32_16x16x32_bf16 v[56:59], a[140:143], v[100:103], v[56:59]\n"
      "v_mfma_f32_16x16x32_bf16 v[60:63], a[60:63], v[104:107], v[60:63]\n"
      "ds_read_b128 v[100:103], v7 offset:18432\n"
      "v_mfma_f32_16x16x32_bf16 v[64:67], a[140:143], v[104:107], v[64:67]\n"
      "s_waitcnt lgkmcnt(3)\n"
      "v_mfma_f32_16x16x32_bf16 v[68:71], a[60:63], v[84:87], v[68:71]\n"
      "ds_read_b128 v[104:107], v7 offset:19456\n"
      "v_mfma_f32_16x16x32_bf16 v[72:75], a[140:143], v[84:87], v[72:75]\n"
      "v_mfma_f32_16x16x32_bf16 v[76:79], a[60:63], v[88:91], v[76:79]\n"
      "ds_read_b128 v[84:87], v7 offset:20480\n"
      "v_mfma_f32_16x16x32_bf16 v[80:83], a[140:143], v[88:91], v[80:83]\n"
      "s_waitcnt lgkmcnt(3)\n"
      "v_mfma_f32_16x16x32_bf16 v[44:47], a[64:67], v[92:95], v[44:47]\n"
      "ds_read_b128 v[88:91], v7 offset:21504\n"
      "v_mfma_f32_16x16x32_bf16 v[48:51], a[144:147], v[92:95], v[48:51]\n"
      "v_mfma_f32_16x16x32_bf16 v[52:55], a[64:67], v[96:99], v[52:55]\n"
      "ds_read_b128 v[92:95], v7 offset:22528\n"
      "v_mfma_f32_16x16x32_bf16 v[56:59], a[144:147], v[96:99], v[56:59]\n"
      "s_waitcnt lgkmcnt(3)\n"
      "v_mfma_f32_16x16x32_bf16 v[60:63], a[64:67], v[100:103], v[60:63]\n"
      "ds_read_b128 v[96:99], v7 offset:23552\n"
      "v_mfma_f32_16x16x32_bf16 v[64:67], a[144:147], v[100:103], v[64:67]\n"
      "v_mfma_f32_16x16x32_bf16 v[68:71], a[64:67], v[104:107], v[68:71]\n"
      "ds_read_b128 v[100:103], v7 offset:24576\n"
      "v_mfma_f32_16x16x32_bf16 v[72:75], a[144:147], v[104:107], v[72:75]\n"
      "s_waitcnt lgkmcnt(3)\n"
      "v_mfma_f32_16x16x32_bf16 v[76:79], a[64:67], v[84:87], v[76:79]\n"
      "ds_read_b128 v[104:107], v7 offset:25600\n"
      "v_mfma_f32_16x16x32_bf16 v[80:83], a[144:147], v[84:87], v[80:83]\n"
      "v_mfma_f32_16x16x32_bf16 v[44:47], a[68:71], v[88:91], v[44:47]\n"
      "ds_read_b128 v[84:87], v7 offset:26624\n"
      "s_or_b32 s98, s40, s41\n"
      "s_cbranch_scc1 Lc2p_%=\n"
      "v_and_b32 v199, 0xf0, v0\n"
      "v_lshlrev_b32 v199, 5, v199\n"
      "s_mov_b32 s84, 400000\n"
      "s_waitcnt vmcnt(3)\n"
      "Lc2u_%=:\n"
      "v_sub_u32 v196, v196, v194\n"
      "v_sub_u32 v197, v197, v195\n"
      "v_xor_b32 v196, 0x7f4a7c15, v196\n"
      "v_xor_b32 v197, 0x9e3779b9, v197\n"
      "v_or_b32 v196, v196, v197\n"
      "v_cmp_ne_u32 vcc, 0, v196\n"
      "s_cbranch_vccz Lc2v_%=\n"
      "s_sleep 6\n"
      "s_mov_b32 s98, 26279936\n"
      "buffer_load_dwordx4 v[194:197], v193, s[20:23], s98 offen sc1\n"
      "s_waitcnt vmcnt(0)\n"
      "s_sub_u32 s84, s84, 1\n"
      "s_cmp_lg_u32 s84, 0\n"
      "s_cbranch_scc1 Lc2u_%=\n"
      "Lc2v_%=:\n"
      "s_mov_b32 s98, 15990784\n"
      "buffer_load_dword v198, v199, s[24:27], s98 offen sc1\n"
      "Lc2p_%=:\n"
      "v_mfma_f32_16x16x32_bf16 v[48:51], a[148:151], v[88:91], v[48:51]\n"
      "s_waitcnt lgkmcnt(3)\n"
      "v_mfma_f32_16x16x32_bf16 v[52:55], a[68:71], v[92:95], v[52:55]\n"
      "ds_read_b128 v[88:91], v7 offset:27648\n"
      "v_mfma_f32_16x16x32_bf16 v[56:59], a[148:151], v[92:95], v[56:59]\n"
      "v_mfma_f32_16x16x32_bf16 v[60:63], a[68:71], v[96:99], v[60:63]\n"
      "ds_read_b128 v[92:95], v7 offset:28672\n"
      "v_mfma_f32_16x16x32_bf16 v[64:67], a[148:151], v[96:99], v[64:67]\n"
      "s_waitcnt lgkmcnt(3)\n"
      "v_mfma_f32_16x16x32_bf16 v[68:71], a[68:71], v[100:103], v[68:71]\n"
      "ds_read_b128 v[96:99], v7 offset:29696\n"
      "v_mfma_f32_16x16x32_bf16 v[72:75], a[148:151], v[100:103], v[72:75]\n"
      "v_mfma_f32_16x16x32_bf16 v[76:79], a[68:71], v[104:107], v[76:79]\n"
      "ds_read_b128 v[100:103], v7 offset:30720\n"
      "v_mfma_f32_16x16x32_bf16 v[80:83], a[148:151], v[104:107], v[80:83]\n"
      "s_waitcnt lgkmcnt(3)\n"
      "v_mfma_f32_16x16x32_bf16 v[44:47], a[72:75], v[84:87], v[44:47]\n"
      "ds_read_b128 v[104:107], v7 offset:31744\n"
      "v_mfma_f32_16x16x32_bf16 v[48:51], a[152:155], v[84:87], v[48:51]\n"
      "v_mfma_f32_16x16x32_bf16 v[52:55], a[72:75], v[88:91], v[52:55]\n"
      "ds_read_b128 v[84:87], v7 offset:32768\n"
      "v_mfma_f32_16x16x32_bf16 v[56:59], a[152:155], v[88:91], v[56:59]\n"
      "s_waitcnt lgkmcnt(3)\n"
      "v_mfma_f32_16x16x32_bf16 v[60:63], a[72:75], v[92:95], v[60:63]\n"
      "ds_read_b128 v[88:91], v7 offset:33792\n"
      "v_mfma_f32_16x16x32_bf16 v[64:67], a[152:155], v[92:95], v[64:67]\n"
      "v_mfma_f32_16x16x32_bf16 v[68:71], a[72:75], v[96:99], v[68:71]\n"
      "ds_read_b128 v[92:95], v7 offset:34816\n"
      "v_mfma_f32_16x16x32_bf16 v[72:75], a[152:155], v[96:99], v[72:75]\n"
      "s_waitcnt lgkmcnt(3)\n"
      "v_mfma_f32_16x16x32_bf16 v[76:79], a[72:75], v[100:103], v[76:79]\n"
      "ds_read_b128 v[96:99], v7 offset:35840\n"
      "v_mfma_f32_16x16x32_bf16 v[80:83], a[152:155], v[100:103], v[80:83]\n"
      "v_mfma_f32_16x16x32_bf16 v[44:47], a[76:79], v[104:107], v[44:47]\n"
      "v_mfma_f32_16x16x32_bf16 v[48:51], a[156:159], v[104:107], v[48:51]\n"
      "s_waitcnt lgkmcnt(2)\n"
      "v_mfma_f32_16x16x32_bf16 v[52:55], a[76:79], v[84:87], v[52:55]\n"
      "v_mfma_f32_16x16x32_bf16 v[56:59], a[156:159], v[84:87], v[56:59]\n"
      "v_mfma_f32_16x16x32_bf16 v[60:63], a[76:79], v[88:91], v[60:63]\n"
      "v_mfma_f32_16x16x32_bf16 v[64:67], a[156:159], v[88:91], v[64:67]\n"
      "s_waitcnt lgkmcnt(0)\n"
      "v_mfma_f32_16x16x32_bf16 v[68:71], a[76:79], v[92:95], v[68:71]\n"
      "v_mfma_f32_16x16x32_bf16 v[72:75], a[156:159], v[92:95], v[72:75]\n"
      "s_cmp_lg_u32 s41, 0\n"
      "s_cbranch_scc1 Lc2_%=\n"
      "s_cmp_lg_u32 s40, 0\n"
      "s_cbranch_scc1 Lc2b_%=\n"
      "s_waitcnt vmcnt(0)\n"
      "s_mov_b32 s84, 400000\n"
      "s_mov_b32 s98, 15990784\n"
      "Lc2l_%=:\n"
      "v_cmp_gt_u32 vcc, 16, v198\n"
      "s_cbranch_vccz Lc2b_%=\n"
      "s_sleep 6\n"
      "buffer_load_dword v198, v199, s[24:27], s98 offen sc1\n"
      "s_waitcnt vmcnt(0)\n"
      "s_sub_u32 s84, s84, 1\n"
      "s_cmp_lg_u32 s84, 0\n"
      "s_cbranch_scc1 Lc2l_%=\n"
      "Lc2b_%=:\n"
      "s_barrier\n"
      "Lc2_%=:\n"
      "v_mfma_f32_16x16x32_bf16 v[76:79], a[76:79], v[96:99], v[76:79]\n"
      "v_mfma_f32_16x16x32_bf16 v[80:83], a[156:159], v[96:99], v[80:83]\n"
      "s_branch LJe_%=\n"
      "Lloop_%=:\n"
      "v_add_u32 v120, 20480, v0\n"
      "s_add_u32 s75, s42, 0\n"
      "buffer_load_dwordx4 a[0:3], v0, s[24:27], s75 offen offset:0 sc1\n"
      "buffer_load_dwordx4 a[80:83], v120, s[24:27], s75 offen offset:0 sc1\n"
      "buffer_load_dwordx4 a[4:7], v0, s[24:27], s75 offen offset:1024 sc1\n"
      "buffer_load_dwordx4 a[84:87], v120, s[24:27], s75 offen offset:1024 sc1\n"
      "s_add_u32 s76, s43, 15728640\n"
      "buffer_load_dword v36, v3, s[24:27], s76 offen offset:0 sc1\n"
      "buffer_load_dword v37, v3, s[24:27], s76 offen offset:128 sc1\n"
      "buffer_load_dword v38, v3, s[24:27], s76 offen offset:256 sc1\n"
      "buffer_load_dword v39, v3, s[24:27], s76 offen offset:384 sc1\n"
      "buffer_load_dword v40, v3, s[24:27], s76 offen offset:512 sc1\n"
      "buffer_load_dword v41, v3, s[24:27], s76 offen offset:640 sc1\n"
      "buffer_load_dword v42, v3, s[24:27], s76 offen offset:768 sc1\n"
      "buffer_load_dword v43, v3, s[24:27], s76 offen offset:896 sc1\n"
      "buffer_load_dwordx4 a[8:11], v0, s[24:27], s75 offen offset:2048 sc1\n"
      "buffer_load_dwordx4 a[88:91], v120, s[24:27], s75 offen offset:2048 sc1\n"
      "buffer_load_dwordx4 a[12:15], v0, s[24:27], s75 offen offset:3072 sc1\n"
      "buffer_load_dwordx4 a[92:95], v120, s[24:27], s75 offen offset:3072 sc1\n"
      "s_add_u32 s75, s42, 4096\n"
      "buffer_load_dwordx4 a[16:19], v0, s[24:27], s75 offen offset:0 sc1\n"
      "buffer_load_dwordx4 a[96:99], v120, s[24:27], s75 offen offset:0 sc1\n"
      "ds_read_b128 v[84:87], v0 offset:0\n"
      "ds_read_b128 v[88:91], v0 offset:1024\n"
      "ds_read_b128 v[92:95], v0 offset:2048\n"
      "ds_read_b128 v[96:99], v0 offset:3072\n"
      "ds_read_b128 v[100:103], v0 offset:4096\n"
      "s_waitcnt vmcnt(17) lgkmcnt(3)\n"
      "v_mfma_f32_16x16x32_bf16 v[44:47], a[0:3], v[84:87], 0\n"
      "ds_read_b128 v[104:107], v0 offset:5120\n"
      "s_waitcnt vmcnt(16)\n"
      "v_mfma_f32_16x16x32_bf16 v[48:51], a[80:83], v[84:87], 0\n"
      "v_mfma_f32_16x16x32_bf16 v[52:55], a[0:3], v[88:91], 0\n"
      "ds_read_b128 v[84:87], v0 offset:6144\n"
      "v_mfma_f32_16x16x32_bf16 v[56:59], a[80:83], v[88:91], 0\n"
      "s_waitcnt lgkmcnt(3)\n"
      "v_mfma_f32_16x16x32_bf16 v[60:63], a[0:3], v[92:95], 0\n"
      "ds_read_b128 v[88:91], v0 offset:7168\n"
      "buffer_load_dwordx4 a[20:23], v0, s[24:27], s75 offen offset:1024 sc1\n"
      "v_mfma_f32_16x16x32_bf16 v[64:67], a[80:83], v[92:95], 0\n"
      "v_mfma_f32_16x16x32_bf16 v[68:71], a[0:3], v[96:99], 0\n"
      "ds_read_b128 v[92:95], v0 offset:8192\n"
      "v_mfma_f32_16x16x32_bf16 v[72:75], a[80:83], v[96:99], 0\n"
      "s_waitcnt lgkmcnt(3)\n"
      "v_mfma_f32_16x16x32_bf16 v[76:79], a[0:3], v[100:103], 0\n"
      "ds_read_b128 v[96:99], v0 offset:9216\n"
      "v_mfma_f32_16x16x32_bf16 v[80:83], a[80:83], v[100:103], 0\n"
      "buffer_load_dwordx4 a[100:103], v120, s[24:27], s75 offen offset:1024 sc1\n"
      "s_waitcnt vmcnt(17)\n"
      "v_mfma_f32_16x16x32_bf16 v[44:47], a[4:7], v[104:107], v[44:47]\n"
      "ds_read_b128 v[100:103], v0 offset:10240\n"
      "s_waitcnt vmcnt(16)\n"
      "v_mfma_f32_16x16x32_bf16 v[48:51], a[84:87], v[104:107], v[48:51]\n"
      "s_waitcnt lgkmcnt(3)\n"
      "v_mfma_f32_16x16x32_bf16 v[52:55], a[4:7], v[84:87], v[52:55]\n"
      "ds_read_b128 v[104:107], v0 offset:11264\n"
      "v_mfma_f32_16x16x32_bf16 v[56:59], a[84:87], v[84:87], v[56:59]\n"
      "buffer_load_dwordx4 a[24:27], v0, s[24:27], s75 offen offset:2048 sc1\n"
      "v_mfma_f32_16x16x32_bf16 v[60:63], a[4:7], v[88:91], v[60:63]\n"
      "ds_read_b128 v[84:87], v0 offset:12288\n"
      "v_mfma_f32_16x16x32_bf16 v[64:67], a[84:87], v[88:91], v[64:67]\n"
      "s_waitcnt lgkmcnt(3)\n"
      "v_mfma_f32_16x16x32_bf16 v[68:71], a[4:7], v[92:95], v[68:71]\n"
      "ds_read_b128 v[88:91], v0 offset:13312\n"
      "v_mfma_f32_16x16x32_bf16 v[72:75], a[84:87], v[92:95], v[72:75]\n"
      "v_mfma_f32_16x16x32_bf16 v[76:79], a[4:7], v[96:99], v[76:79]\n"
      "ds_read_b128 v[92:95], v0 offset:14336\n"
      "buffer_load_dwordx4 a[104:107], v120, s[24:27], s75 offen offset:2048 sc1\n"
      "v_mfma_f32_16x16x32_bf16 v[80:83], a[84:87], v[96:99], v[80:83]\n"
      "s_waitcnt vmcnt(9) lgkmcnt(3)\n"
      "v_mfma_f32_16x16x32_bf16 v[44:47], a[8:11], v[100:103], v[44:47]\n"
      "ds_read_b128 v[96:99], v0 offset:15360\n"
      "s_waitcnt vmcnt(8)\n"
      "v_mfma_f32_16x16x32_bf16 v[48:51], a[88:91], v[100:103], v[48:51]\n"
      "v_mfma_f32_16x16x32_bf16 v[52:55], a[8:11], v[104:107], v[52:55]\n"
      "ds_read_b128 v[100:103], v0 offset:16384\n"
      "buffer_load_dwordx4 a[28:31], v0, s[24:27], s75 offen offset:3072 sc1\n"
      "v_mfma_f32_16x16x32_bf16 v[56:59], a[88:91], v[104:107], v[56:59]\n"
      "s_waitcnt lgkmcnt(3)\n"
      "v_mfma_f32_16x16x32_bf16 v[60:63], a[8:11], v[84:87], v[60:63]\n"
      "ds_read_b128 v[104:107], v0 offset:17408\n"
      "v_mfma_f32_16x16x32_bf16 v[64:67], a[88:91], v[84:87], v[64:67]\n"
      "v_mfma_f32_16x16x32_bf16 v[68:71], a[8:11], v[88:91], v[68:71]\n"
      "ds_read_b128 v[84:87], v0 offset:18432\n"
      "v_mfma_f32_16x16x32_bf16 v[72:75], a[88:91], v[88:91], v[72:75]\n"
      "buffer_load_dwordx4 a[108:111], v120, s[24:27], s75 offen offset:3072 sc1\n"
      "s_waitcnt lgkmcnt(3)\n"
      "v_mfma_f32_16x16x32_bf16 v[76:79], a[8:11], v[92:95], v[76:79]\n"
      "ds_read_b128 v[88:91], v0 offset:19456\n"
      "v_mfma_f32_16x16x32_bf16 v[80:83], a[88:91], v[92:95], v[80:83]\n"
      "s_waitcnt vmcnt(9)\n"
      "v_mfma_f32_16x16x32_bf16 v[44:47], a[12:15], v[96:99], v[44:47]\n"
      "ds_read_b128 v[92:95], v0 offset:20480\n"
      "s_waitcnt vmcnt(8)\n"
      "v_mfma_f32_16x16x32_bf16 v[48:51], a[92:95], v[96:99], v[48:51]\n"
      "s_add_u32 s75, s42, 8192\n"
      "buffer_load_dwordx4 a[32:35], v0, s[24:27], s75 offen offset:0 sc1\n"
      "s_waitcnt lgkmcnt(3)\n"
      "v_mfma_f32_16x16x32_bf16 v[52:55], a[12:15], v[100:103], v[52:55]\n"
      "ds_read_b128 v[96:99], v0 offset:21504\n"
      "v_mfma_f32_16x16x32_bf16 v[56:59], a[92:95], v[100:103], v[56:59]\n"
      "v_mfma_f32_16x16x32_bf16 v[60:63], a[12:15], v[104:107], v[60:63]\n"
      "ds_read_b128 v[100:103], v0 offset:22528\n"
      "v_mfma_f32_16x16x32_bf16 v[64:67], a[92:95], v[104:107], v[64:67]\n"
      "s_waitcnt lgkmcnt(3)\n"
      "v_mfma_f32_16x16x32_bf16 v[68:71], a[12:15], v[84:87], v[68:71]\n"
      "ds_read_b128 v[104:107], v0 offset:23552\n"
      "buffer_load_dwordx4 a[112:115], v120, s[24:27], s75 offen offset:0 sc1\n"
      "v_mfma_f32_16x16x32_bf16 v[72:75], a[92:95], v[84:87], v[72:75]\n"
      "v_mfma_f32_16x16x32_bf16 v[76:79], a[12:15], v[88:91], v[76:79]\n"
      "ds_read_b128 v[84:87], v0 offset:24576\n"
      "v_mfma_f32_16x16x32_bf16 v[80:83], a[92:95], v[88:91], v[80:83]\n"
      "s_waitcnt vmcnt(9) lgkmcnt(3)\n"
      "v_mfma_f32_16x16x32_bf16 v[44:47], a[16:19], v[92:95], v[44:47]\n"
      "ds_read_b128 v[88:91], v0 offset:25600\n"
      "buffer_load_dwordx4 a[36:39], v0, s[24:27], s75 offen offset:1024 sc1\n"
      "s_waitcnt vmcnt(9)\n"
      "v_mfma_f32_16x16x32_bf16 v[48:51], a[96:99], v[92:95], v[48:51]\n"
      "v_mfma_f32_16x16x32_bf16 v[52:55], a[16:19], v[96:99], v[52:55]\n"
      "ds_read_b128 v[92:95], v0 offset:26624\n"
      "v_mfma_f32_16x16x32_bf16 v[56:59], a[96:99], v[96:99], v[56:59]\n"
      "s_waitcnt lgkmcnt(3)\n"
      "v_mfma_f32_16x16x32_bf16 v[60:63], a[16:19], v[100:103], v[60:63]\n"
      "ds_read_b128 v[96:99], v0 offset:27648\n"
      "v_mfma_f32_16x16x32_bf16 v[64:67], a[96:99], v[100:103], v[64:67]\n"
      "buffer_load_dwordx4 a[116:119], v120, s[24:27], s75 offen offset:1024 sc1\n"
      "v_mfma_f32_16x16x32_bf16 v[68:71], a[16:19], v[104:107], v[68:71]\n"
      "ds_read_b128 v[100:103], v0 offset:28672\n"
      "v_mfma_f32_16x16x32_bf16 v[72:75], a[96:99], v[104:107], v[72:75]\n"
      "s_waitcnt lgkmcnt(3)\n"
      "v_mfma_f32_16x16x32_bf16 v[76:79], a[16:19], v[84:87], v[76:79]\n"
      "ds_read_b128 v[104:107], v0 offset:29696\n"
      "v_mfma_f32_16x16x32_bf16 v[80:83], a[96:99], v[84:87], v[80:83]\n"
      "buffer_load_dwordx4 a[40:43], v0, s[24:27], s75 offen offset:2048 sc1\n"
      "s_waitcnt vmcnt(10)\n"
      "v_mfma_f32_16x16x32_bf16 v[44:47], a[20:23], v[88:91], v[44:47]\n"
      "ds_read_b128 v[84:87], v0 offset:30720\n"
      "s_waitcnt vmcnt(9)\n"
      "v_mfma_f32_16x16x32_bf16 v[48:51], a[100:103], v[88:91], v[48:51]\n"
      "s_waitcnt lgkmcnt(3)\n"
      "v_mfma_f32_16x16x32_bf16 v[52:55], a[20:23], v[92:95], v[52:55]\n"
      "ds_read_b128 v[88:91], v0 offset:31744\n"
      "v_mfma_f32_16x16x32_bf16 v[56:59], a[100:103], v[92:95], v[56:59]\n"
      "v_mfma_f32_16x16x32_bf16 v[60:63], a[20:23], v[96:99], v[60:63]\n"
      "ds_read_b128 v[92:95], v0 offset:32768\n"
      "buffer_load_dwordx4 a[120:123], v120, s[24:27], s75 offen offset:2048 sc1\n"
      "v_mfma_f32_16x16x32_bf16 v[64:67], a[100:103], v[96:99], v[64:67]\n"
      "s_waitcnt lgkmcnt(3)\n"
      "v_mfma_f32_16x16x32_bf16 v[68:71], a[20:23], v[100:103], v[68:71]\n"
      "ds_read_b128 v[96:99], v0 offset:33792\n"
      "v_mfma_f32_16x16x32_bf16 v[72:75], a[100:103], v[100:103], v[72:75]\n"
      "v_mfma_f32_16x16x32_bf16 v[76:79], a[20:23], v[104:107], v[76:79]\n"
      "ds_read_b128 v[100:103], v0 offset:34816\n"
      "v_mfma_f32_16x16x32_bf16 v[80:83], a[100:103], v[104:107], v[80:83]\n"
      "buffer_load_dwordx4 a[44:47], v0, s[24:27], s75 offen offset:3072 sc1\n"
      "s_waitcnt vmcnt(10) lgkmcnt(3)\n"
      "v_mfma_f32_16x16x32_bf16 v[44:47], a[24:27], v[84:87], v[44:47]\n"
      "ds_read_b128 v[104:107], v0 offset:35840\n"
      "s_waitcnt vmcnt(9)\n"
      "v_mfma_f32_16x16x32_bf16 v[48:51], a[104:107], v[84:87], v[48:51]\n"
      "v_add_f32 v36, v36, v37\n"
      "v_mfma_f32_16x16x32_bf16 v[52:55], a[24:27], v[88:91], v[52:55]\n"
      "ds_read_b128 v[84:87], v0 offset:36864\n"
      "v_mfma_f32_16x16x32_bf16 v[56:59], a[104:107], v[88:91], v[56:59]\n"
      "buffer_load_dwordx4 a[124:127], v120, s[24:27], s75 offen offset:3072 sc1\n"
      "v_add_f32 v38, v38, v39\n"
      "s_waitcnt lgkmcnt(3)\n"
      "v_mfma_f32_16x16x32_bf16 v[60:63], a[24:27], v[92:95], v[60:63]\n"
      "ds_read_b128 v[88:91], v0 offset:37888\n"
      "v_mfma_f32_16x16x32_bf16 v[64:67], a[104:107], v[92:95], v[64:67]\n"
      "v_add_f32 v40, v40, v41\n"
      "v_mfma_f32_16x16x32_bf16 v[68:71], a[24:27], v[96:99], v[68:71]\n"
      "ds_read_b128 v[92:95], v0 offset:38912\n"
      "v_mfma_f32_16x16x32_bf16 v[72:75], a[104:107], v[96:99], v[72:75]\n"
      "v_add_f32 v42, v42, v43\n"
      "s_waitcnt lgkmcnt(3)\n"
      "v_mfma_f32_16x16x32_bf16 v[76:79], a[24:27], v[100:103], v[76:79]\n"
      "ds_read_b128 v[96:99], v0 offset:39936\n"
      "s_add_u32 s75, s42, 12288\n"
      "buffer_load_dwordx4 a[48:51], v0, s[24:27], s75 offen offset:0 sc1\n"
      "s_barrier\n"
      "v_mfma_f32_16x16x32_bf16 v[80:83], a[104:107], v[100:103], v[80:83]\n"
      "v_add_f32 v36, v36, v38\n"
      "s_waitcnt vmcnt(10)\n"
      "v_mfma_f32_16x16x32_bf16 v[44:47], a[28:31], v[104:107], v[44:47]\n"
      "ds_read_b128 v[100:103], v0 offset:40960\n"
      "s_waitcnt vmcnt(9)\n"
      "v_mfma_f32_16x16x32_bf16 v[48:51], a[108:111], v[104:107], v[48:51]\n"
      "v_add_f32 v40, v40, v42\n"
      "s_waitcnt lgkmcnt(3)\n"
      "v_mfma_f32_16x16x32_bf16 v[52:55], a[28:31], v[84:87], v[52:55]\n"
      "ds_read_b128 v[104:107], v0 offset:41984\n"
      "buffer_load_dwordx4 a[128:131], v120, s[24:27], s75 offen offset:0 sc1\n"
      "v_mfma_f32_16x16x32_bf16 v[56:59], a[108:111], v[84:87], v[56:59]\n"
      "v_add_f32 v36, v36, v40\n"
      "v_mfma_f32_16x16x32_bf16 v[60:63], a[28:31], v[88:91], v[60:63]\n"
      "ds_read_b128 v[84:87], v0 offset:43008\n"
      "v_mfma_f32_16x16x32_bf16 v[64:67], a[108:111], v[88:91], v[64:67]\n"
      "v_mul_f32 v36, 0x3acccccd, v36\n"
      "s_waitcnt lgkmcnt(3)\n"
      "v_mfma_f32_16x16x32_bf16 v[68:71], a[28:31], v[92:95], v[68:71]\n"
      "ds_read_b128 v[88:91], v0 offset:44032\n"
      "v_mfma_f32_16x16x32_bf16 v[72:75], a[108:111], v[92:95], v[72:75]\n"
      "buffer_load_dwordx4 a[52:55], v0, s[24:27], s75 offen offset:1024 sc1\n"
      "v_mov_b32 v37, v36\n"
      "v_mfma_f32_16x16x32_bf16 v[76:79], a[28:31], v[96:99], v[76:79]\n"
      "ds_read_b128 v[92:95], v0 offset:45056\n"
      "v_mfma_f32_16x16x32_bf16 v[80:83], a[108:111], v[96:99], v[80:83]\n"
      "v_permlane32_swap_b32 v37, v36\n"
      "s_waitcnt vmcnt(10) lgkmcnt(3)\n"
      "v_mfma_f32_16x16x32_bf16 v[44:47], a[32:35], v[100:103], v[44:47]\n"
      "ds_read_b128 v[96:99], v0 offset:46080\n"
      "s_waitcnt vmcnt(9)\n"
      "v_mfma_f32_16x16x32_bf16 v[48:51], a[112:115], v[100:103], v[48:51]\n"
      "buffer_load_dwordx4 a[132:135], v120, s[24:27], s75 offen offset:1024 sc1\n"
      "v_fma_f32 v36, -v37, v37, v36\n"
      "v_mfma_f32_16x16x32_bf16 v[52:55], a[32:35], v[104:107], v[52:55]\n"
      "ds_read_b128 v[100:103], v0 offset:47104\n"
      "v_mfma_f32_16x16x32_bf16 v[56:59], a[112:115], v[104:107], v[56:59]\n"
      "v_max_f32 v36, 0, v36\n"
      "s_waitcnt lgkmcnt(3)\n"
      "v_mfma_f32_16x16x32_bf16 v[60:63], a[32:35], v[84:87], v[60:63]\n"
      "ds_read_b128 v[104:107], v0 offset:48128\n"
      "v_mfma_f32_16x16x32_bf16 v[64:67], a[112:115], v[84:87], v[64:67]\n"
      "v_add_f32 v36, 0x3727c5ac, v36\n"
      "v_mfma_f32_16x16x32_bf16 v[68:71], a[32:35], v[88:91], v[68:71]\n"
      "ds_read_b128 v[84:87], v0 offset:49152\n"
      "buffer_load_dwordx4 a[56:59], v0, s[24:27], s75 offen offset:2048 sc1\n"
      "v_mfma_f32_16x16x32_bf16 v[72:75], a[112:115], v[88:91], v[72:75]\n"
      "v_rsq_f32 v37, v36\n"
      "s_waitcnt lgkmcnt(3)\n"
      "v_mfma_f32_16x16x32_bf16 v[76:79], a[32:35], v[92:95], v[76:79]\n"
      "ds_read_b128 v[88:91], v0 offset:50176\n"
      "v_mfma_f32_16x16x32_bf16 v[80:83], a[112:115], v[92:95], v[80:83]\n"
      "v_mul_f32 v37, 0x3fb8aa3b, v37\n"
      "s_waitcnt vmcnt(10)\n"
      "v_mfma_f32_16x16x32_bf16 v[44:47], a[36:39], v[96:99], v[44:47]\n"
      "ds_read_b128 v[92:95], v0 offset:51200\n"
      "buffer_load_dwordx4 a[136:139], v120, s[24:27], s75 offen offset:2048 sc1\n"
      "s_waitcnt vmcnt(10)\n"
      "v_mfma_f32_16x16x32_bf16 v[48:51], a[116:119], v[96:99], v[48:51]\n"
      "v_lshrrev_b32 v38, 2, v0\n"
      "s_waitcnt lgkmcnt(3)\n"
      "v_mfma_f32_16x16x32_bf16 v[52:55], a[36:39], v[100:103], v[52:55]\n"
      "ds_read_b128 v[96:99], v0 offset:52224\n"
      "v_mfma_f32_16x16x32_bf16 v[56:59], a[116:119], v[100:103], v[56:59]\n"
      "v_and_b32 v39, 0x7c, v38\n"
      "v_mfma_f32_16x16x32_bf16 v[60:63], a[36:39], v[104:107], v[60:63]\n"
      "ds_read_b128 v[100:103], v0 offset:53248\n"
      "v_mfma_f32_16x16x32_bf16 v[64:67], a[116:119], v[104:107], v[64:67]\n"
      "buffer_load_dwordx4 a[60:63], v0, s[24:27], s75 offen offset:3072 sc1\n"
      "s_lshl_b32 s76, s40, 7\n"
      "s_waitcnt lgkmcnt(3)\n"
      "v_mfma_f32_16x16x32_bf16 v[68:71], a[36:39], v[84:87], v[68:71]\n"
      "ds_read_b128 v[104:107], v0 offset:54272\n"
      "v_mfma_f32_16x16x32_bf16 v[72:75], a[116:119], v[84:87], v[72:75]\n"
      "s_add_u32 s76, s76, 159744\n"
      "v_mfma_f32_16x16x32_bf16 v[76:79], a[36:39], v[88:91], v[76:79]\n"
      "ds_read_b128 v[84:87], v0 offset:55296\n"
      "v_mfma_f32_16x16x32_bf16 v[80:83], a[116:119], v[88:91], v[80:83]\n"
      "buffer_load_dwordx4 a[140:143], v120, s[24:27], s75 offen offset:3072 sc1\n"
      "v_add_u32 v39, s76, v39\n"
      "s_waitcnt vmcnt(11) lgkmcnt(3)\n"
      "v_mfma_f32_16x16x32_bf16 v[44:47], a[40:43], v[92:95], v[44:47]\n"
      "ds_read_b128 v[88:91], v0 offset:56320\n"
      "s_waitcnt vmcnt(10)\n"
      "v_mfma_f32_16x16x32_bf16 v[48:51], a[120:123], v[92:95], v[48:51]\n"
      "ds_write_b32 v39, v37\n"
      "v_mfma_f32_16x16x32_bf16 v[52:55], a[40:43], v[96:99], v[52:55]\n"
      "ds_read_b128 v[92:95], v0 offset:57344\n"
      "v_mfma_f32_16x16x32_bf16 v[56:59], a[120:123], v[96:99], v[56:59]\n"
      "v_lshrrev_b32 v40, 4, v0\n"
      "s_waitcnt lgkmcnt(4)\n"
      "v_mfma_f32_16x16x32_bf16 v[60:63], a[40:43], v[100:103], v[60:63]\n"
      "ds_read_b128 v[96:99], v0 offset:58368\n"
      "s_add_u32 s75, s42, 16384\n"
      "buffer_load_dwordx4 a[64:67], v0, s[24:27], s75 offen offset:0 sc1\n"
      "v_mfma_f32_16x16x32_bf16 v[64:67], a[120:123], v[100:103], v[64:67]\n"
      "v_and_b32 v40, 0x30, v40\n"
      "v_mfma_f32_16x16x32_bf16 v[68:71], a[40:43], v[104:107], v[68:71]\n"
      "ds_read_b128 v[100:103], v0 offset:59392\n"
      "v_mfma_f32_16x16x32_bf16 v[72:75], a[120:123], v[104:107], v[72:75]\n"
      "v_add_u32 v40, s76, v40\n"
      "s_waitcnt lgkmcnt(4)\n"
      "v_mfma_f32_16x16x32_bf16 v[76:79], a[40:43], v[84:87], v[76:79]\n"
      "ds_read_b128 v[104:107], v0 offset:60416\n"
      "v_mfma_f32_16x16x32_bf16 v[80:83], a[120:123], v[84:87], v[80:83]\n"
      "buffer_load_dwordx4 a[144:147], v120, s[24:27], s75 offen offset:0 sc1\n"
      "v_and_b32 v38, 0x3c, v38\n"
      "s_waitcnt vmcnt(11)\n"
      "v_mfma_f32_16x16x32_bf16 v[44:47], a[44:47], v[88:91], v[44:47]\n"
      "ds_read_b128 v[84:87], v0 offset:61440\n"
      "s_waitcnt vmcnt(10)\n"
      "v_mfma_f32_16x16x32_bf16 v[48:51], a[124:127], v[88:91], v[48:51]\n"
      "v_add_u32 v38, s76, v38\n"
      "s_waitcnt lgkmcnt(3)\n"
      "v_mfma_f32_16x16x32_bf16 v[52:55], a[44:47], v[92:95], v[52:55]\n"
      "ds_read_b128 v[88:91], v0 offset:62464\n"
      "v_mfma_f32_16x16x32_bf16 v[56:59], a[124:127], v[92:95], v[56:59]\n"
      "buffer_load_dwordx4 a[68:71], v0, s[24:27], s75 offen offset:1024 sc1\n"
      "ds_read_b128 v[108:111], v40\n"
      "v_mfma_f32_16x16x32_bf16 v[60:63], a[44:47], v[96:99], v[60:63]\n"
      "ds_read_b128 v[92:95], v0 offset:63488\n"
      "v_mfma_f32_16x16x32_bf16 v[64:67], a[124:127], v[96:99], v[64:67]\n"
      "ds_read_b128 v[112:115], v40 offset:64\n"
      "s_waitcnt lgkmcnt(5)\n"
      "v_mfma_f32_16x16x32_bf16 v[68:71], a[44:47], v[100:103], v[68:71]\n"
      "ds_read_b128 v[96:99], v0 offset:64512\n"
      "v_mfma_f32_16x16x32_bf16 v[72:75], a[124:127], v[100:103], v[72:75]\n"
      "ds_read_b32 v116, v38\n"
      "v_mfma_f32_16x16x32_bf16 v[76:79], a[44:47], v[104:107], v[76:79]\n"
      "ds_read_b128 v[100:103], v7 offset:0\n"
      "buffer_load_dwordx4 a[148:151], v120, s[24:27], s75 offen offset:1024 sc1\n"
      "v_mfma_f32_16x16x32_bf16 v[80:83], a[124:127], v[104:107], v[80:83]\n"
      "ds_read_b32 v118, v38 offset:64\n"
      "s_waitcnt vmcnt(11) lgkmcnt(7)\n"
      "v_mfma_f32_16x16x32_bf16 v[44:47], a[48:51], v[84:87], v[44:47]\n"
      "ds_read_b128 v[104:107], v7 offset:1024\n"
      "s_waitcnt vmcnt(10)\n"
      "v_mfma_f32_16x16x32_bf16 v[48:51], a[128:131], v[84:87], v[48:51]\n"
      "v_mfma_f32_16x16x32_bf16 v[52:55], a[48:51], v[88:91], v[52:55]\n"
      "ds_read_b128 v[84:87], v7 offset:2048\n"
      "buffer_load_dwordx4 a[72:75], v0, s[24:27], s75 offen offset:2048 sc1\n"
      "v_mfma_f32_16x16x32_bf16 v[56:59], a[128:131], v[88:91], v[56:59]\n"
      "s_waitcnt lgkmcnt(5)\n"
      "v_mfma_f32_16x16x32_bf16 v[60:63], a[48:51], v[92:95], v[60:63]\n"
      "ds_read_b128 v[88:91], v7 offset:3072\n"
      "v_mfma_f32_16x16x32_bf16 v[64:67], a[128:131], v[92:95], v[64:67]\n"
      "v_mfma_f32_16x16x32_bf16 v[68:71], a[48:51], v[96:99], v[68:71]\n"
      "ds_read_b128 v[92:95], v7 offset:4096\n"
      "v_mfma_f32_16x16x32_bf16 v[72:75], a[128:131], v[96:99], v[72:75]\n"
      "buffer_load_dwordx4 a[152:155], v120, s[24:27], s75 offen offset:2048 sc1\n"
      "s_waitcnt lgkmcnt(3)\n"
      "v_mfma_f32_16x16x32_bf16 v[76:79], a[48:51], v[100:103], v[76:79]\n"
      "ds_read_b128 v[96:99], v7 offset:5120\n"
      "v_mfma_f32_16x16x32_bf16 v[80:83], a[128:131], v[100:103], v[80:83]\n"
      "s_waitcnt vmcnt(11)\n"
      "v_mfma_f32_16x16x32_bf16 v[44:47], a[52:55], v[104:107], v[44:47]\n"
      "ds_read_b128 v[100:103], v7 offset:6144\n"
      "s_waitcnt vmcnt(10)\n"
      "v_mfma_f32_16x16x32_bf16 v[48:51], a[132:135], v[104:107], v[48:51]\n"
      "buffer_load_dwordx4 a[76:79], v0, s[24:27], s75 offen offset:3072 sc1\n"
      "s_waitcnt lgkmcnt(3)\n"
      "v_mfma_f32_16x16x32_bf16 v[52:55], a[52:55], v[84:87], v[52:55]\n"
      "ds_read_b128 v[104:107], v7 offset:7168\n"
      "v_mfma_f32_16x16x32_bf16 v[56:59], a[132:135], v[84:87], v[56:59]\n"
      "v_mfma_f32_16x16x32_bf16 v[60:63], a[52:55], v[88:91], v[60:63]\n"
      "ds_read_b128 v[84:87], v7 offset:8192\n"
      "v_mfma_f32_16x16x32_bf16 v[64:67], a[132:135], v[88:91], v[64:67]\n"
      "s_waitcnt lgkmcnt(3)\n"
      "v_mfma_f32_16x16x32_bf16 v[68:71], a[52:55], v[92:95], v[68:71]\n"
      "ds_read_b128 v[88:91], v7 offset:9216\n"
      "buffer_load_dwordx4 a[156:159], v120, s[24:27], s75 offen offset:3072 sc1\n"
      "v_mfma_f32_16x16x32_bf16 v[72:75], a[132:135], v[92:95], v[72:75]\n"
      "v_mfma_f32_16x16x32_bf16 v[76:79], a[52:55], v[96:99], v[76:79]\n"
      "ds_read_b128 v[92:95], v7 offset:10240\n"
      "v_mfma_f32_16x16x32_bf16 v[80:83], a[132:135], v[96:99], v[80:83]\n"
      "s_waitcnt vmcnt(11) lgkmcnt(3)\n"
      "v_mfma_f32_16x16x32_bf16 v[44:47], a[56:59], v[100:103], v[44:47]\n"
      "ds_read_b128 v[96:99], v7 offset:11264\n"
      "s_waitcnt vmcnt(10)\n"
      "v_mfma_f32_16x16x32_bf16 v[48:51], a[136:139], v[100:103], v[48:51]\n"
      "v_mfma_f32_16x16x32_bf16 v[52:55], a[56:59], v[104:107], v[52:55]\n"
      "ds_read_b128 v[100:103], v7 offset:12288\n"
      "v_mfma_f32_16x16x32_bf16 v[56:59], a[136:139], v[104:107], v[56:59]\n"
      "s_waitcnt lgkmcnt(3)\n"
      "v_mfma_f32_16x16x32_bf16 v[60:63], a[56:59], v[84:87], v[60:63]\n"
      "ds_read_b128 v[104:107], v7 offset:13312\n"
      "s_add_u32 s79, s66, 102400\n"
      "s_add_u32 m0, s71, 102400\n"
      "s_nop 0\n"
      "buffer_load_dwordx4 v0, s[20:23], s79 offen offset:0 sc1 lds\n"
      "v_mfma_f32_16x16x32_bf16 v[64:67], a[136:139], v[84:87], v[64:67]\n"
      "v_mfma_f32_16x16x32_bf16 v[68:71], a[56:59], v[88:91], v[68:71]\n"
      "ds_read_b128 v[84:87], v7 offset:14336\n"
      "v_mfma_f32_16x16x32_bf16 v[72:75], a[136:139], v[88:91], v[72:75]\n"
      "s_waitcnt lgkmcnt(3)\n"
      "v_mfma_f32_16x16x32_bf16 v[76:79], a[56:59], v[92:95], v[76:79]\n"
      "ds_read_b128 v[88:91], v7 offset:15360\n"
      "v_mfma_f32_16x16x32_bf16 v[80:83], a[136:139], v[92:95], v[80:83]\n"
      "buffer_load_dwordx4 v0, s[20:23], s79 offen offset:1024 sc1 lds\n"
      "s_waitcnt vmcnt(11)\n"
      "v_mfma_f32_16x16x32_bf16 v[44:47], a[60:63], v[96:99], v[44:47]\n"
      "ds_read_b128 v[92:95], v7 offset:16384\n"
      "s_waitcnt vmcnt(10)\n"
      "v_mfma_f32_16x16x32_bf16 v[48:51], a[140:143], v[96:99], v[48:51]\n"
      "s_waitcnt lgkmcnt(3)\n"
      "v_mfma_f32_16x16x32_bf16 v[52:55], a[60:63], v[100:103], v[52:55]\n"
      "ds_read_b128 v[96:99], v7 offset:17408\n"
      "v_mfma_f32_16x16x32_bf16 v[56:59], a[140:143], v[100:103], v[56:59]\n"
      "buffer_load_dwordx4 v0, s[20:23], s79 offen offset:2048 sc1 lds\n"
      "v_mfma_f32_16x16x32_bf16 v[60:63], a[60:63], v[104:107], v[60:63]\n"
      "ds_read_b128 v[100:103], v7 offset:18432\n"
      "v_mfma_f32_16x16x32_bf16 v[64:67], a[140:143], v[104:107], v[64:67]\n"
      "s_waitcnt lgkmcnt(3)\n"
      "v_mfma_f32_16x16x32_bf16 v[68:71], a[60:63], v[84:87], v[68:71]\n"
      "ds_read_b128 v[104:107], v7 offset:19456\n"
      "v_mfma_f32_16x16x32_bf16 v[72:75], a[140:143], v[84:87], v[72:75]\n"
      "v_mfma_f32_16x16x32_bf16 v[76:79], a[60:63], v[88:91], v[76:79]\n"
      "ds_read_b128 v[84:87], v7 offset:20480\n"
      "buffer_load_dwordx4 v0, s[20:23], s79 offen offset:3072 sc1 lds\n"
      "v_mfma_f32_16x16x32_bf16 v[80:83], a[140:143], v[88:91], v[80:83]\n"
      "s_waitcnt vmcnt(11) lgkmcnt(3)\n"
      "v_mfma_f32_16x16x32_bf16 v[44:47], a[64:67], v[92:95], v[44:47]\n"
      "ds_read_b128 v[88:91], v7 offset:21504\n"
      "s_waitcnt vmcnt(10)\n"
      "v_mfma_f32_16x16x32_bf16 v[48:51], a[144:147], v[92:95], v[48:51]\n"
      "v_mfma_f32_16x16x32_bf16 v[52:55], a[64:67], v[96:99], v[52:55]\n"
      "ds_read_b128 v[92:95], v7 offset:22528\n"
      "s_add_u32 s79, s66, 106496\n"
      "s_add_u32 m0, s71, 106496\n"
      "s_nop 0\n"
      "buffer_load_dwordx4 v0, s[20:23], s79 offen offset:0 sc1 lds\n"
      "v_mfma_f32_16x16x32_bf16 v[56:59], a[144:147], v[96:99], v[56:59]\n"
      "s_waitcnt lgkmcnt(3)\n"
      "v_mfma_f32_16x16x32_bf16 v[60:63], a[64:67], v[100:103], v[60:63]\n"
      "ds_read_b128 v[96:99], v7 offset:23552\n"
      "v_mfma_f32_16x16x32_bf16 v[64:67], a[144:147], v[100:103], v[64:67]\n"
      "v_mfma_f32_16x16x32_bf16 v[68:71], a[64:67], v[104:107], v[68:71]\n"
      "ds_read_b128 v[100:103], v7 offset:24576\n"
      "v_mfma_f32_16x16x32_bf16 v[72:75], a[144:147], v[104:107], v[72:75]\n"
      "buffer_load_dwordx4 v0, s[20:23], s79 offen offset:1024 sc1 lds\n"
      "s_waitcnt lgkmcnt(3)\n"
      "v_mfma_f32_16x16x32_bf16 v[76:79], a[64:67], v[84:87], v[76:79]\n"
      "ds_read_b128 v[104:107], v7 offset:25600\n"
      "v_mfma_f32_16x16x32_bf16 v[80:83], a[144:147], v[84:87], v[80:83]\n"
      "s_waitcnt vmcnt(11)\n"
      "v_mfma_f32_16x16x32_bf16 v[44:47], a[68:71], v[88:91], v[44:47]\n"
      "ds_read_b128 v[84:87], v7 offset:26624\n"
      "s_waitcnt vmcnt(10)\n"
      "v_mfma_f32_16x16x32_bf16 v[48:51], a[148:151], v[88:91], v[48:51]\n"
      "buffer_load_dwordx4 v0, s[20:23], s79 offen offset:2048 sc1 lds\n"
      "s_waitcnt lgkmcnt(3)\n"
      "v_mfma_f32_16x16x32_bf16 v[52:55], a[68:71], v[92:95], v[52:55]\n"
      "ds_read_b128 v[88:91], v7 offset:27648\n"
      "v_mfma_f32_16x16x32_bf16 v[56:59], a[148:151], v[92:95], v[56:59]\n"
      "v_mfma_f32_16x16x32_bf16 v[60:63], a[68:71], v[96:99], v[60:63]\n"
      "ds_read_b128 v[92:95], v7 offset:28672\n"
      "v_mfma_f32_16x16x32_bf16 v[64:67], a[148:151], v[96:99], v[64:67]\n"
      "s_waitcnt lgkmcnt(3)\n"
      "v_mfma_f32_16x16x32_bf16 v[68:71], a[68:71], v[100:103], v[68:71]\n"
      "ds_read_b128 v[96:99], v7 offset:29696\n"
      "buffer_load_dwordx4 v0, s[20:23], s79 offen offset:3072 sc1 lds\n"
      "v_mfma_f32_16x16x32_bf16 v[72:75], a[148:151], v[100:103], v[72:75]\n"
      "v_mfma_f32_16x16x32_bf16 v[76:79], a[68:71], v[104:107], v[76:79]\n"
      "ds_read_b128 v[100:103], v7 offset:30720\n"
      "v_mfma_f32_16x16x32_bf16 v[80:83], a[148:151], v[104:107], v[80:83]\n"
      "s_waitcnt vmcnt(11) lgkmcnt(3)\n"
      "v_mfma_f32_16x16x32_bf16 v[44:47], a[72:75], v[84:87], v[44:47]\n"
      "ds_read_b128 v[104:107], v7 offset:31744\n"
      "s_add_u32 s79, s66, 110592\n"
      "s_add_u32 m0, s71, 110592\n"
      "s_nop 0\n"
      "buffer_load_dwordx4 v0, s[20:23], s79 offen offset:0 sc1 lds\n"
      "s_waitcnt vmcnt(11)\n"
      "v_mfma_f32_16x16x32_bf16 v[48:51], a[152:155], v[84:87], v[48:51]\n"
      "v_mfma_f32_16x16x32_bf16 v[52:55], a[72:75], v[88:91], v[52:55]\n"
      "ds_read_b128 v[84:87], v7 offset:32768\n"
      "v_mfma_f32_16x16x32_bf16 v[56:59], a[152:155], v[88:91], v[56:59]\n"
      "s_waitcnt lgkmcnt(3)\n"
      "v_mfma_f32_16x16x32_bf16 v[60:63], a[72:75], v[92:95], v[60:63]\n"
      "ds_read_b128 v[88:91], v7 offset:33792\n"
      "v_mfma_f32_16x16x32_bf16 v[64:67], a[152:155], v[92:95], v[64:67]\n"
      "buffer_load_dwordx4 v0, s[20:23], s79 offen offset:1024 sc1 lds\n"
      "v_mfma_f32_16x16x32_bf16 v[68:71], a[72:75], v[96:99], v[68:71]\n"
      "ds_read_b128 v[92:95], v7 offset:34816\n"
      "v_mfma_f32_16x16x32_bf16 v[72:75], a[152:155], v[96:99], v[72:75]\n"
      "s_waitcnt lgkmcnt(3)\n"
      "v_mfma_f32_16x16x32_bf16 v[76:79], a[72:75], v[100:103], v[76:79]\n"
      "ds_read_b128 v[96:99], v7 offset:35840\n"
      "v_mfma_f32_16x16x32_bf16 v[80:83], a[152:155], v[100:103], v[80:83]\n"
      "buffer_load_dwordx4 v0, s[20:23], s79 offen offset:2048 sc1 lds\n"
      "s_waitcnt vmcnt(12)\n"
      "v_mfma_f32_16x16x32_bf16 v[44:47], a[76:79], v[104:107], v[44:47]\n"
      "s_waitcnt vmcnt(11)\n"
      "v_mfma_f32_16x16x32_bf16 v[48:51], a[156:159], v[104:107], v[48:51]\n"
      "s_waitcnt lgkmcnt(2)\n"
      "v_mfma_f32_16x16x32_bf16 v[52:55], a[76:79], v[84:87], v[52:55]\n"
      "v_mfma_f32_16x16x32_bf16 v[56:59], a[156:159], v[84:87], v[56:59]\n"
      "v_mfma_f32_16x16x32_bf16 v[60:63], a[76:79], v[88:91], v[60:63]\n"
      "buffer_load_dwordx4 v0, s[20:23], s79 offen offset:3072 sc1 lds\n"
      "v_mfma_f32_16x16x32_bf16 v[64:67], a[156:159], v[88:91], v[64:67]\n"
      "s_waitcnt lgkmcnt(0)\n"
      "v_mfma_f32_16x16x32_bf16 v[68:71], a[76:79], v[92:95], v[68:71]\n"
      "v_mfma_f32_16x16x32_bf16 v[72:75], a[156:159], v[92:95], v[72:75]\n"
      "v_mfma_f32_16x16x32_bf16 v[76:79], a[76:79], v[96:99], v[76:79]\n"
      "v_mfma_f32_16x16x32_bf16 v[80:83], a[156:159], v[96:99], v[80:83]\n"
      "s_add_u32 s79, s66, 114688\n"
      "s_add_u32 m0, s71, 114688\n"
      "s_nop 0\n"
      "buffer_load_dwordx4 v0, s[20:23], s79 offen offset:0 sc1 lds\n"
      "LJe_%=:\n"
      "v_pk_fma_f32 v[140:141], v[44:45], v[108:109], s[86:87]\n"
      "v_exp_f32 v142, v140 clamp\n"
      "v_exp_f32 v143, v141 clamp\n"
      "v_max_f32 v140, s86, v140\n"
      "v_max_f32 v141, s86, v141\n"
      "v_pk_fma_f32 v[140:141], v[140:141], s[88:89], v[142:143]\n"
      "v_cvt_pk_bf16_f32 v84, v140, v141\n"
      "v_pk_fma_f32 v[144:145], v[46:47], v[110:111], s[86:87]\n"
      "v_exp_f32 v146, v144 clamp\n"
      "v_exp_f32 v147, v145 clamp\n"
      "v_max_f32 v144, s86, v144\n"
      "v_max_f32 v145, s86, v145\n"
      "s_add_u32 s77, s68, 2458624\n"
      "buffer_load_dwordx4 v[156:159], v0, s[20:23], s77 offen offset:0 sc1\n"
      "v_pk_fma_f32 v[144:145], v[144:145], s[88:89], v[146:147]\n"
      "v_cvt_pk_bf16_f32 v85, v144, v145\n"
      "v_pk_fma_f32 v[140:141], v[48:49], v[112:113], s[86:87]\n"
      "v_exp_f32 v142, v140 clamp\n"
      "v_exp_f32 v143, v141 clamp\n"
      "v_max_f32 v140, s86, v140\n"
      "v_max_f32 v141, s86, v141\n"
      "v_pk_fma_f32 v[140:141], v[140:141], s[88:89], v[142:143]\n"
      "v_cvt_pk_bf16_f32 v86, v140, v141\n"
      "v_pk_fma_f32 v[144:145], v[50:51], v[114:115], s[86:87]\n"
      "v_exp_f32 v146, v144 clamp\n"
      "v_exp_f32 v147, v145 clamp\n"
      "buffer_load_dwordx4 v[160:163], v0, s[20:23], s77 offen offset:1024 sc1\n"
      "v_max_f32 v144, s86, v144\n"
      "v_max_f32 v145, s86, v145\n"
      "v_pk_fma_f32 v[144:145], v[144:145], s[88:89], v[146:147]\n"
      "v_cvt_pk_bf16_f32 v87, v144, v145\n"
      "v_pk_fma_f32 v[140:141], v[52:53], v[108:109], s[86:87]\n"
      "v_exp_f32 v142, v140 clamp\n"
      "v_exp_f32 v143, v141 clamp\n"
      "v_max_f32 v140, s86, v140\n"
      "v_max_f32 v141, s86, v141\n"
      "v_pk_fma_f32 v[140:141], v[140:141], s[88:89], v[142:143]\n"
      "v_cvt_pk_bf16_f32 v88, v140, v141\n"
      "v_pk_fma_f32 v[144:145], v[54:55], v[110:111], s[86:87]\n"
      "buffer_load_dwordx4 v[164:167], v0, s[20:23], s77 offen offset:2048 sc1\n"
      "v_exp_f32 v146, v144 clamp\n"
      "v_exp_f32 v147, v145 clamp\n"
      "v_max_f32 v144, s86, v144\n"
      "v_max_f32 v145, s86, v145\n"
      "v_pk_fma_f32 v[144:145], v[144:145], s[88:89], v[146:147]\n"
      "v_cvt_pk_bf16_f32 v89, v144, v145\n"
      "v_pk_fma_f32 v[140:141], v[56:57], v[112:113], s[86:87]\n"
      "v_exp_f32 v142, v140 clamp\n"
      "v_exp_f32 v143, v141 clamp\n"
      "v_max_f32 v140, s86, v140\n"
      "v_max_f32 v141, s86, v141\n"
      "v_pk_fma_f32 v[140:141], v[140:141], s[88:89], v[142:143]\n"
      "buffer_load_dwordx4 v[168:171], v0, s[20:23], s77 offen offset:3072 sc1\n"
      "v_cvt_pk_bf16_f32 v90, v140, v141\n"
      "v_pk_fma_f32 v[144:145], v[58:59], v[114:115], s[86:87]\n"
      "v_exp_f32 v146, v144 clamp\n"
      "v_exp_f32 v147, v145 clamp\n"
      "v_max_f32 v144, s86, v144\n"
      "v_max_f32 v145, s86, v145\n"
      "v_pk_fma_f32 v[144:145], v[144:145], s[88:89], v[146:147]\n"
      "v_cvt_pk_bf16_f32 v91, v144, v145\n"
      "v_pk_fma_f32 v[140:141], v[60:61], v[108:109], s[86:87]\n"
      "v_exp_f32 v142, v140 clamp\n"
      "v_exp_f32 v143, v141 clamp\n"
      "s_add_u32 s77, s68, 2462720\n"
      "buffer_load_dwordx4 v[172:175], v0, s[20:23], s77 offen offset:0 sc1\n"
      "v_max_f32 v140, s86, v140\n"
      "v_max_f32 v141, s86, v141\n"
      "v_pk_fma_f32 v[140:141], v[140:141], s[88:89], v[142:143]\n"
      "v_cvt_pk_bf16_f32 v92, v140, v141\n"
      "v_pk_fma_f32 v[144:145], v[62:63], v[110:111], s[86:87]\n"
      "v_exp_f32 v146, v144 clamp\n"
      "v_exp_f32 v147, v145 clamp\n"
      "v_max_f32 v144, s86, v144\n"
      "v_max_f32 v145, s86, v145\n"
      "v_pk_fma_f32 v[144:145], v[144:145], s[88:89], v[146:147]\n"
      "v_cvt_pk_bf16_f32 v93, v144, v145\n"
      "v_pk_fma_f32 v[140:141], v[64:65], v[112:113], s[86:87]\n"
      "buffer_load_dwordx4 v[176:179], v0, s[20:23], s77 offen offset:1024 sc1\n"
      "v_exp_f32 v142, v140 clamp\n"
      "v_exp_f32 v143, v141 clamp\n"
      "v_max_f32 v140, s86, v140\n"
      "v_max_f32 v141, s86, v141\n"
      "v_pk_fma_f32 v[140:141], v[140:141], s[88:89], v[142:143]\n"
      "v_cvt_pk_bf16_f32 v94, v140, v141\n"
      "v_pk_fma_f32 v[144:145], v[66:67], v[114:115], s[86:87]\n"
      "v_exp_f32 v146, v144 clamp\n"
      "v_exp_f32 v147, v145 clamp\n"
      "v_max_f32 v144, s86, v144\n"
      "v_max_f32 v145, s86, v145\n"
      "v_pk_fma_f32 v[144:145], v[144:145], s[88:89], v[146:147]\n"
      "buffer_load_dwordx4 v[180:183], v0, s[20:23], s77 offen offset:2048 sc1\n"
      "v_cvt_pk_bf16_f32 v95, v144, v145\n"
      "v_pk_mul_f32 v[148:149], v[68:69], v[108:109]\n"
      "v_cvt_pk_bf16_f32 v96, v148, v149\n"
      "v_pk_mul_f32 v[150:151], v[70:71], v[110:111]\n"
      "v_cvt_pk_bf16_f32 v97, v150, v151\n"
      "v_pk_mul_f32 v[148:149], v[72:73], v[112:113]\n"
      "v_cvt_pk_bf16_f32 v98, v148, v149\n"
      "v_pk_mul_f32 v[150:151], v[74:75], v[114:115]\n"
      "v_cvt_pk_bf16_f32 v99, v150, v151\n"
      "v_pk_mul_f32 v[148:149], v[76:77], v[108:109]\n"
      "v_cvt_pk_bf16_f32 v100, v148, v149\n"
      "v_pk_mul_f32 v[150:151], v[78:79], v[110:111]\n"
      "buffer_load_dwordx4 v[184:187], v0, s[20:23], s77 offen offset:3072 sc1\n"
      "v_cvt_pk_bf16_f32 v101, v150, v151\n"
      "v_pk_mul_f32 v[148:149], v[80:81], v[112:113]\n"
      "v_cvt_pk_bf16_f32 v102, v148, v149\n"
      "v_pk_mul_f32 v[150:151], v[82:83], v[114:115]\n"
      "v_cvt_pk_bf16_f32 v103, v150, v151\n"
      "v_pk_mul_f32 v[148:149], v[60:61], v[108:109]\n"
      "v_cvt_pk_bf16_f32 v104, v148, v149\n"
      "v_pk_mul_f32 v[150:151], v[62:63], v[110:111]\n"
      "v_cvt_pk_bf16_f32 v105, v150, v151\n"
      "v_pk_mul_f32 v[148:149], v[64:65], v[112:113]\n"
      "v_cvt_pk_bf16_f32 v106, v148, v149\n"
      "s_add_u32 s77, s68, 2466816\n"
      "buffer_load_dwordx4 v[188:191], v0, s[20:23], s77 offen offset:0 sc1\n"
      "v_pk_mul_f32 v[150:151], v[66:67], v[114:115]\n"
      "v_cvt_pk_bf16_f32 v107, v150, v151\n"
      "s_mov_b32 s90, 0xff00ff00\n"
      "s_mov_b32 s91, 0xff00ff00\n"
      "s_mov_b32 s92, 0x0f0f0f0f\n"
      "s_mov_b32 s93, 0x0f0f0f0f\n"
      "s_mov_b32 s94, 0xf0f0f0f0\n"
      "s_mov_b32 s95, 0xf0f0f0f0\n"
      "v_mov_b32 v140, 0x3f803f80\n"
      "v_mfma_f32_16x16x32_bf16 v[44:47], v[84:87], v[96:99], 0\n"
      "v_mfma_f32_16x16x32_bf16 v[48:51], v[88:91], v[100:103], 0\n"
      "v_cndmask_b32 v104, v140, v104, s[90:91]\n"
      "buffer_load_dwordx4 v[192:195], v0, s[20:23], s77 offen offset:1024 sc1\n"
      "v_cndmask_b32 v105, v140, v105, s[90:91]\n"
      "v_cndmask_b32 v106, v140, v106, s[90:91]\n"
      "v_cndmask_b32 v107, v140, v107, s[90:91]\n"
      "v_cndmask_b32 v120, 0, v104, s[92:93]\n"
      "v_cndmask_b32 v121, 0, v105, s[92:93]\n"
      "v_cndmask_b32 v122, 0, v106, s[92:93]\n"
      "v_cndmask_b32 v123, 0, v107, s[92:93]\n"
      "v_cndmask_b32 v124, 0, v104, s[94:95]\n"
      "v_cndmask_b32 v125, 0, v105, s[94:95]\n"
      "v_cndmask_b32 v126, 0, v106, s[94:95]\n"
      "v_cndmask_b32 v127, 0, v107, s[94:95]\n"
      "s_mov_b32 s90, 0x000f000f\n"
      "buffer_load_dwordx4 v[196:199], v0, s[20:23], s77 offen offset:2048 sc1\n"
      "s_mov_b32 s91, 0x000f000f\n"
      "s_mov_b32 s92, 0x00f000f0\n"
      "s_mov_b32 s93, 0x00f000f0\n"
      "v_mfma_f32_16x16x32_bf16 v[52:55], v[84:87], v[120:123], 0\n"
      "v_mfma_f32_16x16x32_bf16 v[52:55], v[88:91], v[124:127], v[52:55]\n"
      "v_cndmask_b32 v128, 0, v92, s[90:91]\n"
      "v_cndmask_b32 v129, 0, v93, s[90:91]\n"
      "v_cndmask_b32 v130, 0, v94, s[90:91]\n"
      "v_cndmask_b32 v131, 0, v95, s[90:91]\n"
      "v_cndmask_b32 v132, 0, v92, s[92:93]\n"
      "v_cndmask_b32 v133, 0, v93, s[92:93]\n"
      "v_cndmask_b32 v134, 0, v94, s[92:93]\n"
      "buffer_load_dwordx4 v[200:203], v0, s[20:23], s77 offen offset:3072 sc1\n"
      "v_cndmask_b32 v135, 0, v95, s[92:93]\n"
      "v_mov_b32_dpp v136, v92 row_shr:8 row_mask:0xf bank_mask:0xf bound_ctrl:0\n"
      "v_mov_b32_dpp v137, v93 row_shr:8 row_mask:0xf bank_mask:0xf bound_ctrl:0\n"
      "v_mov_b32_dpp v138, v94 row_shr:8 row_mask:0xf bank_mask:0xf bound_ctrl:0\n"
      "v_mov_b32_dpp v139, v95 row_shr:8 row_mask:0xf bank_mask:0xf bound_ctrl:0\n"
      "s_lshl_b32 s76, s40, 10\n"
      "v_add_u32 v140, s76, v0\n"
      "s_lshl_b32 s76, s40, 12\n"
      "v_add_u32 v141, s76, v0\n"
      "v_mfma_f32_16x16x32_bf16 v[56:59], v[128:131], v[96:99], 0\n"
      "v_mfma_f32_16x16x32_bf16 v[56:59], v[132:135], v[100:103], v[56:59]\n"
      "s_add_u32 s77, s68, 2470912\n"
      "buffer_load_dwordx4 v[204:207], v0, s[20:23], s77 offen offset:0 sc1\n"
      "v_mfma_f32_16x16x32_bf16 v[56:59], v[136:139], v[104:107], v[56:59]\n"
      "ds_write_b128 v140, v[44:47] offset:0\n"
      "ds_write_b128 v140, v[48:51] offset:4096\n"
      "ds_write_b128 v140, v[52:55] offset:8192\n"
      "s_nop 4\n"
      "ds_write_b128 v140, v[56:59] offset:12288\n"
      "s_waitcnt vmcnt(13)\n"
      "s_waitcnt lgkmcnt(0)\n"
      "s_barrier\n"
      "ds_read_b128 v[68:71], v141 offset:0\n"
      "ds_read_b128 v[72:75], v141 offset:1024\n"
      "ds_read_b128 v[76:79], v141 offset:2048\n"
      "ds_read_b128 v[80:83], v141 offset:3072\n"
      "ds_read_b128 v[84:87], v7 offset:36864\n"
      "ds_read_b128 v[88:91], v7 offset:37888\n"
      "ds_read_b128 v[92:95], v6 offset:0\n"
      "ds_read_b128 v[96:99], v7 offset:38912\n"
      "ds_read_b128 v[100:103], v7 offset:39936\n"
      "s_waitcnt lgkmcnt(7)\n"
      "v_pk_add_f32 v[68:69], v[68:69], v[72:73]\n"
      "v_pk_add_f32 v[70:71], v[70:71], v[74:75]\n"
      "s_waitcnt lgkmcnt(5)\n"
      "v_pk_add_f32 v[76:77], v[76:77], v[80:81]\n"
      "v_pk_add_f32 v[78:79], v[78:79], v[82:83]\n"
      "v_pk_add_f32 v[68:69], v[68:69], v[76:77]\n"
      "v_pk_add_f32 v[70:71], v[70:71], v[78:79]\n"
      "v_cvt_pk_bf16_f32 v68, v68, v69\n"
      "v_cvt_pk_bf16_f32 v69, v70, v71\n"
      "s_add_u32 s76, s63, 15204352\n"
      "buffer_store_dwordx2 v[68:69], v5, s[24:27], s76 offen offset:0 sc1\n"
      "s_waitcnt lgkmcnt(3)\n"
      "v_mfma_f32_16x16x32_bf16 v[44:47], v[84:87], a[0:3], 0\n"
      "ds_read_b128 v[104:107], v6 offset:512\n"
      "v_mfma_f32_16x16x32_bf16 v[48:51], v[84:87], a[80:83], 0\n"
      "v_mfma_f32_16x16x32_bf16 v[52:55], v[88:91], a[0:3], 0\n"
      "ds_read_b128 v[84:87], v7 offset:40960\n"
      "v_mfma_f32_16x16x32_bf16 v[56:59], v[88:91], a[80:83], 0\n"
      "s_waitcnt lgkmcnt(3)\n"
      "v_mfma_f32_16x16x32_bf16 v[60:63], v[92:95], a[0:3], 0\n"
      "ds_read_b128 v[88:91], v7 offset:41984\n"
      "buffer_load_dwordx4 v[208:211], v0, s[20:23], s77 offen offset:1024 sc1\n"
      "v_mfma_f32_16x16x32_bf16 v[64:67], v[92:95], a[80:83], 0\n"
      "v_mfma_f32_16x16x32_bf16 v[44:47], v[96:99], a[4:7], v[44:47]\n"
      "ds_read_b128 v[92:95], v6 offset:1024\n"
      "v_mfma_f32_16x16x32_bf16 v[48:51], v[96:99], a[84:87], v[48:51]\n"
      "s_waitcnt lgkmcnt(3)\n"
      "v_mfma_f32_16x16x32_bf16 v[52:55], v[100:103], a[4:7], v[52:55]\n"
      "ds_read_b128 v[96:99], v7 offset:43008\n"
      "v_mfma_f32_16x16x32_bf16 v[56:59], v[100:103], a[84:87], v[56:59]\n"
      "buffer_load_dwordx4 v[212:215], v0, s[20:23], s77 offen offset:2048 sc1\n"
      "v_mfma_f32_16x16x32_bf16 v[60:63], v[104:107], a[4:7], v[60:63]\n"
      "ds_read_b128 v[100:103], v7 offset:44032\n"
      "v_mfma_f32_16x16x32_bf16 v[64:67], v[104:107], a[84:87], v[64:67]\n"
      "s_waitcnt lgkmcnt(3)\n"
      "v_mfma_f32_16x16x32_bf16 v[44:47], v[84:87], a[8:11], v[44:47]\n"
      "ds_read_b128 v[104:107], v6 offset:1536\n"
      "v_mfma_f32_16x16x32_bf16 v[48:51], v[84:87], a[88:91], v[48:51]\n"
      "buffer_load_dwordx4 v[216:219], v0, s[20:23], s77 offen offset:3072 sc1\n"
      "v_mfma_f32_16x16x32_bf16 v[52:55], v[88:91], a[8:11], v[52:55]\n"
      "ds_read_b128 v[84:87], v7 offset:45056\n"
      "v_mfma_f32_16x16x32_bf16 v[56:59], v[88:91], a[88:91], v[56:59]\n"
      "s_waitcnt lgkmcnt(3)\n"
      "v_mfma_f32_16x16x32_bf16 v[60:63], v[92:95], a[8:11], v[60:63]\n"
      "ds_read_b128 v[88:91], v7 offset:46080\n"
      "v_mfma_f32_16x16x32_bf16 v[64:67], v[92:95], a[88:91], v[64:67]\n"
      "v_mfma_f32_16x16x32_bf16 v[44:47], v[96:99], a[12:15], v[44:47]\n"
      "ds_read_b128 v[92:95], v6 offset:2048\n"
      "s_add_u32 s77, s68, 2475008\n"
      "buffer_load_dwordx4 v[220:223], v0, s[20:23], s77 offen offset:0 sc1\n"
      "v_mfma_f32_16x16x32_bf16 v[48:51], v[96:99], a[92:95], v[48:51]\n"
      "s_waitcnt lgkmcnt(3)\n"
      "v_mfma_f32_16x16x32_bf16 v[52:55], v[100:103], a[12:15], v[52:55]\n"
      "ds_read_b128 v[96:99], v7 offset:47104\n"
      "v_mfma_f32_16x16x32_bf16 v[56:59], v[100:103], a[92:95], v[56:59]\n"
      "v_mfma_f32_16x16x32_bf16 v[60:63], v[104:107], a[12:15], v[60:63]\n"
      "ds_read_b128 v[100:103], v7 offset:48128\n"
      "buffer_load_dwordx4 v[224:227], v0, s[20:23], s77 offen offset:1024 sc1\n"
      "v_mfma_f32_16x16x32_bf16 v[64:67], v[104:107], a[92:95], v[64:67]\n"
      "s_waitcnt lgkmcnt(3)\n"
      "v_mfma_f32_16x16x32_bf16 v[44:47], v[84:87], a[16:19], v[44:47]\n"
      "ds_read_b128 v[104:107], v6 offset:2560\n"
      "v_mfma_f32_16x16x32_bf16 v[48:51], v[84:87], a[96:99], v[48:51]\n"
      "v_mfma_f32_16x16x32_bf16 v[52:55], v[88:91], a[16:19], v[52:55]\n"
      "ds_read_b128 v[84:87], v7 offset:49152\n"
      "v_mfma_f32_16x16x32_bf16 v[56:59], v[88:91], a[96:99], v[56:59]\n"
      "buffer_load_dwordx4 v[228:231], v0, s[20:23], s77 offen offset:2048 sc1\n"
      "s_waitcnt lgkmcnt(3)\n"
      "v_mfma_f32_16x16x32_bf16 v[60:63], v[92:95], a[16:19], v[60:63]\n"
      "ds_read_b128 v[88:91], v7 offset:50176\n"
      "v_mfma_f32_16x16x32_bf16 v[64:67], v[92:95], a[96:99], v[64:67]\n"
      "v_mfma_f32_16x16x32_bf16 v[44:47], v[96:99], a[20:23], v[44:47]\n"
      "ds_read_b128 v[92:95], v6 offset:3072\n"
      "v_mfma_f32_16x16x32_bf16 v[48:51], v[96:99], a[100:103], v[48:51]\n"
      "buffer_load_dwordx4 v[232:235], v0, s[20:23], s77 offen offset:3072 sc1\n"
      "s_waitcnt lgkmcnt(3)\n"
      "v_mfma_f32_16x16x32_bf16 v[52:55], v[100:103], a[20:23], v[52:55]\n"
      "ds_read_b128 v[96:99], v7 offset:51200\n"
      "v_mfma_f32_16x16x32_bf16 v[56:59], v[100:103], a[100:103], v[56:59]\n"
      "v_mfma_f32_16x16x32_bf16 v[60:63], v[104:107], a[20:23], v[60:63]\n"
      "ds_read_b128 v[100:103], v7 offset:52224\n"
      "v_mfma_f32_16x16x32_bf16 v[64:67], v[104:107], a[100:103], v[64:67]\n"
      "s_waitcnt lgkmcnt(3)\n"
      "v_mfma_f32_16x16x32_bf16 v[44:47], v[84:87], a[24:27], v[44:47]\n"
      "ds_read_b128 v[104:107], v6 offset:3584\n"
      "s_add_u32 s77, s68, 2479104\n"
      "buffer_load_dwordx4 v[236:239], v0, s[20:23], s77 offen offset:0 sc1\n"
      "v_mfma_f32_16x16x32_bf16 v[48:51], v[84:87], a[104:107], v[48:51]\n"
      "v_mfma_f32_16x16x32_bf16 v[52:55], v[88:91], a[24:27], v[52:55]\n"
      "ds_read_b128 v[84:87], v7 offset:53248\n"
      "v_mfma_f32_16x16x32_bf16 v[56:59], v[88:91], a[104:107], v[56:59]\n"
      "s_waitcnt lgkmcnt(3)\n"
      "v_mfma_f32_16x16x32_bf16 v[60:63], v[92:95], a[24:27], v[60:63]\n"
      "ds_read_b128 v[88:91], v7 offset:54272\n"
      "buffer_load_dwordx4 v[240:243], v0, s[20:23], s77 offen offset:1024 sc1\n"
      "v_mfma_f32_16x16x32_bf16 v[64:67], v[92:95], a[104:107], v[64:67]\n"
      "v_mfma_f32_16x16x32_bf16 v[44:47], v[96:99], a[28:31], v[44:47]\n"
      "ds_read_b128 v[92:95], v6 offset:4096\n"
      "v_mfma_f32_16x16x32_bf16 v[48:51], v[96:99], a[108:111], v[48:51]\n"
      "s_waitcnt lgkmcnt(3)\n"
      "v_mfma_f32_16x16x32_bf16 v[52:55], v[100:103], a[28:31], v[52:55]\n"
      "ds_read_b128 v[96:99], v7 offset:55296\n"
      "s_waitcnt vmcnt(9)\n"
      "s_lshr_b32 s80, s73, 5\n"
      "s_add_u32 s80, s80, s74\n"
      "s_barrier\n"
      "s_cmp_lg_u32 s40, 0\n"
      "s_cbranch_scc1 Lsg2_%=\n"
      "s_add_u32 s80, s80, 15998976\n"
      "s_mov_b64 exec, 1\n"
      "buffer_store_dword v10, v9, s[24:27], s80 offen sc1\n"
      "s_mov_b64 exec, -1\n"
      "Lsg2_%=:\n"
      "v_mfma_f32_16x16x32_bf16 v[56:59], v[100:103], a[108:111], v[56:59]\n"
      "buffer_load_dwordx4 v[244:247], v0, s[20:23], s77 offen offset:2048 sc1\n"
      "v_mfma_f32_16x16x32_bf16 v[60:63], v[104:107], a[28:31], v[60:63]\n"
      "ds_read_b128 v[100:103], v7 offset:56320\n"
      "v_mfma_f32_16x16x32_bf16 v[64:67], v[104:107], a[108:111], v[64:67]\n"
      "s_waitcnt lgkmcnt(3)\n"
      "v_mfma_f32_16x16x32_bf16 v[44:47], v[84:87], a[32:35], v[44:47]\n"
      "ds_read_b128 v[104:107], v6 offset:4608\n"
      "v_mfma_f32_16x16x32_bf16 v[48:51], v[84:87], a[112:115], v[48:51]\n"
      "buffer_load_dwordx4 v[248:251], v0, s[20:23], s77 offen offset:3072 sc1\n"
      "v_mfma_f32_16x16x32_bf16 v[52:55], v[88:91], a[32:35], v[52:55]\n"
      "ds_read_b128 v[84:87], v7 offset:57344\n"
      "v_mfma_f32_16x16x32_bf16 v[56:59], v[88:91], a[112:115], v[56:59]\n"
      "s_waitcnt lgkmcnt(3)\n"
      "v_mfma_f32_16x16x32_bf16 v[60:63], v[92:95], a[32:35], v[60:63]\n"
      "ds_read_b128 v[88:91], v7 offset:58368\n"
      "v_mfma_f32_16x16x32_bf16 v[64:67], v[92:95], a[112:115], v[64:67]\n"
      "v_mfma_f32_16x16x32_bf16 v[44:47], v[96:99], a[36:39], v[44:47]\n"
      "ds_read_b128 v[92:95], v6 offset:5120\n"
      "s_add_u32 s77, s68, 2483200\n"
      "buffer_load_dwordx4 v[252:255], v0, s[20:23], s77 offen offset:0 sc1\n"
      "v_mfma_f32_16x16x32_bf16 v[48:51], v[96:99], a[116:119], v[48:51]\n"
      "s_waitcnt lgkmcnt(3)\n"
      "v_mfma_f32_16x16x32_bf16 v[52:55], v[100:103], a[36:39], v[52:55]\n"
      "ds_read_b128 v[96:99], v7 offset:59392\n"
      "v_mfma_f32_16x16x32_bf16 v[56:59], v[100:103], a[116:119], v[56:59]\n"
      "v_mfma_f32_16x16x32_bf16 v[60:63], v[104:107], a[36:39], v[60:63]\n"
      "ds_read_b128 v[100:103], v7 offset:60416\n"
      "v_mfma_f32_16x16x32_bf16 v[64:67], v[104:107], a[116:119], v[64:67]\n"
      "s_add_u32 s79, s67, 3277824\n"
      "s_add_u32 m0, s72, 61440\n"
      "s_nop 0\n"
      "buffer_load_dwordx4 v0, s[20:23], s79 offen offset:0 sc1 lds\n"
      "s_waitcnt lgkmcnt(3)\n"
      "v_mfma_f32_16x16x32_bf16 v[44:47], v[84:87], a[40:43], v[44:47]\n"
      "ds_read_b128 v[104:107], v6 offset:5632\n"
      "v_mfma_f32_16x16x32_bf16 v[48:51], v[84:87], a[120:123], v[48:51]\n"
      "v_mfma_f32_16x16x32_bf16 v[52:55], v[88:91], a[40:43], v[52:55]\n"
      "ds_read_b128 v[84:87], v7 offset:61440\n"
      "v_mfma_f32_16x16x32_bf16 v[56:59], v[88:91], a[120:123], v[56:59]\n"
      "buffer_load_dwordx4 v0, s[20:23], s79 offen offset:1024 sc1 lds\n"
      "s_waitcnt lgkmcnt(3)\n"
      "v_mfma_f32_16x16x32_bf16 v[60:63], v[92:95], a[40:43], v[60:63]\n"
      "ds_read_b128 v[88:91], v7 offset:62464\n"
      "v_mfma_f32_16x16x32_bf16 v[64:67], v[92:95], a[120:123], v[64:67]\n"
      "v_mfma_f32_16x16x32_bf16 v[44:47], v[96:99], a[44:47], v[44:47]\n"
      "ds_read_b128 v[92:95], v6 offset:6144\n"
      "v_mfma_f32_16x16x32_bf16 v[48:51], v[96:99], a[124:127], v[48:51]\n"
      "s_waitcnt lgkmcnt(3)\n"
      "v_mfma_f32_16x16x32_bf16 v[52:55], v[100:103], a[44:47], v[52:55]\n"
      "ds_read_b128 v[96:99], v7 offset:63488\n"
      "buffer_load_dwordx4 v0, s[20:23], s79 offen offset:2048 sc1 lds\n"
      "v_mfma_f32_16x16x32_bf16 v[56:59], v[100:103], a[124:127], v[56:59]\n"
      "v_mfma_f32_16x16x32_bf16 v[60:63], v[104:107], a[44:47], v[60:63]\n"
      "ds_read_b128 v[100:103], v7 offset:64512\n"
      "v_mfma_f32_16x16x32_bf16 v[64:67], v[104:107], a[124:127], v[64:67]\n"
      "s_waitcnt lgkmcnt(3)\n"
      "v_mfma_f32_16x16x32_bf16 v[44:47], v[84:87], a[48:51], v[44:47]\n"
      "ds_read_b128 v[104:107], v6 offset:6656\n"
      "buffer_load_dwordx4 v0, s[20:23], s79 offen offset:3072 sc1 lds\n"
      "v_mfma_f32_16x16x32_bf16 v[48:51], v[84:87], a[128:131], v[48:51]\n"
      "v_mfma_f32_16x16x32_bf16 v[52:55], v[88:91], a[48:51], v[52:55]\n"
      "ds_read_b128 v[84:87], v8 offset:0\n"
      "v_mfma_f32_16x16x32_bf16 v[56:59], v[88:91], a[128:131], v[56:59]\n"
      "s_waitcnt lgkmcnt(3)\n"
      "v_mfma_f32_16x16x32_bf16 v[60:63], v[92:95], a[48:51], v[60:63]\n"
      "ds_read_b128 v[88:91], v8 offset:1024\n"
      "v_mfma_f32_16x16x32_bf16 v[64:67], v[92:95], a[128:131], v[64:67]\n"
      "s_add_u32 s79, s67, 3281920\n"
      "s_add_u32 m0, s72, 65536\n"
      "s_nop 0\n"
      "buffer_load_dwordx4 v0, s[20:23], s79 offen offset:0 sc1 lds\n"
      "v_mfma_f32_16x16x32_bf16 v[44:47], v[96:99], a[52:55], v[44:47]\n"
      "ds_read_b128 v[92:95], v6 offset:7168\n"
      "v_mfma_f32_16x16x32_bf16 v[48:51], v[96:99], a[132:135], v[48:51]\n"
      "s_waitcnt lgkmcnt(3)\n"
      "v_mfma_f32_16x16x32_bf16 v[52:55], v[100:103], a[52:55], v[52:55]\n"
      "ds_read_b128 v[96:99], v8 offset:2048\n"
      "v_mfma_f32_16x16x32_bf16 v[56:59], v[100:103], a[132:135], v[56:59]\n"
      "s_add_u32 s79, s67, 3318784\n"
      "s_add_u32 m0, s72, 81920\n"
      "s_nop 0\n"
      "buffer_load_dwordx4 v0, s[20:23], s79 offen offset:0 sc1 lds\n"
      "v_mfma_f32_16x16x32_bf16 v[60:63], v[104:107], a[52:55], v[60:63]\n"
      "ds_read_b128 v[100:103], v8 offset:3072\n"
      "v_mfma_f32_16x16x32_bf16 v[64:67], v[104:107], a[132:135], v[64:67]\n"
      "s_waitcnt lgkmcnt(3)\n"
      "v_mfma_f32_16x16x32_bf16 v[44:47], v[84:87], a[56:59], v[44:47]\n"
      "ds_read_b128 v[104:107], v6 offset:7680\n"
      "v_mfma_f32_16x16x32_bf16 v[48:51], v[84:87], a[136:139], v[48:51]\n"
      "v_mfma_f32_16x16x32_bf16 v[52:55], v[88:91], a[56:59], v[52:55]\n"
      "ds_read_b128 v[84:87], v8 offset:4096\n"
      "buffer_load_dwordx4 v0, s[20:23], s79 offen offset:1024 sc1 lds\n"
      "v_mfma_f32_16x16x32_bf16 v[56:59], v[88:91], a[136:139], v[56:59]\n"
      "s_waitcnt lgkmcnt(3)\n"
      "v_mfma_f32_16x16x32_bf16 v[60:63], v[92:95], a[56:59], v[60:63]\n"
      "ds_read_b128 v[88:91], v8 offset:5120\n"
      "v_mfma_f32_16x16x32_bf16 v[64:67], v[92:95], a[136:139], v[64:67]\n"
      "v_mfma_f32_16x16x32_bf16 v[44:47], v[96:99], a[60:63], v[44:47]\n"
      "ds_read_b128 v[92:95], v6 offset:8192\n"
      "buffer_load_dwordx4 v0, s[20:23], s79 offen offset:2048 sc1 lds\n"
      "v_mov_b32 v152, v0\n"
      "s_add_u32 s98, s74, 15998976\n"
      "s_mov_b64 exec, 0xffff\n"
      "buffer_load_dword v152, v152, s[24:27], s98 offen sc1\n"
      "s_mov_b64 exec, -1\n"
      "v_mfma_f32_16x16x32_bf16 v[48:51], v[96:99], a[140:143], v[48:51]\n"
      "s_waitcnt lgkmcnt(3)\n"
      "v_mfma_f32_16x16x32_bf16 v[52:55], v[100:103], a[60:63], v[52:55]\n"
      "ds_read_b128 v[96:99], v8 offset:6144\n"
      "v_mfma_f32_16x16x32_bf16 v[56:59], v[100:103], a[140:143], v[56:59]\n"
      "v_mfma_f32_16x16x32_bf16 v[60:63], v[104:107], a[60:63], v[60:63]\n"
      "ds_read_b128 v[100:103], v8 offset:7168\n"
      "v_mfma_f32_16x16x32_bf16 v[64:67], v[104:107], a[140:143], v[64:67]\n"
      "buffer_load_dwordx4 v0, s[20:23], s79 offen offset:3072 sc1 lds\n"
      "s_waitcnt lgkmcnt(3)\n"
      "v_mfma_f32_16x16x32_bf16 v[44:47], v[84:87], a[64:67], v[44:47]\n"
      "ds_read_b128 v[104:107], v6 offset:8704\n"
      "v_mfma_f32_16x16x32_bf16 v[48:51], v[84:87], a[144:147], v[48:51]\n"
      "v_mfma_f32_16x16x32_bf16 v[52:55], v[88:91], a[64:67], v[52:55]\n"
      "ds_read_b128 v[84:87], v8 offset:8192\n"
      "v_mfma_f32_16x16x32_bf16 v[56:59], v[88:91], a[144:147], v[56:59]\n"
      "s_add_u32 s79, s67, 3322880\n"
      "s_add_u32 m0, s72, 86016\n"
      "s_nop 0\n"
      "buffer_load_dwordx4 v0, s[20:23], s79 offen offset:0 sc1 lds\n"
      "s_waitcnt lgkmcnt(3)\n"
      "v_mfma_f32_16x16x32_bf16 v[60:63], v[92:95], a[64:67], v[60:63]\n"
      "ds_read_b128 v[88:91], v8 offset:9216\n"
      "v_mfma_f32_16x16x32_bf16 v[64:67], v[92:95], a[144:147], v[64:67]\n"
      "v_mfma_f32_16x16x32_bf16 v[44:47], v[96:99], a[68:71], v[44:47]\n"
      "ds_read_b128 v[92:95], v6 offset:9216\n"
      "v_mfma_f32_16x16x32_bf16 v[48:51], v[96:99], a[148:151], v[48:51]\n"
      "s_waitcnt lgkmcnt(3)\n"
      "v_mfma_f32_16x16x32_bf16 v[52:55], v[100:103], a[68:71], v[52:55]\n"
      "ds_read_b128 v[96:99], v8 offset:10240\n"
      "s_add_u32 s78, s69, 7865344\n"
      "buffer_load_dwordx4 a[160:163], v0, s[20:23], s78 offen offset:0 sc1\n"
      "v_mfma_f32_16x16x32_bf16 v[56:59], v[100:103], a[148:151], v[56:59]\n"
      "v_mfma_f32_16x16x32_bf16 v[60:63], v[104:107], a[68:71], v[60:63]\n"
      "ds_read_b128 v[100:103], v8 offset:11264\n"
      "v_mfma_f32_16x16x32_bf16 v[64:67], v[104:107], a[148:151], v[64:67]\n"
      "s_waitcnt lgkmcnt(3)\n"
      "v_mfma_f32_16x16x32_bf16 v[44:47], v[84:87], a[72:75], v[44:47]\n"
      "ds_read_b128 v[104:107], v6 offset:9728\n"
      "v_mfma_f32_16x16x32_bf16 v[48:51], v[84:87], a[152:155], v[48:51]\n"
      "buffer_load_dwordx4 a[164:167], v0, s[20:23], s78 offen offset:1024 sc1\n"
      "v_mfma_f32_16x16x32_bf16 v[52:55], v[88:91], a[72:75], v[52:55]\n"
      "v_mfma_f32_16x16x32_bf16 v[56:59], v[88:91], a[152:155], v[56:59]\n"
      "s_waitcnt lgkmcnt(2)\n"
      "v_mfma_f32_16x16x32_bf16 v[60:63], v[92:95], a[72:75], v[60:63]\n"
      "v_mfma_f32_16x16x32_bf16 v[64:67], v[92:95], a[152:155], v[64:67]\n"
      "buffer_load_dwordx4 a[168:171], v0, s[20:23], s78 offen offset:2048 sc1\n"
      "v_mfma_f32_16x16x32_bf16 v[44:47], v[96:99], a[76:79], v[44:47]\n"
      "v_mfma_f32_16x16x32_bf16 v[48:51], v[96:99], a[156:159], v[48:51]\n"
      "s_waitcnt lgkmcnt(0)\n"
      "v_mfma_f32_16x16x32_bf16 v[52:55], v[100:103], a[76:79], v[52:55]\n"
      "v_mfma_f32_16x16x32_bf16 v[56:59], v[100:103], a[156:159], v[56:59]\n"
      "v_mfma_f32_16x16x32_bf16 v[60:63], v[104:107], a[76:79], v[60:63]\n"
      "buffer_load_dwordx4 a[172:175], v0, s[20:23], s78 offen offset:3072 sc1\n"
      "v_mfma_f32_16x16x32_bf16 v[64:67], v[104:107], a[156:159], v[64:67]\n"
      "v_and_b32 v153, 0x300, v0\n"
      "v_add_u32 v154, v153, v0\n"
      "v_pk_fma_f32 v[140:141], v[44:45], v[116:117], s[86:87] op_sel_hi:[1,0,1]\n"
      "v_exp_f32 v142, v140 clamp\n"
      "v_exp_f32 v143, v141 clamp\n"
      "v_max_f32 v140, s86, v140\n"
      "v_max_f32 v141, s86, v141\n"
      "v_pk_fma_f32 v[140:141], v[140:141], s[88:89], v[142:143]\n"
      "v_cvt_pk_bf16_f32 v68, v140, v141\n"
      "v_pk_fma_f32 v[144:145], v[46:47], v[116:117], s[86:87] op_sel_hi:[1,0,1]\n"
      "v_exp_f32 v146, v144 clamp\n"
      "v_exp_f32 v147, v145 clamp\n"
      "v_max_f32 v144, s86, v144\n"
      "s_waitcnt vmcnt(6)\n"
      "v_cmp_eq_u32 vcc, 0, v152\n"
      "s_cbranch_vccz Lpd3_%=\n"
      "s_add_u32 s80, s74, 15998976\n"
      "s_mov_b32 s84, 400000\n"
      "v_mov_b32 v152, v0\n"
      "s_mov_b64 exec, 0xffff\n"
      "Lpl3_%=:\n"
      "buffer_load_dword v11, v152, s[24:27], s80 offen sc1\n"
      "s_waitcnt vmcnt(0)\n"
      "v_cmp_eq_u32 vcc, 0, v11\n"
      "s_cbranch_vccz Lpx3_%=\n"
      "s_sub_u32 s84, s84, 1\n"
      "s_cmp_lg_u32 s84, 0\n"
      "s_cbranch_scc1 Lpl3_%=\n"
      "Lpx3_%=:\n"
      "s_mov_b64 exec, -1\n"
      "Lpd3_%=:\n"
      "s_add_u32 s76, s64, 15204352\n"
      "buffer_load_dwordx4 v[84:87], v154, s[24:27], s76 offen offset:0 sc1\n"
      "buffer_load_dwordx4 v[88:91], v154, s[24:27], s76 offen offset:256 sc1\n"
      "buffer_load_dwordx4 v[92:95], v154, s[24:27], s76 offen offset:2048 sc1\n"
      "buffer_load_dwordx4 v[96:99], v154, s[24:27], s76 offen offset:2304 sc1\n"
      "s_add_u32 s76, s64, 15208448\n"
      "buffer_load_dwordx4 v[100:103], v154, s[24:27], s76 offen offset:0 sc1\n"
      "buffer_load_dwordx4 v[104:107], v154, s[24:27], s76 offen offset:256 sc1\n"
      "buffer_load_dwordx4 v[108:111], v154, s[24:27], s76 offen offset:2048 sc1\n"
      "buffer_load_dwordx4 v[112:115], v154, s[24:27], s76 offen offset:2304 sc1\n"
      "v_max_f32 v145, s86, v145\n"
      "v_pk_fma_f32 v[144:145], v[144:145], s[88:89], v[146:147]\n"
      "v_cvt_pk_bf16_f32 v69, v144, v145\n"
      "v_pk_fma_f32 v[140:141], v[52:53], v[116:117], s[86:87] op_sel_hi:[1,0,1]\n"
      "v_exp_f32 v142, v140 clamp\n"
      "v_exp_f32 v143, v141 clamp\n"
      "v_max_f32 v140, s86, v140\n"
      "v_max_f32 v141, s86, v141\n"
      "v_pk_fma_f32 v[140:141], v[140:141], s[88:89], v[142:143]\n"
      "s_add_u32 s78, s69, 7869440\n"
      "buffer_load_dwordx4 a[176:179], v0, s[20:23], s78 offen offset:0 sc1\n"
      "v_cvt_pk_bf16_f32 v72, v140, v141\n"
      "v_pk_fma_f32 v[144:145], v[54:55], v[116:117], s[86:87] op_sel_hi:[1,0,1]\n"
      "v_exp_f32 v146, v144 clamp\n"
      "v_exp_f32 v147, v145 clamp\n"
      "v_max_f32 v144, s86, v144\n"
      "v_max_f32 v145, s86, v145\n"
      "v_pk_fma_f32 v[144:145], v[144:145], s[88:89], v[146:147]\n"
      "v_cvt_pk_bf16_f32 v73, v144, v145\n"
      "buffer_load_dwordx4 a[180:183], v0, s[20:23], s78 offen offset:1024 sc1\n"
      "v_pk_fma_f32 v[140:141], v[60:61], v[116:117], s[86:87] op_sel_hi:[1,0,1]\n"
      "v_exp_f32 v142, v140 clamp\n"
      "v_exp_f32 v143, v141 clamp\n"
      "v_max_f32 v140, s86, v140\n"
      "v_max_f32 v141, s86, v141\n"
      "v_pk_fma_f32 v[140:141], v[140:141], s[88:89], v[142:143]\n"
      "v_cvt_pk_bf16_f32 v70, v140, v141\n"
      "v_cvt_pk_bf16_f32 v74, v140, v141\n"
      "buffer_load_dwordx4 a[184:187], v0, s[20:23], s78 offen offset:2048 sc1\n"
      "v_pk_fma_f32 v[144:145], v[62:63], v[116:117], s[86:87] op_sel_hi:[1,0,1]\n"
      "v_exp_f32 v146, v144 clamp\n"
      "v_exp_f32 v147, v145 clamp\n"
      "v_max_f32 v144, s86, v144\n"
      "v_max_f32 v145, s86, v145\n"
      "v_pk_fma_f32 v[144:145], v[144:145], s[88:89], v[146:147]\n"
      "v_cvt_pk_bf16_f32 v71, v144, v145\n"
      "v_cvt_pk_bf16_f32 v75, v144, v145\n"
      "v_pk_fma_f32 v[140:141], v[48:49], v[118:119], s[86:87] op_sel_hi:[1,0,1]\n"
      "buffer_load_dwordx4 a[188:191], v0, s[20:23], s78 offen offset:3072 sc1\n"
      "v_exp_f32 v142, v140 clamp\n"
      "v_exp_f32 v143, v141 clamp\n"
      "v_max_f32 v140, s86, v140\n"
      "v_max_f32 v141, s86, v141\n"
      "v_pk_fma_f32 v[140:141], v[140:141], s[88:89], v[142:143]\n"
      "v_cvt_pk_bf16_f32 v76, v140, v141\n"
      "v_pk_fma_f32 v[144:145], v[50:51], v[118:119], s[86:87] op_sel_hi:[1,0,1]\n"
      "v_exp_f32 v146, v144 clamp\n"
      "v_exp_f32 v147, v145 clamp\n"
      "v_max_f32 v144, s86, v144\n"
      "v_max_f32 v145, s86, v145\n"
      "v_pk_fma_f32 v[144:145], v[144:145], s[88:89], v[146:147]\n"
      "v_cvt_pk_bf16_f32 v77, v144, v145\n"
      "v_pk_fma_f32 v[140:141], v[56:57], v[118:119], s[86:87] op_sel_hi:[1,0,1]\n"
      "v_exp_f32 v142, v140 clamp\n"
      "v_exp_f32 v143, v141 clamp\n"
      "v_max_f32 v140, s86, v140\n"
      "v_max_f32 v141, s86, v141\n"
      "v_pk_fma_f32 v[140:141], v[140:141], s[88:89], v[142:143]\n"
      "v_cvt_pk_bf16_f32 v80, v140, v141\n"
      "v_pk_fma_f32 v[144:145], v[58:59], v[118:119], s[86:87] op_sel_hi:[1,0,1]\n"
      "v_exp_f32 v146, v144 clamp\n"
      "v_exp_f32 v147, v145 clamp\n"
      "v_max_f32 v144, s86, v144\n"
      "v_max_f32 v145, s86, v145\n"
      "v_pk_fma_f32 v[144:145], v[144:145], s[88:89], v[146:147]\n"
      "v_cvt_pk_bf16_f32 v81, v144, v145\n"
      "v_pk_fma_f32 v[140:141], v[64:65], v[118:119], s[86:87] op_sel_hi:[1,0,1]\n"
      "v_exp_f32 v142, v140 clamp\n"
      "v_exp_f32 v143, v141 clamp\n"
      "v_max_f32 v140, s86, v140\n"
      "v_max_f32 v141, s86, v141\n"
      "v_pk_fma_f32 v[140:141], v[140:141], s[88:89], v[142:143]\n"
      "v_cvt_pk_bf16_f32 v78, v140, v141\n"
      "v_cvt_pk_bf16_f32 v82, v140, v141\n"
      "v_pk_fma_f32 v[144:145], v[66:67], v[118:119], s[86:87] op_sel_hi:[1,0,1]\n"
      "v_exp_f32 v146, v144 clamp\n"
      "v_exp_f32 v147, v145 clamp\n"
      "v_max_f32 v144, s86, v144\n"
      "v_max_f32 v145, s86, v145\n"
      "v_pk_fma_f32 v[144:145], v[144:145], s[88:89], v[146:147]\n"
      "v_cvt_pk_bf16_f32 v79, v144, v145\n"
      "v_cvt_pk_bf16_f32 v83, v144, v145\n"
      "s_mov_b32 s90, 0xaaaaaaaa\n"
      "s_mov_b32 s91, 0xaaaaaaaa\n"
      "v_mov_b32 v140, 0x3f80\n"
      "v_mov_b32 v141, 0x3f800000\n"
      "v_cndmask_b32 v140, v140, v141, s[90:91]\n"
      "s_mov_b32 s92, 0x00030003\n"
      "s_mov_b32 s93, 0x00030003\n"
      "v_cndmask_b32 v120, 0, v140, s[92:93]\n"
      "s_mov_b32 s94, 0x000c000c\n"
      "s_mov_b32 s95, 0x000c000c\n"
      "v_cndmask_b32 v121, 0, v140, s[94:95]\n"
      "s_mov_b32 s92, 0x00300030\n"
      "s_mov_b32 s93, 0x00300030\n"
      "v_cndmask_b32 v122, 0, v140, s[92:93]\n"
      "s_mov_b32 s94, 0x00c000c0\n"
      "s_mov_b32 s95, 0x00c000c0\n"
      "v_cndmask_b32 v123, 0, v140, s[94:95]\n"
      "s_mov_b32 s92, 0x03000300\n"
      "s_mov_b32 s93, 0x03000300\n"
      "v_cndmask_b32 v124, 0, v140, s[92:93]\n"
      "s_mov_b32 s94, 0x0c000c00\n"
      "s_mov_b32 s95, 0x0c000c00\n"
      "v_cndmask_b32 v125, 0, v140, s[94:95]\n"
      "s_mov_b32 s92, 0x30003000\n"
      "s_mov_b32 s93, 0x30003000\n"
      "v_cndmask_b32 v126, 0, v140, s[92:93]\n"
      "s_mov_b32 s94, 0xc000c000\n"
      "s_mov_b32 s95, 0xc000c000\n"
      "v_cndmask_b32 v127, 0, v140, s[94:95]\n"
      "v_lshrrev_b32 v143, 1, v0\n"
      "v_lshrrev_b32 v141, 1, v153\n"
      "s_lshl_b32 s75, s40, 9\n"
      "v_add_u32 v144, s75, v143\n"
      "s_waitcnt vmcnt(11)\n"
      "v_mfma_f32_16x16x32_bf16 v[44:47], v[120:123], v[84:87], 0\n"
      "s_waitcnt vmcnt(10)\n"
      "v_mfma_f32_16x16x32_bf16 v[44:47], v[124:127], v[88:91], v[44:47]\n"
      "s_waitcnt vmcnt(9)\n"
      "v_mfma_f32_16x16x32_bf16 v[44:47], v[120:123], v[92:95], v[44:47]\n"
      "s_waitcnt vmcnt(8)\n"
      "v_mfma_f32_16x16x32_bf16 v[44:47], v[124:127], v[96:99], v[44:47]\n"
      "s_waitcnt vmcnt(7)\n"
      "v_mfma_f32_16x16x32_bf16 v[44:47], v[120:123], v[100:103], v[44:47]\n"
      "s_waitcnt vmcnt(6)\n"
      "v_mfma_f32_16x16x32_bf16 v[44:47], v[124:127], v[104:107], v[44:47]\n"
      "s_waitcnt vmcnt(5)\n"
      "v_mfma_f32_16x16x32_bf16 v[44:47], v[120:123], v[108:111], v[44:47]\n"
      "s_waitcnt vmcnt(4)\n"
      "v_mfma_f32_16x16x32_bf16 v[44:47], v[124:127], v[112:115], v[44:47]\n"
      "s_mov_b32 s90, 0x0000ffff\n"
      "s_mov_b32 s91, 0x00000000\n"
      "s_mov_b32 s92, 0xffff0000\n"
      "s_mov_b32 s93, 0x00000000\n"
      "s_mov_b32 s94, 0x0f0f0f0f\n"
      "s_mov_b32 s95, 0x0f0f0f0f\n"
      "s_mov_b32 s96, 0xf0f0f0f0\n"
      "s_mov_b32 s97, 0xf0f0f0f0\n"
      "v_cvt_pk_bf16_f32 v44, v44, v45\n"
      "v_cvt_pk_bf16_f32 v45, v46, v47\n"
      "ds_write_b64 v144, v[44:45] offset:16384\n"
      "s_waitcnt lgkmcnt(0)\n"
      "s_barrier\n"
      "ds_read_b64 v[110:111], v143 offset:17920\n"
      "ds_read_b64 v[84:85], v143 offset:16384\n"
      "ds_read_b64 v[88:89], v143 offset:16896\n"
      "ds_read_b64 v[112:113], v141 offset:17920\n"
      "ds_read_b64 v[114:115], v141 offset:17952\n"
      "ds_read_b64 v[100:101], v141 offset:17408\n"
      "ds_read_b64 v[104:105], v141 offset:17440\n"
      "ds_read_b64 v[108:109], v143 offset:17408\n"
      "s_waitcnt lgkmcnt(7)\n"
      "v_cndmask_b32 v86, 0, v110, s[90:91]\n"
      "v_cndmask_b32 v87, 0, v111, s[90:91]\n"
      "v_cndmask_b32 v90, 0, v110, s[92:93]\n"
      "v_cndmask_b32 v91, 0, v111, s[92:93]\n"
      "s_mov_b32 s90, 0x00000000\n"
      "s_mov_b32 s91, 0x0000ffff\n"
      "s_mov_b32 s92, 0x00000000\n"
      "s_mov_b32 s93, 0xffff0000\n"
      "s_waitcnt lgkmcnt(4)\n"
      "v_cndmask_b32 v102, 0, v112, s[90:91]\n"
      "v_cndmask_b32 v103, 0, v113, s[90:91]\n"
      "s_waitcnt lgkmcnt(3)\n"
      "v_cndmask_b32 v106, 0, v114, s[92:93]\n"
      "v_cndmask_b32 v107, 0, v115, s[92:93]\n"
      "s_waitcnt lgkmcnt(2)\n"
      "v_mfma_f32_16x16x32_bf16 v[56:59], v[100:103], v[68:71], 0\n"
      "s_waitcnt lgkmcnt(1)\n"
      "v_mfma_f32_16x16x32_bf16 v[60:63], v[104:107], v[72:75], 0\n"
      "v_mfma_f32_16x16x32_bf16 v[132:135], v[100:103], v[76:79], 0\n"
      "v_mfma_f32_16x16x32_bf16 v[136:139], v[104:107], v[80:83], 0\n"
      "s_waitcnt lgkmcnt(0)\n"
      "v_cndmask_b32 v92, 0, v108, s[94:95]\n"
      "v_cndmask_b32 v93, 0, v109, s[94:95]\n"
      "v_cndmask_b32 v96, 0, v108, s[96:97]\n"
      "v_cndmask_b32 v97, 0, v109, s[96:97]\n"
      "s_mov_b32 s94, 0x00000000\n"
      "s_mov_b32 s95, 0x00000f0f\n"
      "s_mov_b32 s96, 0x00000000\n"
      "s_mov_b32 s97, 0xf0f00000\n"
      "v_mfma_f32_16x16x32_bf16 v[44:47], v[84:87], v[68:71], 0\n"
      "v_mfma_f32_16x16x32_bf16 v[48:51], v[88:91], v[72:75], 0\n"
      "v_mfma_f32_16x16x32_bf16 v[120:123], v[84:87], v[76:79], 0\n"
      "v_mfma_f32_16x16x32_bf16 v[124:127], v[88:91], v[80:83], 0\n"
      "v_cndmask_b32 v94, 0, v110, s[94:95]\n"
      "v_cndmask_b32 v95, 0, v111, s[94:95]\n"
      "v_cndmask_b32 v98, 0, v110, s[96:97]\n"
      "v_cndmask_b32 v99, 0, v111, s[96:97]\n"
      "v_mov_b32 v141, 0x3fb8aa3b\n"
      "v_mfma_f32_16x16x32_bf16 v[52:55], v[92:95], v[68:71], 0\n"
      "v_mfma_f32_16x16x32_bf16 v[52:55], v[96:99], v[72:75], v[52:55]\n"
      "v_mfma_f32_16x16x32_bf16 v[128:131], v[92:95], v[76:79], 0\n"
      "v_mfma_f32_16x16x32_bf16 v[128:131], v[96:99], v[80:83], v[128:131]\n"
      "v_fma_f32 v56, v56, v141, s86\n"
      "v_fma_f32 v60, v60, v141, s86\n"
      "v_rcp_f32 v56, v56\n"
      "v_rcp_f32 v60, v60\n"
      "v_fma_f32 v132, v132, v141, s86\n"
      "v_fma_f32 v136, v136, v141, s86\n"
      "v_rcp_f32 v132, v132\n"
      "v_rcp_f32 v136, v136\n"
      "v_cndmask_b32 v58, v60, v56, s[90:91]\n"
      "v_pk_mul_f32 v[44:45], v[44:45], v[56:57] op_sel_hi:[1,0]\n"
      "v_pk_mul_f32 v[46:47], v[46:47], v[56:57] op_sel_hi:[1,0]\n"
      "v_pk_mul_f32 v[48:49], v[48:49], v[60:61] op_sel_hi:[1,0]\n"
      "v_pk_mul_f32 v[50:51], v[50:51], v[60:61] op_sel_hi:[1,0]\n"
      "v_pk_mul_f32 v[52:53], v[52:53], v[58:59] op_sel_hi:[1,0]\n"
      "v_pk_mul_f32 v[54:55], v[54:55], v[58:59] op_sel_hi:[1,0]\n"
      "v_cvt_pk_bf16_f32 v144, v44, v45\n"
      "v_cvt_pk_bf16_f32 v145, v46, v47\n"
      "v_cvt_pk_bf16_f32 v146, v48, v49\n"
      "v_cvt_pk_bf16_f32 v147, v50, v51\n"
      "v_cvt_pk_bf16_f32 v148, v52, v53\n"
      "v_cvt_pk_bf16_f32 v149, v54, v55\n"
      "s_add_u32 s75, s59, 2621440\n"
      "buffer_store_dwordx4 v[144:147], v0, s[24:27], s75 offen offset:0 sc1\n"
      "s_mov_b32 exec_lo, 0\n"
      "s_add_u32 s76, s60, 2625280\n"
      "buffer_store_dwordx2 v[148:149], v5, s[24:27], s76 offen offset:0 sc1\n"
      "s_mov_b32 exec_lo, -1\n"
      "v_cndmask_b32 v134, v136, v132, s[90:91]\n"
      "v_pk_mul_f32 v[120:121], v[120:121], v[132:133] op_sel_hi:[1,0]\n"
      "v_pk_mul_f32 v[122:123], v[122:123], v[132:133] op_sel_hi:[1,0]\n"
      "v_pk_mul_f32 v[124:125], v[124:125], v[136:137] op_sel_hi:[1,0]\n"
      "v_pk_mul_f32 v[126:127], v[126:127], v[136:137] op_sel_hi:[1,0]\n"
      "v_pk_mul_f32 v[128:129], v[128:129], v[134:135] op_sel_hi:[1,0]\n"
      "v_pk_mul_f32 v[130:131], v[130:131], v[134:135] op_sel_hi:[1,0]\n"
      "v_cvt_pk_bf16_f32 v150, v120, v121\n"
      "v_cvt_pk_bf16_f32 v151, v122, v123\n"
      "v_cvt_pk_bf16_f32 v152, v124, v125\n"
      "v_cvt_pk_bf16_f32 v153, v126, v127\n"
      "v_cvt_pk_bf16_f32 v154, v128, v129\n"
      "v_cvt_pk_bf16_f32 v155, v130, v131\n"
      "s_add_u32 s75, s59, 2641920\n"
      "buffer_store_dwordx4 v[150:153], v0, s[24:27], s75 offen offset:0 sc1\n"
      "s_mov_b32 exec_lo, 0\n"
      "s_add_u32 s76, s60, 2645760\n"
      "buffer_store_dwordx2 v[154:155], v5, s[24:27], s76 offen offset:0 sc1\n"
      "s_mov_b32 exec_lo, -1\n"
      "s_waitcnt vmcnt(0)\n"
      "s_bfe_u32 s80, s74, 262153\n"
      "s_mul_i32 s80, s80, 64\n"
      "s_lshl2_add_u32 s80, s73, s80\n"
      "s_add_u32 s80, s80, 16089088\n"
      "s_barrier\n"
      "s_cmp_lg_u32 s40, 0\n"
      "s_cbranch_scc1 Lsg4_%=\n"
      "s_mov_b64 exec, 1\n"
      "buffer_store_dword v10, v9, s[24:27], s80 offen sc1\n"
      "s_mov_b64 exec, -1\n"
      "Lsg4_%=:\n"
      "s_cmp_lg_u32 s40, 0\n"
      "s_cbranch_scc1 Lsd5_%=\n"
      "s_lshl2_add_u32 s80, s73, 16089088\n"
      "v_mul_u32_u24 v43, 16, v0\n"
      "v_lshrrev_b32 v43, 2, v43\n"
      "s_sleep 6\n"
      "s_mov_b32 s84, 400000\n"
      "s_mov_b64 exec, 65535\n"
      "Lpl5_%=:\n"
      "buffer_load_dword v40, v43, s[24:27], s80 offen sc1\n"
      "s_waitcnt vmcnt(0)\n"
      "v_cmp_gt_u32 vcc, 1, v40\n"
      "s_cbranch_vccz Lpx5_%=\n"
      "s_sub_u32 s84, s84, 1\n"
      "s_cmp_lg_u32 s84, 0\n"
      "s_cbranch_scc1 Lpl5_%=\n"
      "Lpx5_%=:\n"
      "s_mov_b64 exec, -1\n"
      "s_branch Lpd5_%=\n"
      "Lsd5_%=:\n"
      "s_mov_b32 s79, 3298304\n"
      "s_cmp_eq_u32 s40, 1\n"
      "s_cselect_b32 s79, 3359744, s79\n"
      "s_cmp_eq_u32 s40, 3\n"
      "s_cselect_b32 s79, 3339264, s79\n"
      "s_add_u32 s79, s79, s67\n"
      "s_sub_u32 s79, s79, s72\n"
      "s_add_u32 s84, s40, 4\n"
      "s_mul_i32 m0, s84, 20480\n"
      "s_mov_b32 s84, 5\n"
      "Lsl5_%=:\n"
      "buffer_load_dwordx4 v0, s[20:23], s79 offen offset:0 sc1 lds\n"
      "buffer_load_dwordx4 v0, s[20:23], s79 offen offset:1024 sc1 lds\n"
      "buffer_load_dwordx4 v0, s[20:23], s79 offen offset:2048 sc1 lds\n"
      "buffer_load_dwordx4 v0, s[20:23], s79 offen offset:3072 sc1 lds\n"
      "s_add_u32 s79, s79, 4096\n"
      "s_add_u32 m0, m0, 4096\n"
      "s_sub_u32 s84, s84, 1\n"
      "s_cmp_lg_u32 s84, 0\n"
      "s_cbranch_scc1 Lsl5_%=\n"
      "Lpd5_%=:\n"
      "s_barrier\n"
      "s_add_u32 s75, s47, 2621440\n"
      "buffer_load_dwordx4 a[0:3], v0, s[24:27], s75 offen offset:0 sc1\n"
      "buffer_load_dwordx4 a[4:7], v0, s[24:27], s75 offen offset:1024 sc1\n"
      "buffer_load_dwordx4 a[8:11], v0, s[24:27], s75 offen offset:2048 sc1\n"
      "buffer_load_dwordx4 a[12:15], v0, s[24:27], s75 offen offset:3072 sc1\n"
      "s_add_u32 s75, s47, 2625536\n"
      "buffer_load_dwordx4 a[16:19], v0, s[24:27], s75 offen offset:0 sc1\n"
      "s_add_u32 s75, s48, 2621440\n"
      "buffer_load_dwordx4 a[20:23], v0, s[24:27], s75 offen offset:0 sc1\n"
      "buffer_load_dwordx4 a[24:27], v0, s[24:27], s75 offen offset:1024 sc1\n"
      "buffer_load_dwordx4 a[28:31], v0, s[24:27], s75 offen offset:2048 sc1\n"
      "buffer_load_dwordx4 a[32:35], v0, s[24:27], s75 offen offset:3072 sc1\n"
      "s_add_u32 s75, s48, 2625536\n"
      "buffer_load_dwordx4 a[36:39], v0, s[24:27], s75 offen offset:0 sc1\n"
      "s_waitcnt vmcnt(9)\n"
      "v_mfma_f32_16x16x32_bf16 v[44:47], v[156:159], a[0:3], 0\n"
      "v_mfma_f32_16x16x32_bf16 v[48:51], v[160:163], a[0:3], 0\n"
      "v_mfma_f32_16x16x32_bf16 v[52:55], v[164:167], a[0:3], 0\n"
      "v_mfma_f32_16x16x32_bf16 v[56:59], v[168:171], a[0:3], 0\n"
      "s_add_u32 s75, s49, 2621440\n"
      "buffer_load_dwordx4 a[40:43], v0, s[24:27], s75 offen offset:0 sc1\n"
      "v_mfma_f32_16x16x32_bf16 v[60:63], v[172:175], a[0:3], 0\n"
      "s_waitcnt vmcnt(9)\n"
      "v_mfma_f32_16x16x32_bf16 v[44:47], v[176:179], a[4:7], v[44:47]\n"
      "v_mfma_f32_16x16x32_bf16 v[48:51], v[180:183], a[4:7], v[48:51]\n"
      "v_mfma_f32_16x16x32_bf16 v[52:55], v[184:187], a[4:7], v[52:55]\n"
      "buffer_load_dwordx4 a[44:47], v0, s[24:27], s75 offen offset:1024 sc1\n"
      "v_mfma_f32_16x16x32_bf16 v[56:59], v[188:191], a[4:7], v[56:59]\n"
      "v_mfma_f32_16x16x32_bf16 v[60:63], v[192:195], a[4:7], v[60:63]\n"
      "s_waitcnt vmcnt(9)\n"
      "v_mfma_f32_16x16x32_bf16 v[44:47], v[196:199], a[8:11], v[44:47]\n"
      "v_mfma_f32_16x16x32_bf16 v[48:51], v[200:203], a[8:11], v[48:51]\n"
      "buffer_load_dwordx4 a[48:51], v0, s[24:27], s75 offen offset:2048 sc1\n"
      "v_mfma_f32_16x16x32_bf16 v[52:55], v[204:207], a[8:11], v[52:55]\n"
      "v_mfma_f32_16x16x32_bf16 v[56:59], v[208:211], a[8:11], v[56:59]\n"
      "v_mfma_f32_16x16x32_bf16 v[60:63], v[212:215], a[8:11], v[60:63]\n"
      "s_waitcnt vmcnt(9)\n"
      "v_mfma_f32_16x16x32_bf16 v[44:47], v[216:219], a[12:15], v[44:47]\n"
      "buffer_load_dwordx4 a[52:55], v0, s[24:27], s75 offen offset:3072 sc1\n"
      "v_mfma_f32_16x16x32_bf16 v[48:51], v[220:223], a[12:15], v[48:51]\n"
      "v_mfma_f32_16x16x32_bf16 v[52:55], v[224:227], a[12:15], v[52:55]\n"
      "v_mfma_f32_16x16x32_bf16 v[56:59], v[228:231], a[12:15], v[56:59]\n"
      "v_mfma_f32_16x16x32_bf16 v[60:63], v[232:235], a[12:15], v[60:63]\n"
      "s_add_u32 s75, s49, 2625536\n"
      "buffer_load_dwordx4 a[56:59], v0, s[24:27], s75 offen offset:0 sc1\n"
      "s_waitcnt vmcnt(10)\n"
      "v_mfma_f32_16x16x32_bf16 v[44:47], v[236:239], a[16:19], v[44:47]\n"
      "v_mfma_f32_16x16x32_bf16 v[48:51], v[240:243], a[16:19], v[48:51]\n"
      "v_mfma_f32_16x16x32_bf16 v[52:55], v[244:247], a[16:19], v[52:55]\n"
      "v_mfma_f32_16x16x32_bf16 v[56:59], v[248:251], a[16:19], v[56:59]\n"
      "s_add_u32 s75, s50, 2621440\n"
      "buffer_load_dwordx4 a[60:63], v0, s[24:27], s75 offen offset:0 sc1\n"
      "v_mfma_f32_16x16x32_bf16 v[60:63], v[252:255], a[16:19], v[60:63]\n"
      "s_waitcnt vmcnt(10)\n"
      "v_mfma_f32_16x16x32_bf16 v[64:67], v[156:159], a[20:23], 0\n"
      "s_add_u32 s80, s58, 0\n"
      "v_add_u32 v154, s80, v0\n"
      "s_add_u32 s80, s55, 0\n"
      "v_add_u32 v152, s80, v0\n"
      "v_mfma_f32_16x16x32_bf16 v[68:71], v[160:163], a[20:23], 0\n"
      "ds_write_b128 v152, v[44:47] offset:0\n"
      "v_mfma_f32_16x16x32_bf16 v[72:75], v[164:167], a[20:23], 0\n"
      "buffer_load_dwordx4 a[64:67], v0, s[24:27], s75 offen offset:1024 sc1\n"
      "v_mfma_f32_16x16x32_bf16 v[76:79], v[168:171], a[20:23], 0\n"
      "v_mfma_f32_16x16x32_bf16 v[80:83], v[172:175], a[20:23], 0\n"
      "ds_write_b128 v152, v[48:51] offset:1024\n"
      "s_waitcnt vmcnt(10)\n"
      "v_mfma_f32_16x16x32_bf16 v[64:67], v[176:179], a[24:27], v[64:67]\n"
      "v_mfma_f32_16x16x32_bf16 v[68:71], v[180:183], a[24:27], v[68:71]\n"
      "buffer_load_dwordx4 a[68:71], v0, s[24:27], s75 offen offset:2048 sc1\n"
      "v_mfma_f32_16x16x32_bf16 v[72:75], v[184:187], a[24:27], v[72:75]\n"
      "ds_write_b128 v152, v[52:55] offset:2048\n"
      "v_mfma_f32_16x16x32_bf16 v[76:79], v[188:191], a[24:27], v[76:79]\n"
      "v_mfma_f32_16x16x32_bf16 v[80:83], v[192:195], a[24:27], v[80:83]\n"
      "s_waitcnt vmcnt(10)\n"
      "v_mfma_f32_16x16x32_bf16 v[64:67], v[196:199], a[28:31], v[64:67]\n"
      "buffer_load_dwordx4 a[72:75], v0, s[24:27], s75 offen offset:3072 sc1\n"
      "v_mfma_f32_16x16x32_bf16 v[68:71], v[200:203], a[28:31], v[68:71]\n"
      "ds_write_b128 v152, v[56:59] offset:3072\n"
      "v_mfma_f32_16x16x32_bf16 v[72:75], v[204:207], a[28:31], v[72:75]\n"
      "v_mfma_f32_16x16x32_bf16 v[76:79], v[208:211], a[28:31], v[76:79]\n"
      "v_mfma_f32_16x16x32_bf16 v[80:83], v[212:215], a[28:31], v[80:83]\n"
      "s_add_u32 s75, s50, 2625536\n"
      "buffer_load_dwordx4 a[76:79], v0, s[24:27], s75 offen offset:0 sc1\n"
      "ds_write_b128 v152, v[60:63] offset:4096\n"
      "s_waitcnt vmcnt(11)\n"
      "v_mfma_f32_16x16x32_bf16 v[64:67], v[216:219], a[32:35], v[64:67]\n"
      "v_mfma_f32_16x16x32_bf16 v[68:71], v[220:223], a[32:35], v[68:71]\n"
      "v_mfma_f32_16x16x32_bf16 v[72:75], v[224:227], a[32:35], v[72:75]\n"
      "v_mfma_f32_16x16x32_bf16 v[76:79], v[228:231], a[32:35], v[76:79]\n"
      "s_add_u32 s78, s69, 7873536\n"
      "buffer_load_dwordx4 a[192:195], v0, s[20:23], s78 offen offset:0 sc1\n"
      "v_mfma_f32_16x16x32_bf16 v[80:83], v[232:235], a[32:35], v[80:83]\n"
      "s_waitcnt vmcnt(11)\n"
      "v_mfma_f32_16x16x32_bf16 v[64:67], v[236:239], a[36:39], v[64:67]\n"
      "v_mfma_f32_16x16x32_bf16 v[68:71], v[240:243], a[36:39], v[68:71]\n"
      "v_mfma_f32_16x16x32_bf16 v[72:75], v[244:247], a[36:39], v[72:75]\n"
      "buffer_load_dwordx4 a[196:199], v0, s[20:23], s78 offen offset:1024 sc1\n"
      "v_mfma_f32_16x16x32_bf16 v[76:79], v[248:251], a[36:39], v[76:79]\n"
      "v_mfma_f32_16x16x32_bf16 v[80:83], v[252:255], a[36:39], v[80:83]\n"
      "s_waitcnt vmcnt(11)\n"
      "v_mfma_f32_16x16x32_bf16 v[44:47], v[156:159], a[40:43], 0\n"
      "s_add_u32 s80, s56, 0\n"
      "v_add_u32 v153, s80, v0\n"
      "v_mfma_f32_16x16x32_bf16 v[48:51], v[160:163], a[40:43], 0\n"
      "buffer_load_dwordx4 a[200:203], v0, s[20:23], s78 offen offset:2048 sc1\n"
      "ds_write_b128 v153, v[64:67] offset:0\n"
      "v_mfma_f32_16x16x32_bf16 v[52:55], v[164:167], a[40:43], 0\n"
      "v_mfma_f32_16x16x32_bf16 v[56:59], v[168:171], a[40:43], 0\n"
      "v_mfma_f32_16x16x32_bf16 v[60:63], v[172:175], a[40:43], 0\n"
      "ds_write_b128 v153, v[68:71] offset:1024\n"
      "s_waitcnt vmcnt(11)\n"
      "v_mfma_f32_16x16x32_bf16 v[44:47], v[176:179], a[44:47], v[44:47]\n"
      "buffer_load_dwordx4 a[204:207], v0, s[20:23], s78 offen offset:3072 sc1\n"
      "v_mfma_f32_16x16x32_bf16 v[48:51], v[180:183], a[44:47], v[48:51]\n"
      "v_mfma_f32_16x16x32_bf16 v[52:55], v[184:187], a[44:47], v[52:55]\n"
      "ds_write_b128 v153, v[72:75] offset:2048\n"
      "v_mfma_f32_16x16x32_bf16 v[56:59], v[188:191], a[44:47], v[56:59]\n"
      "v_mfma_f32_16x16x32_bf16 v[60:63], v[192:195], a[44:47], v[60:63]\n"
      "s_add_u32 s78, s69, 7877632\n"
      "buffer_load_dwordx4 a[208:211], v0, s[20:23], s78 offen offset:0 sc1\n"
      "s_waitcnt vmcnt(12)\n"
      "v_mfma_f32_16x16x32_bf16 v[44:47], v[196:199], a[48:51], v[44:47]\n"
      "v_mfma_f32_16x16x32_bf16 v[48:51], v[200:203], a[48:51], v[48:51]\n"
      "ds_write_b128 v153, v[76:79] offset:3072\n"
      "v_mfma_f32_16x16x32_bf16 v[52:55], v[204:207], a[48:51], v[52:55]\n"
      "v_mfma_f32_16x16x32_bf16 v[56:59], v[208:211], a[48:51], v[56:59]\n"
      "buffer_load_dwordx4 a[212:215], v0, s[20:23], s78 offen offset:1024 sc1\n"
      "v_mfma_f32_16x16x32_bf16 v[60:63], v[212:215], a[48:51], v[60:63]\n"
      "ds_write_b128 v153, v[80:83] offset:4096\n"
      "s_waitcnt vmcnt(12)\n"
      "v_mfma_f32_16x16x32_bf16 v[44:47], v[216:219], a[52:55], v[44:47]\n"
      "v_mfma_f32_16x16x32_bf16 v[48:51], v[220:223], a[52:55], v[48:51]\n"
      "v_mfma_f32_16x16x32_bf16 v[52:55], v[224:227], a[52:55], v[52:55]\n"
      "buffer_load_dwordx4 a[216:219], v0, s[20:23], s78 offen offset:2048 sc1\n"
      "v_mfma_f32_16x16x32_bf16 v[56:59], v[228:231], a[52:55], v[56:59]\n"
      "v_mfma_f32_16x16x32_bf16 v[60:63], v[232:235], a[52:55], v[60:63]\n"
      "s_waitcnt vmcnt(12)\n"
      "v_mfma_f32_16x16x32_bf16 v[44:47], v[236:239], a[56:59], v[44:47]\n"
      "v_mfma_f32_16x16x32_bf16 v[48:51], v[240:243], a[56:59], v[48:51]\n"
      "v_mfma_f32_16x16x32_bf16 v[52:55], v[244:247], a[56:59], v[52:55]\n"
      "v_mfma_f32_16x16x32_bf16 v[56:59], v[248:251], a[56:59], v[56:59]\n"
      "v_mfma_f32_16x16x32_bf16 v[60:63], v[252:255], a[56:59], v[60:63]\n"
      "s_waitcnt vmcnt(11)\n"
      "v_mfma_f32_16x16x32_bf16 v[12:15], v[156:159], a[60:63], v[12:15]\n"
      "s_add_u32 s80, s57, 0\n"
      "v_add_u32 v152, s80, v0\n"
      "v_mfma_f32_16x16x32_bf16 v[16:19], v[160:163], a[60:63], v[16:19]\n"
      "ds_write_b128 v152, v[44:47] offset:0\n"
      "v_mfma_f32_16x16x32_bf16 v[20:23], v[164:167], a[60:63], v[20:23]\n"
      "v_mfma_f32_16x16x32_bf16 v[24:27], v[168:171], a[60:63], v[24:27]\n"
      "v_mfma_f32_16x16x32_bf16 v[28:31], v[172:175], a[60:63], v[28:31]\n"
      "ds_write_b128 v152, v[48:51] offset:1024\n"
      "s_waitcnt vmcnt(10)\n"
      "v_mfma_f32_16x16x32_bf16 v[12:15], v[176:179], a[64:67], v[12:15]\n"
      "buffer_load_dwordx4 a[220:223], v0, s[20:23], s78 offen offset:3072 sc1\n"
      "v_mfma_f32_16x16x32_bf16 v[16:19], v[180:183], a[64:67], v[16:19]\n"
      "v_mfma_f32_16x16x32_bf16 v[20:23], v[184:187], a[64:67], v[20:23]\n"
      "ds_write_b128 v152, v[52:55] offset:2048\n"
      "v_mfma_f32_16x16x32_bf16 v[24:27], v[188:191], a[64:67], v[24:27]\n"
      "v_mfma_f32_16x16x32_bf16 v[28:31], v[192:195], a[64:67], v[28:31]\n"
      "s_waitcnt vmcnt(10)\n"
      "v_mfma_f32_16x16x32_bf16 v[12:15], v[196:199], a[68:71], v[12:15]\n"
      "s_add_u32 s78, s69, 7881728\n"
      "buffer_load_dwordx4 a[224:227], v0, s[20:23], s78 offen offset:0 sc1\n"
      "v_mfma_f32_16x16x32_bf16 v[16:19], v[200:203], a[68:71], v[16:19]\n"
      "ds_write_b128 v152, v[56:59] offset:3072\n"
      "v_mfma_f32_16x16x32_bf16 v[20:23], v[204:207], a[68:71], v[20:23]\n"
      "v_mfma_f32_16x16x32_bf16 v[24:27], v[208:211], a[68:71], v[24:27]\n"
      "v_mfma_f32_16x16x32_bf16 v[28:31], v[212:215], a[68:71], v[28:31]\n"
      "ds_write_b128 v152, v[60:63] offset:4096\n"
      "s_waitcnt vmcnt(10)\n"
      "v_mfma_f32_16x16x32_bf16 v[12:15], v[216:219], a[72:75], v[12:15]\n"
      "buffer_load_dwordx4 a[228:231], v0, s[20:23], s78 offen offset:1024 sc1\n"
      "v_mfma_f32_16x16x32_bf16 v[16:19], v[220:223], a[72:75], v[16:19]\n"
      "v_mfma_f32_16x16x32_bf16 v[20:23], v[224:227], a[72:75], v[20:23]\n"
      "v_mfma_f32_16x16x32_bf16 v[24:27], v[228:231], a[72:75], v[24:27]\n"
      "s_waitcnt lgkmcnt(0)\n"
      "s_barrier\n"
      "ds_read_b128 v[84:87], v154 offset:0\n"
      "v_mfma_f32_16x16x32_bf16 v[28:31], v[232:235], a[72:75], v[28:31]\n"
      "ds_read_b128 v[88:91], v154 offset:1024\n"
      "s_waitcnt vmcnt(10)\n"
      "v_mfma_f32_16x16x32_bf16 v[12:15], v[236:239], a[76:79], v[12:15]\n"
      "ds_read_b128 v[92:95], v154 offset:2048\n"
      "buffer_load_dwordx4 a[232:235], v0, s[20:23], s78 offen offset:2048 sc1\n"
      "v_mfma_f32_16x16x32_bf16 v[16:19], v[240:243], a[76:79], v[16:19]\n"
      "ds_read_b128 v[96:99], v154 offset:3072\n"
      "v_mfma_f32_16x16x32_bf16 v[20:23], v[244:247], a[76:79], v[20:23]\n"
      "ds_read_b128 v[100:103], v154 offset:4096\n"
      "v_mfma_f32_16x16x32_bf16 v[24:27], v[248:251], a[76:79], v[24:27]\n"
      "ds_read_b128 v[104:107], v154 offset:5120\n"
      "v_mfma_f32_16x16x32_bf16 v[28:31], v[252:255], a[76:79], v[28:31]\n"
      "ds_read_b128 v[108:111], v154 offset:6144\n"
      "ds_read_b128 v[112:115], v154 offset:7168\n"
      "ds_read_b128 v[116:119], v154 offset:8192\n"
      "ds_read_b128 v[120:123], v154 offset:9216\n"
      "ds_read_b128 v[124:127], v154 offset:10240\n"
      "ds_read_b128 v[128:131], v154 offset:11264\n"
      "ds_read_b128 v[132:135], v154 offset:12288\n"
      "ds_read_b128 v[136:139], v154 offset:13312\n"
      "ds_read_b128 v[140:143], v154 offset:14336\n"
      "s_waitcnt lgkmcnt(9)\n"
      "v_pk_add_f32 v[84:85], v[84:85], v[104:105]\n"
      "v_pk_add_f32 v[86:87], v[86:87], v[106:107]\n"
      "s_waitcnt lgkmcnt(8)\n"
      "v_pk_add_f32 v[88:89], v[88:89], v[108:109]\n"
      "v_pk_add_f32 v[90:91], v[90:91], v[110:111]\n"
      "s_waitcnt lgkmcnt(7)\n"
      "v_pk_add_f32 v[92:93], v[92:93], v[112:113]\n"
      "v_pk_add_f32 v[94:95], v[94:95], v[114:115]\n"
      "s_waitcnt lgkmcnt(6)\n"
      "v_pk_add_f32 v[96:97], v[96:97], v[116:117]\n"
      "v_pk_add_f32 v[98:99], v[98:99], v[118:119]\n"
      "s_waitcnt lgkmcnt(5)\n"
      "v_pk_add_f32 v[100:101], v[100:101], v[120:121]\n"
      "v_pk_add_f32 v[102:103], v[102:103], v[122:123]\n"
      "buffer_load_dwordx4 a[236:239], v0, s[20:23], s78 offen offset:3072 sc1\n"
      "v_pk_add_f32 v[12:13], v[12:13], v[84:85]\n"
      "v_pk_add_f32 v[14:15], v[14:15], v[86:87]\n"
      "v_pk_add_f32 v[16:17], v[16:17], v[88:89]\n"
      "v_pk_add_f32 v[18:19], v[18:19], v[90:91]\n"
      "v_pk_add_f32 v[20:21], v[20:21], v[92:93]\n"
      "v_pk_add_f32 v[22:23], v[22:23], v[94:95]\n"
      "v_pk_add_f32 v[24:25], v[24:25], v[96:97]\n"
      "v_pk_add_f32 v[26:27], v[26:27], v[98:99]\n"
      "v_pk_add_f32 v[28:29], v[28:29], v[100:101]\n"
      "v_pk_add_f32 v[30:31], v[30:31], v[102:103]\n"
      "s_add_u32 s78, s69, 7885824\n"
      "buffer_load_dwordx4 a[240:243], v0, s[20:23], s78 offen offset:0 sc1\n"
      "s_waitcnt lgkmcnt(4)\n"
      "v_pk_add_f32 v[12:13], v[12:13], v[124:125]\n"
      "v_pk_add_f32 v[14:15], v[14:15], v[126:127]\n"
      "s_waitcnt lgkmcnt(3)\n"
      "v_pk_add_f32 v[16:17], v[16:17], v[128:129]\n"
      "v_pk_add_f32 v[18:19], v[18:19], v[130:131]\n"
      "s_waitcnt lgkmcnt(2)\n"
      "v_pk_add_f32 v[20:21], v[20:21], v[132:133]\n"
      "v_pk_add_f32 v[22:23], v[22:23], v[134:135]\n"
      "s_waitcnt lgkmcnt(1)\n"
      "v_pk_add_f32 v[24:25], v[24:25], v[136:137]\n"
      "v_pk_add_f32 v[26:27], v[26:27], v[138:139]\n"
      "s_waitcnt lgkmcnt(0)\n"
      "v_pk_add_f32 v[28:29], v[28:29], v[140:141]\n"
      "v_pk_add_f32 v[30:31], v[30:31], v[142:143]\n"
      "buffer_load_dwordx4 a[244:247], v0, s[20:23], s78 offen offset:1024 sc1\n"
      "v_cvt_pk_bf16_f32 v44, v12, v13\n"
      "v_cvt_pk_bf16_f32 v45, v14, v15\n"
      "v_cvt_pk_bf16_f32 v46, v16, v17\n"
      "v_cvt_pk_bf16_f32 v47, v18, v19\n"
      "s_add_u32 s75, s44, 5242880\n"
      "buffer_store_dwordx4 v[44:47], v0, s[24:27], s75 offen offset:0 sc1\n"
      "v_cvt_pk_bf16_f32 v48, v20, v21\n"
      "v_cvt_pk_bf16_f32 v49, v22, v23\n"
      "v_cvt_pk_bf16_f32 v50, v24, v25\n"
      "v_cvt_pk_bf16_f32 v51, v26, v27\n"
      "buffer_store_dwordx4 v[48:51], v0, s[24:27], s75 offen offset:1024 sc1\n"
      "v_cvt_pk_bf16_f32 v52, v28, v29\n"
      "v_cvt_pk_bf16_f32 v53, v30, v31\n"
      "buffer_store_dwordx2 v[52:53], v4, s[24:27], s75 offen offset:2048 sc1\n"
      "v_pk_add_f32 v[56:57], v[12:13], v[14:15]\n"
      "v_pk_mul_f32 v[58:59], v[12:13], v[12:13]\n"
      "v_pk_fma_f32 v[58:59], v[14:15], v[14:15], v[58:59]\n"
      "v_pk_add_f32 v[56:57], v[56:57], v[16:17]\n"
      "v_pk_fma_f32 v[58:59], v[16:17], v[16:17], v[58:59]\n"
      "v_pk_add_f32 v[56:57], v[56:57], v[18:19]\n"
      "v_pk_fma_f32 v[58:59], v[18:19], v[18:19], v[58:59]\n"
      "v_pk_add_f32 v[56:57], v[56:57], v[20:21]\n"
      "v_pk_fma_f32 v[58:59], v[20:21], v[20:21], v[58:59]\n"
      "v_pk_add_f32 v[56:57], v[56:57], v[22:23]\n"
      "v_pk_fma_f32 v[58:59], v[22:23], v[22:23], v[58:59]\n"
      "v_pk_add_f32 v[56:57], v[56:57], v[24:25]\n"
      "v_pk_fma_f32 v[58:59], v[24:25], v[24:25], v[58:59]\n"
      "v_pk_add_f32 v[56:57], v[56:57], v[26:27]\n"
      "v_pk_fma_f32 v[58:59], v[26:27], v[26:27], v[58:59]\n"
      "v_pk_add_f32 v[56:57], v[56:57], v[28:29]\n"
      "v_pk_fma_f32 v[58:59], v[28:29], v[28:29], v[58:59]\n"
      "v_pk_add_f32 v[56:57], v[56:57], v[30:31]\n"
      "v_pk_fma_f32 v[58:59], v[30:31], v[30:31], v[58:59]\n"
      "v_add_f32 v56, v56, v57\n"
      "v_add_f32 v58, v58, v59\n"
      "s_nop 1\n"
      "v_permlane32_swap_b32 v56, v58\n"
      "v_add_f32 v56, v56, v58\n"
      "v_mov_b32 v58, v56\n"
      "s_nop 1\n"
      "v_permlane16_swap_b32 v56, v58\n"
      "v_add_f32 v56, v56, v58\n"
      "s_add_u32 s76, s45, 15859712\n"
      "buffer_store_dword v56, v34, s[24:27], s76 offen offset:0 sc1\n"
      "s_waitcnt vmcnt(0)\n"
      "s_bfe_u32 s80, s74, 262153\n"
      "s_mul_i32 s80, s80, 64\n"
      "s_lshl2_add_u32 s80, s73, s80\n"
      "s_add_u32 s80, s80, 16121856\n"
      "s_barrier\n"
      "s_cmp_lg_u32 s40, 0\n"
      "s_cbranch_scc1 Lsg6_%=\n"
      "s_mov_b64 exec, 1\n"
      "buffer_store_dword v10, v9, s[24:27], s80 offen sc1\n"
      "s_mov_b64 exec, -1\n"
      "Lsg6_%=:\n"
      "s_cmp_lg_u32 s40, 0\n"
      "s_cbranch_scc1 Lsd7_%=\n"
      "s_lshl2_add_u32 s80, s73, 16121856\n"
      "v_mul_u32_u24 v43, 16, v0\n"
      "v_lshrrev_b32 v43, 2, v43\n"
      "s_sleep 6\n"
      "s_mov_b32 s84, 400000\n"
      "s_mov_b64 exec, 65535\n"
      "Lpl7_%=:\n"
      "buffer_load_dword v40, v43, s[24:27], s80 offen sc1\n"
      "s_waitcnt vmcnt(0)\n"
      "v_cmp_gt_u32 vcc, 1, v40\n"
      "s_cbranch_vccz Lpx7_%=\n"
      "s_sub_u32 s84, s84, 1\n"
      "s_cmp_lg_u32 s84, 0\n"
      "s_cbranch_scc1 Lpl7_%=\n"
      "Lpx7_%=:\n"
      "s_mov_b64 exec, -1\n"
      "s_branch Lpd7_%=\n"
      "Lsd7_%=:\n"
      "s_mov_b32 s79, 3400704\n"
      "s_cmp_eq_u32 s40, 1\n"
      "s_cselect_b32 s79, 3380224, s79\n"
      "s_cmp_eq_u32 s40, 3\n"
      "s_cselect_b32 s79, 3421184, s79\n"
      "s_add_u32 s79, s79, s67\n"
      "s_sub_u32 s79, s79, s72\n"
      "s_add_u32 s84, s40, -1\n"
      "s_mul_i32 m0, s84, 20480\n"
      "s_mov_b32 s84, 5\n"
      "Lsl7_%=:\n"
      "buffer_load_dwordx4 v0, s[20:23], s79 offen offset:0 sc1 lds\n"
      "buffer_load_dwordx4 v0, s[20:23], s79 offen offset:1024 sc1 lds\n"
      "buffer_load_dwordx4 v0, s[20:23], s79 offen offset:2048 sc1 lds\n"
      "buffer_load_dwordx4 v0, s[20:23], s79 offen offset:3072 sc1 lds\n"
      "s_add_u32 s79, s79, 4096\n"
      "s_add_u32 m0, m0, 4096\n"
      "s_sub_u32 s84, s84, 1\n"
      "s_cmp_lg_u32 s84, 0\n"
      "s_cbranch_scc1 Lsl7_%=\n"
      "Lpd7_%=:\n"
      "s_barrier\n"
      "s_add_u32 s75, s42, 5242880\n"
      "buffer_load_dwordx4 a[0:3], v1, s[24:27], s75 offen offset:0 sc1\n"
      "buffer_load_dwordx4 a[4:7], v1, s[24:27], s75 offen offset:512 sc1\n"
      "buffer_load_dwordx4 a[8:11], v1, s[24:27], s75 offen offset:1024 sc1\n"
      "buffer_load_dwordx4 a[12:15], v1, s[24:27], s75 offen offset:1536 sc1\n"
      "s_add_u32 s76, s43, 15859712\n"
      "buffer_load_dword v36, v3, s[24:27], s76 offen offset:0 sc1\n"
      "buffer_load_dword v37, v3, s[24:27], s76 offen offset:128 sc1\n"
      "buffer_load_dword v38, v3, s[24:27], s76 offen offset:256 sc1\n"
      "buffer_load_dword v39, v3, s[24:27], s76 offen offset:384 sc1\n"
      "buffer_load_dword v40, v3, s[24:27], s76 offen offset:512 sc1\n"
      "buffer_load_dword v41, v3, s[24:27], s76 offen offset:640 sc1\n"
      "buffer_load_dword v42, v3, s[24:27], s76 offen offset:768 sc1\n"
      "buffer_load_dword v43, v3, s[24:27], s76 offen offset:896 sc1\n"
      "buffer_load_dwordx4 a[16:19], v1, s[24:27], s75 offen offset:2048 sc1\n"
      "buffer_load_dwordx4 a[20:23], v1, s[24:27], s75 offen offset:2560 sc1\n"
      "buffer_load_dwordx4 a[24:27], v1, s[24:27], s75 offen offset:3072 sc1\n"
      "buffer_load_dwordx4 a[28:31], v1, s[24:27], s75 offen offset:3584 sc1\n"
      "s_add_u32 s75, s42, 5246976\n"
      "buffer_load_dwordx4 a[32:35], v1, s[24:27], s75 offen offset:0 sc1\n"
      "buffer_load_dwordx4 a[36:39], v1, s[24:27], s75 offen offset:512 sc1\n"
      "ds_read_b128 v[92:95], v0 offset:61440\n"
      "ds_read_b128 v[96:99], v7 offset:16384\n"
      "ds_read_b128 v[100:103], v7 offset:36864\n"
      "ds_read_b128 v[104:107], v0 offset:62464\n"
      "ds_read_b128 v[108:111], v7 offset:17408\n"
      "s_waitcnt vmcnt(17) lgkmcnt(3)\n"
      "v_mfma_f32_32x32x16_bf16 v[44:59], v[92:95], a[0:3], 0\n"
      "ds_read_b128 v[112:115], v7 offset:37888\n"
      "v_mfma_f32_32x32x16_bf16 v[60:75], v[96:99], a[0:3], 0\n"
      "ds_read_b128 v[92:95], v0 offset:63488\n"
      "s_waitcnt lgkmcnt(3)\n"
      "v_mfma_f32_32x32x16_bf16 v[116:131], v[100:103], a[0:3], 0\n"
      "ds_read_b128 v[96:99], v7 offset:18432\n"
      "buffer_load_dwordx4 a[40:43], v1, s[24:27], s75 offen offset:1024 sc1\n"
      "s_waitcnt vmcnt(17)\n"
      "v_mfma_f32_32x32x16_bf16 v[44:59], v[104:107], a[4:7], v[44:59]\n"
      "ds_read_b128 v[100:103], v7 offset:38912\n"
      "s_waitcnt lgkmcnt(3)\n"
      "v_mfma_f32_32x32x16_bf16 v[60:75], v[108:111], a[4:7], v[60:75]\n"
      "ds_read_b128 v[104:107], v0 offset:64512\n"
      "buffer_load_dwordx4 a[44:47], v1, s[24:27], s75 offen offset:1536 sc1\n"
      "v_mfma_f32_32x32x16_bf16 v[116:131], v[112:115], a[4:7], v[116:131]\n"
      "ds_read_b128 v[108:111], v7 offset:19456\n"
      "s_waitcnt vmcnt(17) lgkmcnt(3)\n"
      "v_mfma_f32_32x32x16_bf16 v[44:59], v[92:95], a[8:11], v[44:59]\n"
      "ds_read_b128 v[112:115], v7 offset:39936\n"
      "buffer_load_dwordx4 a[48:51], v1, s[24:27], s75 offen offset:2048 sc1\n"
      "v_mfma_f32_32x32x16_bf16 v[60:75], v[96:99], a[8:11], v[60:75]\n"
      "ds_read_b128 v[92:95], v7 offset:0\n"
      "s_waitcnt lgkmcnt(3)\n"
      "v_mfma_f32_32x32x16_bf16 v[116:131], v[100:103], a[8:11], v[116:131]\n"
      "ds_read_b128 v[96:99], v7 offset:20480\n"
      "buffer_load_dwordx4 a[52:55], v1, s[24:27], s75 offen offset:2560 sc1\n"
      "s_waitcnt vmcnt(18)\n"
      "v_mfma_f32_32x32x16_bf16 v[44:59], v[104:107], a[12:15], v[44:59]\n"
      "ds_read_b128 v[100:103], v7 offset:40960\n"
      "s_waitcnt lgkmcnt(3)\n"
      "v_mfma_f32_32x32x16_bf16 v[60:75], v[108:111], a[12:15], v[60:75]\n"
      "ds_read_b128 v[104:107], v7 offset:1024\n"
      "v_mfma_f32_32x32x16_bf16 v[116:131], v[112:115], a[12:15], v[116:131]\n"
      "ds_read_b128 v[108:111], v7 offset:21504\n"
      "buffer_load_dwordx4 a[56:59], v1, s[24:27], s75 offen offset:3072 sc1\n"
      "s_waitcnt vmcnt(10) lgkmcnt(3)\n"
      "v_mfma_f32_32x32x16_bf16 v[44:59], v[92:95], a[16:19], v[44:59]\n"
      "ds_read_b128 v[112:115], v7 offset:41984\n"
      "v_mfma_f32_32x32x16_bf16 v[60:75], v[96:99], a[16:19], v[60:75]\n"
      "ds_read_b128 v[92:95], v7 offset:2048\n"
      "buffer_load_dwordx4 a[60:63], v1, s[24:27], s75 offen offset:3584 sc1\n"
      "s_waitcnt lgkmcnt(3)\n"
      "v_mfma_f32_32x32x16_bf16 v[116:131], v[100:103], a[16:19], v[116:131]\n"
      "ds_read_b128 v[96:99], v7 offset:22528\n"
      "s_waitcnt vmcnt(10)\n"
      "v_mfma_f32_32x32x16_bf16 v[44:59], v[104:107], a[20:23], v[44:59]\n"
      "ds_read_b128 v[100:103], v7 offset:43008\n"
      "s_add_u32 s75, s42, 5251072\n"
      "buffer_load_dwordx4 a[64:67], v1, s[24:27], s75 offen offset:0 sc1\n"
      "s_waitcnt lgkmcnt(3)\n"
      "v_mfma_f32_32x32x16_bf16 v[60:75], v[108:111], a[20:23], v[60:75]\n"
      "ds_read_b128 v[104:107], v7 offset:3072\n"
      "v_mfma_f32_32x32x16_bf16 v[116:131], v[112:115], a[20:23], v[116:131]\n"
      "ds_read_b128 v[108:111], v7 offset:23552\n"
      "buffer_load_dwordx4 a[68:71], v1, s[24:27], s75 offen offset:512 sc1\n"
      "s_waitcnt vmcnt(11) lgkmcnt(3)\n"
      "v_mfma_f32_32x32x16_bf16 v[44:59], v[92:95], a[24:27], v[44:59]\n"
      "ds_read_b128 v[112:115], v7 offset:44032\n"
      "v_mfma_f32_32x32x16_bf16 v[60:75], v[96:99], a[24:27], v[60:75]\n"
      "ds_read_b128 v[92:95], v7 offset:4096\n"
      "buffer_load_dwordx4 a[72:75], v1, s[24:27], s75 offen offset:1024 sc1\n"
      "s_waitcnt lgkmcnt(3)\n"
      "v_mfma_f32_32x32x16_bf16 v[116:131], v[100:103], a[24:27], v[116:131]\n"
      "ds_read_b128 v[96:99], v7 offset:24576\n"
      "s_waitcnt vmcnt(11)\n"
      "v_mfma_f32_32x32x16_bf16 v[44:59], v[104:107], a[28:31], v[44:59]\n"
      "ds_read_b128 v[100:103], v7 offset:45056\n"
      "s_waitcnt lgkmcnt(3)\n"
      "v_mfma_f32_32x32x16_bf16 v[60:75], v[108:111], a[28:31], v[60:75]\n"
      "ds_read_b128 v[104:107], v7 offset:5120\n"
      "buffer_load_dwordx4 a[76:79], v1, s[24:27], s75 offen offset:1536 sc1\n"
      "v_mfma_f32_32x32x16_bf16 v[116:131], v[112:115], a[28:31], v[116:131]\n"
      "ds_read_b128 v[108:111], v7 offset:25600\n"
      "s_waitcnt vmcnt(11) lgkmcnt(3)\n"
      "v_mfma_f32_32x32x16_bf16 v[44:59], v[92:95], a[32:35], v[44:59]\n"
      "ds_read_b128 v[112:115], v7 offset:46080\n"
      "buffer_load_dwordx4 a[80:83], v1, s[24:27], s75 offen offset:2048 sc1\n"
      "v_mfma_f32_32x32x16_bf16 v[60:75], v[96:99], a[32:35], v[60:75]\n"
      "ds_read_b128 v[92:95], v7 offset:6144\n"
      "s_waitcnt lgkmcnt(3)\n"
      "v_mfma_f32_32x32x16_bf16 v[116:131], v[100:103], a[32:35], v[116:131]\n"
      "ds_read_b128 v[96:99], v7 offset:26624\n"
      "buffer_load_dwordx4 a[84:87], v1, s[24:27], s75 offen offset:2560 sc1\n"
      "s_waitcnt vmcnt(12)\n"
      "v_mfma_f32_32x32x16_bf16 v[44:59], v[104:107], a[36:39], v[44:59]\n"
      "ds_read_b128 v[100:103], v7 offset:47104\n"
      "s_waitcnt lgkmcnt(3)\n"
      "v_mfma_f32_32x32x16_bf16 v[60:75], v[108:111], a[36:39], v[60:75]\n"
      "ds_read_b128 v[104:107], v7 offset:7168\n"
      "buffer_load_dwordx4 a[88:91], v1, s[24:27], s75 offen offset:3072 sc1\n"
      "v_mfma_f32_32x32x16_bf16 v[116:131], v[112:115], a[36:39], v[116:131]\n"
      "ds_read_b128 v[108:111], v7 offset:27648\n"
      "s_waitcnt vmcnt(12) lgkmcnt(3)\n"
      "v_mfma_f32_32x32x16_bf16 v[44:59], v[92:95], a[40:43], v[44:59]\n"
      "ds_read_b128 v[112:115], v7 offset:48128\n"
      "v_add_f32 v36, v36, v37\n"
      "v_mfma_f32_32x32x16_bf16 v[60:75], v[96:99], a[40:43], v[60:75]\n"
      "ds_read_b128 v[92:95], v7 offset:8192\n"
      "buffer_load_dwordx4 a[92:95], v1, s[24:27], s75 offen offset:3584 sc1\n"
      "v_add_f32 v38, v38, v39\n"
      "s_waitcnt lgkmcnt(3)\n"
      "v_mfma_f32_32x32x16_bf16 v[116:131], v[100:103], a[40:43], v[116:131]\n"
      "ds_read_b128 v[96:99], v7 offset:28672\n"
      "v_add_f32 v40, v40, v41\n"
      "s_waitcnt vmcnt(12)\n"
      "v_mfma_f32_32x32x16_bf16 v[44:59], v[104:107], a[44:47], v[44:59]\n"
      "ds_read_b128 v[100:103], v7 offset:49152\n"
      "s_add_u32 s75, s42, 5255168\n"
      "buffer_load_dwordx4 a[96:99], v1, s[24:27], s75 offen offset:0 sc1\n"
      "v_add_f32 v42, v42, v43\n"
      "s_waitcnt lgkmcnt(3)\n"
      "v_mfma_f32_32x32x16_bf16 v[60:75], v[108:111], a[44:47], v[60:75]\n"
      "ds_read_b128 v[104:107], v7 offset:9216\n"
      "v_add_f32 v36, v36, v38\n"
      "v_mfma_f32_32x32x16_bf16 v[116:131], v[112:115], a[44:47], v[116:131]\n"
      "ds_read_b128 v[108:111], v7 offset:29696\n"
      "buffer_load_dwordx4 a[100:103], v1, s[24:27], s75 offen offset:512 sc1\n"
      "v_add_f32 v40, v40, v42\n"
      "s_waitcnt vmcnt(13) lgkmcnt(3)\n"
      "v_mfma_f32_32x32x16_bf16 v[44:59], v[92:95], a[48:51], v[44:59]\n"
      "ds_read_b128 v[112:115], v7 offset:50176\n"
      "v_add_f32 v36, v36, v40\n"
      "v_mfma_f32_32x32x16_bf16 v[60:75], v[96:99], a[48:51], v[60:75]\n"
      "ds_read_b128 v[92:95], v7 offset:10240\n"
      "buffer_load_dwordx4 a[104:107], v1, s[24:27], s75 offen offset:1024 sc1\n"
      "v_mul_f32 v36, 0x3acccccd, v36\n"
      "s_waitcnt lgkmcnt(3)\n"
      "v_mfma_f32_32x32x16_bf16 v[116:131], v[100:103], a[48:51], v[116:131]\n"
      "ds_read_b128 v[96:99], v7 offset:30720\n"
      "v_mov_b32 v37, v36\n"
      "s_waitcnt vmcnt(13)\n"
      "v_mfma_f32_32x32x16_bf16 v[44:59], v[104:107], a[52:55], v[44:59]\n"
      "ds_read_b128 v[100:103], v7 offset:51200\n"
      "buffer_load_dwordx4 a[108:111], v1, s[24:27], s75 offen offset:1536 sc1\n"
      "v_permlane32_swap_b32 v37, v36\n"
      "s_waitcnt lgkmcnt(3)\n"
      "v_mfma_f32_32x32x16_bf16 v[60:75], v[108:111], a[52:55], v[60:75]\n"
      "ds_read_b128 v[104:107], v7 offset:11264\n"
      "v_fma_f32 v36, -v37, v37, v36\n"
      "v_mfma_f32_32x32x16_bf16 v[116:131], v[112:115], a[52:55], v[116:131]\n"
      "ds_read_b128 v[108:111], v7 offset:31744\n"
      "v_max_f32 v36, 0, v36\n"
      "s_waitcnt vmcnt(13) lgkmcnt(3)\n"
      "v_mfma_f32_32x32x16_bf16 v[44:59], v[92:95], a[56:59], v[44:59]\n"
      "ds_read_b128 v[112:115], v7 offset:52224\n"
      "buffer_load_dwordx4 a[112:115], v1, s[24:27], s75 offen offset:2048 sc1\n"
      "v_add_f32 v36, 0x3727c5ac, v36\n"
      "v_mfma_f32_32x32x16_bf16 v[60:75], v[96:99], a[56:59], v[60:75]\n"
      "ds_read_b128 v[92:95], v7 offset:12288\n"
      "v_rsq_f32 v37, v36\n"
      "s_waitcnt lgkmcnt(3)\n"
      "v_mfma_f32_32x32x16_bf16 v[116:131], v[100:103], a[56:59], v[116:131]\n"
      "ds_read_b128 v[96:99], v7 offset:32768\n"
      "buffer_load_dwordx4 a[116:119], v1, s[24:27], s75 offen offset:2560 sc1\n"
      "v_mov_b32 v150, v36\n"
      "s_waitcnt vmcnt(14)\n"
      "v_mfma_f32_32x32x16_bf16 v[44:59], v[104:107], a[60:63], v[44:59]\n"
      "ds_read_b128 v[100:103], v7 offset:53248\n"
      "v_mul_f32 v148, 0xbfb8aa3b, v37\n"
      "s_waitcnt lgkmcnt(3)\n"
      "v_mfma_f32_32x32x16_bf16 v[60:75], v[108:111], a[60:63], v[60:75]\n"
      "ds_read_b128 v[104:107], v7 offset:13312\n"
      "buffer_load_dwordx4 a[120:123], v1, s[24:27], s75 offen offset:3072 sc1\n"
      "v_mfma_f32_32x32x16_bf16 v[116:131], v[112:115], a[60:63], v[116:131]\n"
      "ds_read_b128 v[108:111], v7 offset:33792\n"
      "s_waitcnt vmcnt(14) lgkmcnt(3)\n"
      "v_mfma_f32_32x32x16_bf16 v[44:59], v[92:95], a[64:67], v[44:59]\n"
      "ds_read_b128 v[112:115], v7 offset:54272\n"
      "buffer_load_dwordx4 a[124:127], v1, s[24:27], s75 offen offset:3584 sc1\n"
      "v_mfma_f32_32x32x16_bf16 v[60:75], v[96:99], a[64:67], v[60:75]\n"
      "ds_read_b128 v[92:95], v7 offset:14336\n"
      "s_waitcnt lgkmcnt(3)\n"
      "v_mfma_f32_32x32x16_bf16 v[116:131], v[100:103], a[64:67], v[116:131]\n"
      "ds_read_b128 v[96:99], v7 offset:34816\n"
      "s_waitcnt vmcnt(14)\n"
      "v_mfma_f32_32x32x16_bf16 v[44:59], v[104:107], a[68:71], v[44:59]\n"
      "ds_read_b128 v[100:103], v7 offset:55296\n"
      "s_add_u32 s75, s42, 5259264\n"
      "buffer_load_dwordx4 a[128:131], v1, s[24:27], s75 offen offset:0 sc1\n"
      "s_waitcnt lgkmcnt(3)\n"
      "v_mfma_f32_32x32x16_bf16 v[60:75], v[108:111], a[68:71], v[60:75]\n"
      "ds_read_b128 v[104:107], v7 offset:15360\n"
      "v_mfma_f32_32x32x16_bf16 v[116:131], v[112:115], a[68:71], v[116:131]\n"
      "ds_read_b128 v[108:111], v7 offset:35840\n"
      "buffer_load_dwordx4 a[132:135], v1, s[24:27], s75 offen offset:512 sc1\n"
      "s_waitcnt vmcnt(15) lgkmcnt(3)\n"
      "v_mfma_f32_32x32x16_bf16 v[44:59], v[92:95], a[72:75], v[44:59]\n"
      "ds_read_b128 v[112:115], v7 offset:56320\n"
      "s_barrier\n"
      "v_mfma_f32_32x32x16_bf16 v[60:75], v[96:99], a[72:75], v[60:75]\n"
      "ds_read_b128 v[92:95], v7 offset:57344\n"
      "buffer_load_dwordx4 a[136:139], v1, s[24:27], s75 offen offset:1024 sc1\n"
      "s_waitcnt lgkmcnt(3)\n"
      "v_mfma_f32_32x32x16_bf16 v[116:131], v[100:103], a[72:75], v[116:131]\n"
      "ds_read_b128 v[96:99], v8 offset:12288\n"
      "s_waitcnt vmcnt(15)\n"
      "v_mfma_f32_32x32x16_bf16 v[44:59], v[104:107], a[76:79], v[44:59]\n"
      "ds_read_b128 v[100:103], v0 offset:0\n"
      "buffer_load_dwordx4 a[140:143], v1, s[24:27], s75 offen offset:1536 sc1\n"
      "s_waitcnt lgkmcnt(3)\n"
      "v_mfma_f32_32x32x16_bf16 v[60:75], v[108:111], a[76:79], v[60:75]\n"
      "ds_read_b128 v[104:107], v7 offset:58368\n"
      "v_mfma_f32_32x32x16_bf16 v[116:131], v[112:115], a[76:79], v[116:131]\n"
      "ds_read_b128 v[108:111], v8 offset:13312\n"
      "buffer_load_dwordx4 a[144:147], v1, s[24:27], s75 offen offset:2048 sc1\n"
      "s_waitcnt vmcnt(16) lgkmcnt(3)\n"
      "v_mfma_f32_32x32x16_bf16 v[44:59], v[92:95], a[80:83], v[44:59]\n"
      "ds_read_b128 v[112:115], v0 offset:1024\n"
      "v_mfma_f32_32x32x16_bf16 v[60:75], v[96:99], a[80:83], v[60:75]\n"
      "ds_read_b128 v[92:95], v7 offset:59392\n"
      "s_barrier\n"
      "s_waitcnt lgkmcnt(3)\n"
      "v_mfma_f32_32x32x16_bf16 v[116:131], v[100:103], a[80:83], v[116:131]\n"
      "ds_read_b128 v[96:99], v8 offset:14336\n"
      "buffer_load_dwordx4 a[148:151], v1, s[24:27], s75 offen offset:2560 sc1\n"
      "s_waitcnt vmcnt(16)\n"
      "v_mfma_f32_32x32x16_bf16 v[44:59], v[104:107], a[84:87], v[44:59]\n"
      "ds_read_b128 v[100:103], v0 offset:2048\n"
      "s_waitcnt lgkmcnt(3)\n"
      "v_mfma_f32_32x32x16_bf16 v[60:75], v[108:111], a[84:87], v[60:75]\n"
      "ds_read_b128 v[104:107], v7 offset:60416\n"
      "buffer_load_dwordx4 a[152:155], v1, s[24:27], s75 offen offset:3072 sc1\n"
      "v_mfma_f32_32x32x16_bf16 v[116:131], v[112:115], a[84:87], v[116:131]\n"
      "ds_read_b128 v[108:111], v8 offset:15360\n"
      "s_waitcnt vmcnt(16) lgkmcnt(3)\n"
      "v_mfma_f32_32x32x16_bf16 v[44:59], v[92:95], a[88:91], v[44:59]\n"
      "ds_read_b128 v[112:115], v0 offset:3072\n"
      "buffer_load_dwordx4 a[156:159], v1, s[24:27], s75 offen offset:3584 sc1\n"
      "v_mfma_f32_32x32x16_bf16 v[60:75], v[96:99], a[88:91], v[60:75]\n"
      "ds_read_b128 v[92:95], v7 offset:61440\n"
      "s_waitcnt lgkmcnt(3)\n"
      "v_mfma_f32_32x32x16_bf16 v[116:131], v[100:103], a[88:91], v[116:131]\n"
      "ds_read_b128 v[96:99], v8 offset:16384\n"
      "s_waitcnt vmcnt(16)\n"
      "v_mfma_f32_32x32x16_bf16 v[44:59], v[104:107], a[92:95], v[44:59]\n"
      "ds_read_b128 v[100:103], v0 offset:4096\n"
      "s_add_u32 s79, s67, 3441664\n"
      "s_add_u32 m0, s72, 61440\n"
      "s_nop 0\n"
      "buffer_load_dwordx4 v0, s[20:23], s79 offen offset:0 sc1 lds\n"
      "s_waitcnt lgkmcnt(3)\n"
      "v_mfma_f32_32x32x16_bf16 v[60:75], v[108:111], a[92:95], v[60:75]\n"
      "ds_read_b128 v[104:107], v7 offset:62464\n"
      "v_mfma_f32_32x32x16_bf16 v[116:131], v[112:115], a[92:95], v[116:131]\n"
      "ds_read_b128 v[108:111], v8 offset:17408\n"
      "buffer_load_dwordx4 v0, s[20:23], s79 offen offset:1024 sc1 lds\n"
      "s_waitcnt vmcnt(17) lgkmcnt(3)\n"
      "v_mfma_f32_32x32x16_bf16 v[44:59], v[92:95], a[96:99], v[44:59]\n"
      "ds_read_b128 v[112:115], v0 offset:5120\n"
      "v_mfma_f32_32x32x16_bf16 v[60:75], v[96:99], a[96:99], v[60:75]\n"
      "ds_read_b128 v[92:95], v7 offset:63488\n"
      "s_waitcnt lgkmcnt(3)\n"
      "v_mfma_f32_32x32x16_bf16 v[116:131], v[100:103], a[96:99], v[116:131]\n"
      "ds_read_b128 v[96:99], v8 offset:18432\n"
      "buffer_load_dwordx4 v0, s[20:23], s79 offen offset:2048 sc1 lds\n"
      "s_waitcnt vmcnt(17)\n"
      "v_mfma_f32_32x32x16_bf16 v[44:59], v[104:107], a[100:103], v[44:59]\n"
      "ds_read_b128 v[100:103], v0 offset:6144\n"
      "s_waitcnt lgkmcnt(3)\n"
      "v_mfma_f32_32x32x16_bf16 v[60:75], v[108:111], a[100:103], v[60:75]\n"
      "ds_read_b128 v[104:107], v7 offset:64512\n"
      "buffer_load_dwordx4 v0, s[20:23], s79 offen offset:3072 sc1 lds\n"
      "v_mfma_f32_32x32x16_bf16 v[116:131], v[112:115], a[100:103], v[116:131]\n"
      "ds_read_b128 v[108:111], v8 offset:19456\n"
      "s_waitcnt vmcnt(17) lgkmcnt(3)\n"
      "v_mfma_f32_32x32x16_bf16 v[44:59], v[92:95], a[104:107], v[44:59]\n"
      "ds_read_b128 v[112:115], v0 offset:7168\n"
      "s_add_u32 s79, s67, 3445760\n"
      "s_add_u32 m0, s72, 65536\n"
      "s_nop 0\n"
      "buffer_load_dwordx4 v0, s[20:23], s79 offen offset:0 sc1 lds\n"
      "v_mfma_f32_32x32x16_bf16 v[60:75], v[96:99], a[104:107], v[60:75]\n"
      "ds_read_b128 v[92:95], v8 offset:0\n"
      "s_waitcnt lgkmcnt(3)\n"
      "v_mfma_f32_32x32x16_bf16 v[116:131], v[100:103], a[104:107], v[116:131]\n"
      "ds_read_b128 v[96:99], v8 offset:20480\n"
      "s_waitcnt vmcnt(17)\n"
      "v_mfma_f32_32x32x16_bf16 v[44:59], v[104:107], a[108:111], v[44:59]\n"
      "ds_read_b128 v[100:103], v0 offset:8192\n"
      "s_add_u32 s79, s67, 3462144\n"
      "s_add_u32 m0, s72, 81920\n"
      "s_nop 0\n"
      "buffer_load_dwordx4 v0, s[20:23], s79 offen offset:0 sc1 lds\n"
      "s_waitcnt lgkmcnt(3)\n"
      "v_mfma_f32_32x32x16_bf16 v[60:75], v[108:111], a[108:111], v[60:75]\n"
      "ds_read_b128 v[104:107], v8 offset:1024\n"
      "v_mfma_f32_32x32x16_bf16 v[116:131], v[112:115], a[108:111], v[116:131]\n"
      "ds_read_b128 v[108:111], v8 offset:21504\n"
      "buffer_load_dwordx4 v0, s[20:23], s79 offen offset:1024 sc1 lds\n"
      "s_waitcnt vmcnt(18) lgkmcnt(3)\n"
      "v_mfma_f32_32x32x16_bf16 v[44:59], v[92:95], a[112:115], v[44:59]\n"
      "ds_read_b128 v[112:115], v0 offset:9216\n"
      "v_mfma_f32_32x32x16_bf16 v[60:75], v[96:99], a[112:115], v[60:75]\n"
      "ds_read_b128 v[92:95], v8 offset:2048\n"
      "s_waitcnt lgkmcnt(3)\n"
      "v_mfma_f32_32x32x16_bf16 v[116:131], v[100:103], a[112:115], v[116:131]\n"
      "ds_read_b128 v[96:99], v8 offset:22528\n"
      "buffer_load_dwordx4 v0, s[20:23], s79 offen offset:2048 sc1 lds\n"
      "s_waitcnt vmcnt(18)\n"
      "v_mfma_f32_32x32x16_bf16 v[44:59], v[104:107], a[116:119], v[44:59]\n"
      "ds_read_b128 v[100:103], v0 offset:10240\n"
      "s_waitcnt lgkmcnt(3)\n"
      "v_mfma_f32_32x32x16_bf16 v[60:75], v[108:111], a[116:119], v[60:75]\n"
      "ds_read_b128 v[104:107], v8 offset:3072\n"
      "buffer_load_dwordx4 v0, s[20:23], s79 offen offset:3072 sc1 lds\n"
      "v_mfma_f32_32x32x16_bf16 v[116:131], v[112:115], a[116:119], v[116:131]\n"
      "ds_read_b128 v[108:111], v8 offset:23552\n"
      "s_waitcnt vmcnt(18) lgkmcnt(3)\n"
      "v_mfma_f32_32x32x16_bf16 v[44:59], v[92:95], a[120:123], v[44:59]\n"
      "ds_read_b128 v[112:115], v0 offset:11264\n"
      "s_add_u32 s79, s67, 3466240\n"
      "s_add_u32 m0, s72, 86016\n"
      "s_nop 0\n"
      "buffer_load_dwordx4 v0, s[20:23], s79 offen offset:0 sc1 lds\n"
      "v_mfma_f32_32x32x16_bf16 v[60:75], v[96:99], a[120:123], v[60:75]\n"
      "ds_read_b128 v[92:95], v8 offset:4096\n"
      "s_waitcnt lgkmcnt(3)\n"
      "v_mfma_f32_32x32x16_bf16 v[116:131], v[100:103], a[120:123], v[116:131]\n"
      "ds_read_b128 v[96:99], v8 offset:24576\n"
      "s_waitcnt vmcnt(18)\n"
      "v_mfma_f32_32x32x16_bf16 v[44:59], v[104:107], a[124:127], v[44:59]\n"
      "ds_read_b128 v[100:103], v0 offset:12288\n"
      "s_add_u32 s79, s67, 3482624\n"
      "s_add_u32 m0, s72, 102400\n"
      "s_nop 0\n"
      "buffer_load_dwordx4 v0, s[20:23], s79 offen offset:0 sc1 lds\n"
      "s_waitcnt lgkmcnt(3)\n"
      "v_mfma_f32_32x32x16_bf16 v[60:75], v[108:111], a[124:127], v[60:75]\n"
      "ds_read_b128 v[104:107], v8 offset:5120\n"
      "v_mfma_f32_32x32x16_bf16 v[116:131], v[112:115], a[124:127], v[116:131]\n"
      "ds_read_b128 v[108:111], v8 offset:25600\n"
      "buffer_load_dwordx4 v0, s[20:23], s79 offen offset:1024 sc1 lds\n"
      "s_waitcnt vmcnt(19) lgkmcnt(3)\n"
      "v_mfma_f32_32x32x16_bf16 v[44:59], v[92:95], a[128:131], v[44:59]\n"
      "ds_read_b128 v[112:115], v0 offset:13312\n"
      "v_mfma_f32_32x32x16_bf16 v[60:75], v[96:99], a[128:131], v[60:75]\n"
      "ds_read_b128 v[92:95], v8 offset:6144\n"
      "buffer_load_dwordx4 v0, s[20:23], s79 offen offset:2048 sc1 lds\n"
      "s_waitcnt lgkmcnt(3)\n"
      "v_mfma_f32_32x32x16_bf16 v[116:131], v[100:103], a[128:131], v[116:131]\n"
      "ds_read_b128 v[96:99], v8 offset:26624\n"
      "s_waitcnt vmcnt(19)\n"
      "v_mfma_f32_32x32x16_bf16 v[44:59], v[104:107], a[132:135], v[44:59]\n"
      "ds_read_b128 v[100:103], v0 offset:14336\n"
      "s_waitcnt lgkmcnt(3)\n"
      "v_mfma_f32_32x32x16_bf16 v[60:75], v[108:111], a[132:135], v[60:75]\n"
      "ds_read_b128 v[104:107], v8 offset:7168\n"
      "buffer_load_dwordx4 v0, s[20:23], s79 offen offset:3072 sc1 lds\n"
      "v_mfma_f32_32x32x16_bf16 v[116:131], v[112:115], a[132:135], v[116:131]\n"
      "ds_read_b128 v[108:111], v8 offset:27648\n"
      "s_waitcnt vmcnt(19) lgkmcnt(3)\n"
      "v_mfma_f32_32x32x16_bf16 v[44:59], v[92:95], a[136:139], v[44:59]\n"
      "ds_read_b128 v[112:115], v0 offset:15360\n"
      "s_add_u32 s79, s67, 3486720\n"
      "s_add_u32 m0, s72, 106496\n"
      "s_nop 0\n"
      "buffer_load_dwordx4 v0, s[20:23], s79 offen offset:0 sc1 lds\n"
      "v_mfma_f32_32x32x16_bf16 v[60:75], v[96:99], a[136:139], v[60:75]\n"
      "ds_read_b128 v[92:95], v8 offset:8192\n"
      "s_waitcnt lgkmcnt(3)\n"
      "v_mfma_f32_32x32x16_bf16 v[116:131], v[100:103], a[136:139], v[116:131]\n"
      "ds_read_b128 v[96:99], v8 offset:28672\n"
      "s_waitcnt vmcnt(19)\n"
      "v_mfma_f32_32x32x16_bf16 v[44:59], v[104:107], a[140:143], v[44:59]\n"
      "ds_read_b128 v[100:103], v0 offset:16384\n"
      "buffer_load_dwordx4 a[248:251], v0, s[20:23], s78 offen offset:2048 sc1\n"
      "s_waitcnt lgkmcnt(3)\n"
      "v_mfma_f32_32x32x16_bf16 v[60:75], v[108:111], a[140:143], v[60:75]\n"
      "ds_read_b128 v[104:107], v8 offset:9216\n"
      "v_mfma_f32_32x32x16_bf16 v[116:131], v[112:115], a[140:143], v[116:131]\n"
      "ds_read_b128 v[108:111], v8 offset:29696\n"
      "buffer_load_dwordx4 a[252:255], v0, s[20:23], s78 offen offset:3072 sc1\n"
      "s_waitcnt vmcnt(20) lgkmcnt(3)\n"
      "v_mfma_f32_32x32x16_bf16 v[44:59], v[92:95], a[144:147], v[44:59]\n"
      "ds_read_b128 v[112:115], v0 offset:17408\n"
      "v_mfma_f32_32x32x16_bf16 v[60:75], v[96:99], a[144:147], v[60:75]\n"
      "ds_read_b128 v[92:95], v8 offset:10240\n"
      "s_add_u32 s78, s69, 7889920\n"
      "buffer_load_dwordx4 v[156:159], v0, s[20:23], s78 offen offset:0 sc1\n"
      "s_waitcnt lgkmcnt(3)\n"
      "v_mfma_f32_32x32x16_bf16 v[116:131], v[100:103], a[144:147], v[116:131]\n"
      "ds_read_b128 v[96:99], v8 offset:30720\n"
      "s_waitcnt vmcnt(20)\n"
      "v_mfma_f32_32x32x16_bf16 v[44:59], v[104:107], a[148:151], v[44:59]\n"
      "ds_read_b128 v[100:103], v0 offset:18432\n"
      "s_waitcnt lgkmcnt(3)\n"
      "v_mfma_f32_32x32x16_bf16 v[60:75], v[108:111], a[148:151], v[60:75]\n"
      "ds_read_b128 v[104:107], v8 offset:11264\n"
      "buffer_load_dwordx4 v[160:163], v0, s[20:23], s78 offen offset:1024 sc1\n"
      "v_mfma_f32_32x32x16_bf16 v[116:131], v[112:115], a[148:151], v[116:131]\n"
      "ds_read_b128 v[108:111], v8 offset:31744\n"
      "s_waitcnt vmcnt(20) lgkmcnt(3)\n"
      "v_mfma_f32_32x32x16_bf16 v[44:59], v[92:95], a[152:155], v[44:59]\n"
      "ds_read_b128 v[112:115], v0 offset:19456\n"
      "buffer_load_dwordx4 v[164:167], v0, s[20:23], s78 offen offset:2048 sc1\n"
      "v_mfma_f32_32x32x16_bf16 v[60:75], v[96:99], a[152:155], v[60:75]\n"
      "ds_read_b128 v[92:95], v0 offset:20480\n"
      "s_waitcnt lgkmcnt(3)\n"
      "v_mfma_f32_32x32x16_bf16 v[116:131], v[100:103], a[152:155], v[116:131]\n"
      "ds_read_b128 v[96:99], v0 offset:21504\n"
      "buffer_load_dwordx4 v[168:171], v0, s[20:23], s78 offen offset:3072 sc1\n"
      "s_waitcnt vmcnt(21)\n"
      "v_mfma_f32_32x32x16_bf16 v[44:59], v[104:107], a[156:159], v[44:59]\n"
      "ds_read_b128 v[100:103], v0 offset:22528\n"
      "s_waitcnt lgkmcnt(3)\n"
      "v_mfma_f32_32x32x16_bf16 v[60:75], v[108:111], a[156:159], v[60:75]\n"
      "ds_read_b128 v[104:107], v0 offset:23552\n"
      "v_mfma_f32_32x32x16_bf16 v[116:131], v[112:115], a[156:159], v[116:131]\n"
      "ds_read_b128 v[108:111], v0 offset:24576\n"
      "s_add_u32 s78, s69, 7894016\n"
      "buffer_load_dwordx4 v[172:175], v0, s[20:23], s78 offen offset:0 sc1\n"
      "s_waitcnt lgkmcnt(3)\n"
      "v_mfma_f32_32x32x16_bf16 v[76:91], v[92:95], a[0:3], 0\n"
      "ds_read_b128 v[112:115], v0 offset:25600\n"
      "v_mfma_f32_32x32x16_bf16 v[76:91], v[96:99], a[4:7], v[76:91]\n"
      "ds_read_b128 v[92:95], v0 offset:26624\n"
      "s_barrier\n"
      "v_mul_f32 v132, v44, v148\n"
      "v_mul_f32 v133, v45, v148\n"
      "s_waitcnt lgkmcnt(3)\n"
      "v_mfma_f32_32x32x16_bf16 v[76:91], v[100:103], a[8:11], v[76:91]\n"
      "ds_read_b128 v[96:99], v0 offset:27648\n"
      "v_exp_f32 v132, v132\n"
      "v_mul_f32 v134, v44, v52\n"
      "v_exp_f32 v133, v133\n"
      "v_mfma_f32_32x32x16_bf16 v[76:91], v[104:107], a[12:15], v[76:91]\n"
      "ds_read_b128 v[100:103], v0 offset:28672\n"
      "v_mul_f32 v135, v45, v53\n"
      "v_fma_f32 v132, v132, v150, v150\n"
      "v_rcp_f32 v132, v132\n"
      "s_waitcnt lgkmcnt(3)\n"
      "v_mfma_f32_32x32x16_bf16 v[76:91], v[108:111], a[16:19], v[76:91]\n"
      "ds_read_b128 v[104:107], v0 offset:29696\n"
      "v_fma_f32 v133, v133, v150, v150\n"
      "v_rcp_f32 v133, v133\n"
      "v_mul_f32 v132, v132, v134\n"
      "s_add_u32 s79, s67, 3503104\n"
      "s_add_u32 m0, s72, 122880\n"
      "s_nop 0\n"
      "buffer_load_dwordx4 v0, s[20:23], s79 offen offset:0 sc1 lds\n"
      "v_mfma_f32_32x32x16_bf16 v[76:91], v[112:115], a[20:23], v[76:91]\n"
      "ds_read_b128 v[108:111], v0 offset:30720\n"
      "v_mul_f32 v133, v133, v135\n"
      "v_cvt_pk_bf16_f32 v140, v132, v133\n"
      "s_waitcnt lgkmcnt(3)\n"
      "v_mfma_f32_32x32x16_bf16 v[76:91], v[92:95], a[24:27], v[76:91]\n"
      "ds_read_b128 v[112:115], v0 offset:31744\n"
      "v_mul_f32 v136, v46, v148\n"
      "v_mul_f32 v137, v47, v148\n"
      "v_exp_f32 v136, v136\n"
      "buffer_load_dwordx4 v0, s[20:23], s79 offen offset:1024 sc1 lds\n"
      "v_mfma_f32_32x32x16_bf16 v[76:91], v[96:99], a[28:31], v[76:91]\n"
      "ds_read_b128 v[92:95], v0 offset:32768\n"
      "v_mul_f32 v138, v46, v54\n"
      "v_exp_f32 v137, v137\n"
      "v_mul_f32 v139, v47, v55\n"
      "s_waitcnt lgkmcnt(3)\n"
      "v_mfma_f32_32x32x16_bf16 v[76:91], v[100:103], a[32:35], v[76:91]\n"
      "ds_read_b128 v[96:99], v0 offset:33792\n"
      "v_fma_f32 v136, v136, v150, v150\n"
      "v_rcp_f32 v136, v136\n"
      "v_fma_f32 v137, v137, v150, v150\n"
      "v_mfma_f32_32x32x16_bf16 v[76:91], v[104:107], a[36:39], v[76:91]\n"
      "ds_read_b128 v[100:103], v0 offset:34816\n"
      "v_rcp_f32 v137, v137\n"
      "v_mul_f32 v136, v136, v138\n"
      "buffer_load_dwordx4 v0, s[20:23], s79 offen offset:2048 sc1 lds\n"
      "s_waitcnt lgkmcnt(3)\n"
      "v_mfma_f32_32x32x16_bf16 v[76:91], v[108:111], a[40:43], v[76:91]\n"
      "ds_read_b128 v[104:107], v0 offset:35840\n"
      "v_mul_f32 v137, v137, v139\n"
      "v_cvt_pk_bf16_f32 v141, v136, v137\n"
      "v_mul_f32 v132, v48, v148\n"
      "v_mfma_f32_32x32x16_bf16 v[76:91], v[112:115], a[44:47], v[76:91]\n"
      "ds_read_b128 v[108:111], v0 offset:36864\n"
      "v_mul_f32 v133, v49, v148\n"
      "v_exp_f32 v132, v132\n"
      "v_mul_f32 v134, v48, v56\n"
      "buffer_load_dwordx4 v0, s[20:23], s79 offen offset:3072 sc1 lds\n"
      "s_waitcnt lgkmcnt(3)\n"
      "v_mfma_f32_32x32x16_bf16 v[76:91], v[92:95], a[48:51], v[76:91]\n"
      "ds_read_b128 v[112:115], v0 offset:37888\n"
      "v_exp_f32 v133, v133\n"
      "v_mul_f32 v135, v49, v57\n"
      "v_fma_f32 v132, v132, v150, v150\n"
      "v_mfma_f32_32x32x16_bf16 v[76:91], v[96:99], a[52:55], v[76:91]\n"
      "ds_read_b128 v[92:95], v0 offset:38912\n"
      "v_rcp_f32 v132, v132\n"
      "v_fma_f32 v133, v133, v150, v150\n"
      "s_add_u32 s79, s67, 3507200\n"
      "s_add_u32 m0, s72, 126976\n"
      "s_nop 0\n"
      "buffer_load_dwordx4 v0, s[20:23], s79 offen offset:0 sc1 lds\n"
      "s_waitcnt lgkmcnt(3)\n"
      "v_mfma_f32_32x32x16_bf16 v[76:91], v[100:103], a[56:59], v[76:91]\n"
      "ds_read_b128 v[96:99], v0 offset:39936\n"
      "v_rcp_f32 v133, v133\n"
      "v_mul_f32 v132, v132, v134\n"
      "v_mul_f32 v133, v133, v135\n"
      "v_mfma_f32_32x32x16_bf16 v[76:91], v[104:107], a[60:63], v[76:91]\n"
      "ds_read_b128 v[100:103], v0 offset:40960\n"
      "v_cvt_pk_bf16_f32 v142, v132, v133\n"
      "v_mul_f32 v136, v50, v148\n"
      "v_mul_f32 v137, v51, v148\n"
      "s_waitcnt lgkmcnt(3)\n"
      "v_mfma_f32_32x32x16_bf16 v[76:91], v[108:111], a[64:67], v[76:91]\n"
      "ds_read_b128 v[104:107], v0 offset:41984\n"
      "v_exp_f32 v136, v136\n"
      "v_mul_f32 v138, v50, v58\n"
      "v_exp_f32 v137, v137\n"
      "s_add_u32 s79, s67, 3523584\n"
      "s_add_u32 m0, s72, 143360\n"
      "s_nop 0\n"
      "buffer_load_dwordx4 v0, s[20:23], s79 offen offset:0 sc1 lds\n"
      "v_mfma_f32_32x32x16_bf16 v[76:91], v[112:115], a[68:71], v[76:91]\n"
      "ds_read_b128 v[108:111], v0 offset:43008\n"
      "v_mul_f32 v139, v51, v59\n"
      "v_fma_f32 v136, v136, v150, v150\n"
      "s_waitcnt lgkmcnt(3)\n"
      "v_mfma_f32_32x32x16_bf16 v[76:91], v[92:95], a[72:75], v[76:91]\n"
      "ds_read_b128 v[112:115], v0 offset:44032\n"
      "v_rcp_f32 v136, v136\n"
      "v_fma_f32 v137, v137, v150, v150\n"
      "v_rcp_f32 v137, v137\n"
      "buffer_load_dwordx4 v0, s[20:23], s79 offen offset:1024 sc1 lds\n"
      "v_mfma_f32_32x32x16_bf16 v[76:91], v[96:99], a[76:79], v[76:91]\n"
      "ds_read_b128 v[92:95], v0 offset:45056\n"
      "v_mul_f32 v136, v136, v138\n"
      "v_mul_f32 v137, v137, v139\n"
      "v_cvt_pk_bf16_f32 v143, v136, v137\n"
      "s_waitcnt lgkmcnt(3)\n"
      "v_mfma_f32_32x32x16_bf16 v[76:91], v[100:103], a[80:83], v[76:91]\n"
      "ds_read_b128 v[96:99], v0 offset:46080\n"
      "s_add_u32 s76, s61, 7864320\n"
      "buffer_store_dwordx4 v[140:143], v2, s[24:27], s76 offen offset:0 sc1\n"
      "v_mul_f32 v132, v60, v148\n"
      "v_mul_f32 v133, v61, v148\n"
      "v_mfma_f32_32x32x16_bf16 v[76:91], v[104:107], a[84:87], v[76:91]\n"
      "ds_read_b128 v[100:103], v0 offset:47104\n"
      "v_exp_f32 v132, v132\n"
      "v_mul_f32 v134, v60, v68\n"
      "buffer_load_dwordx4 v0, s[20:23], s79 offen offset:2048 sc1 lds\n"
      "s_waitcnt lgkmcnt(3)\n"
      "v_mfma_f32_32x32x16_bf16 v[76:91], v[108:111], a[88:91], v[76:91]\n"
      "ds_read_b128 v[104:107], v0 offset:48128\n"
      "v_exp_f32 v133, v133\n"
      "v_mul_f32 v135, v61, v69\n"
      "v_fma_f32 v132, v132, v150, v150\n"
      "v_mfma_f32_32x32x16_bf16 v[76:91], v[112:115], a[92:95], v[76:91]\n"
      "ds_read_b128 v[108:111], v0 offset:49152\n"
      "v_rcp_f32 v132, v132\n"
      "v_fma_f32 v133, v133, v150, v150\n"
      "v_rcp_f32 v133, v133\n"
      "buffer_load_dwordx4 v0, s[20:23], s79 offen offset:3072 sc1 lds\n"
      "s_waitcnt lgkmcnt(3)\n"
      "v_mfma_f32_32x32x16_bf16 v[76:91], v[92:95], a[96:99], v[76:91]\n"
      "ds_read_b128 v[112:115], v0 offset:50176\n"
      "v_mul_f32 v132, v132, v134\n"
      "v_mul_f32 v133, v133, v135\n"
      "v_cvt_pk_bf16_f32 v144, v132, v133\n"
      "v_mfma_f32_32x32x16_bf16 v[76:91], v[96:99], a[100:103], v[76:91]\n"
      "ds_read_b128 v[92:95], v0 offset:51200\n"
      "v_mul_f32 v136, v62, v148\n"
      "v_mul_f32 v137, v63, v148\n"
      "s_add_u32 s79, s67, 3527680\n"
      "s_add_u32 m0, s72, 147456\n"
      "s_nop 0\n"
      "buffer_load_dwordx4 v0, s[20:23], s79 offen offset:0 sc1 lds\n"
      "s_waitcnt lgkmcnt(3)\n"
      "v_mfma_f32_32x32x16_bf16 v[76:91], v[100:103], a[104:107], v[76:91]\n"
      "ds_read_b128 v[96:99], v0 offset:52224\n"
      "v_exp_f32 v136, v136\n"
      "v_mul_f32 v138, v62, v70\n"
      "v_exp_f32 v137, v137\n"
      "v_mfma_f32_32x32x16_bf16 v[76:91], v[104:107], a[108:111], v[76:91]\n"
      "ds_read_b128 v[100:103], v0 offset:53248\n"
      "v_mul_f32 v139, v63, v71\n"
      "v_fma_f32 v136, v136, v150, v150\n"
      "v_rcp_f32 v136, v136\n"
      "s_waitcnt lgkmcnt(3)\n"
      "v_mfma_f32_32x32x16_bf16 v[76:91], v[108:111], a[112:115], v[76:91]\n"
      "ds_read_b128 v[104:107], v0 offset:54272\n"
      "v_fma_f32 v137, v137, v150, v150\n"
      "v_rcp_f32 v137, v137\n"
      "v_mul_f32 v136, v136, v138\n"
      "s_add_u32 s79, s67, 3544064\n"
      "s_add_u32 m0, s72, 0\n"
      "s_nop 0\n"
      "buffer_load_dwordx4 v0, s[20:23], s79 offen offset:0 sc1 lds\n"
      "v_mfma_f32_32x32x16_bf16 v[76:91], v[112:115], a[116:119], v[76:91]\n"
      "ds_read_b128 v[108:111], v0 offset:55296\n"
      "v_mul_f32 v137, v137, v139\n"
      "v_cvt_pk_bf16_f32 v145, v136, v137\n"
      "s_waitcnt lgkmcnt(3)\n"
      "v_mfma_f32_32x32x16_bf16 v[76:91], v[92:95], a[120:123], v[76:91]\n"
      "ds_read_b128 v[112:115], v0 offset:56320\n"
      "v_mul_f32 v132, v64, v148\n"
      "v_mul_f32 v133, v65, v148\n"
      "v_exp_f32 v132, v132\n"
      "buffer_load_dwordx4 v0, s[20:23], s79 offen offset:1024 sc1 lds\n"
      "v_mfma_f32_32x32x16_bf16 v[76:91], v[96:99], a[124:127], v[76:91]\n"
      "ds_read_b128 v[92:95], v0 offset:57344\n"
      "v_mul_f32 v134, v64, v72\n"
      "v_exp_f32 v133, v133\n"
      "v_mul_f32 v135, v65, v73\n"
      "s_waitcnt lgkmcnt(3)\n"
      "v_mfma_f32_32x32x16_bf16 v[76:91], v[100:103], a[128:131], v[76:91]\n"
      "ds_read_b128 v[96:99], v0 offset:58368\n"
      "v_fma_f32 v132, v132, v150, v150\n"
      "v_rcp_f32 v132, v132\n"
      "v_fma_f32 v133, v133, v150, v150\n"
      "buffer_load_dwordx4 v0, s[20:23], s79 offen offset:2048 sc1 lds\n"
      "v_mfma_f32_32x32x16_bf16 v[76:91], v[104:107], a[132:135], v[76:91]\n"
      "ds_read_b128 v[100:103], v0 offset:59392\n"
      "v_rcp_f32 v133, v133\n"
      "v_mul_f32 v132, v132, v134\n"
      "s_waitcnt lgkmcnt(3)\n"
      "v_mfma_f32_32x32x16_bf16 v[76:91], v[108:111], a[136:139], v[76:91]\n"
      "ds_read_b128 v[104:107], v0 offset:60416\n"
      "v_mul_f32 v133, v133, v135\n"
      "v_cvt_pk_bf16_f32 v146, v132, v133\n"
      "v_mul_f32 v136, v66, v148\n"
      "s_waitcnt vmcnt(26)\n"
      "s_barrier\n"
      "v_mfma_f32_32x32x16_bf16 v[76:91], v[112:115], a[140:143], v[76:91]\n"
      "ds_read_b128 v[108:111], v0 offset:61440\n"
      "v_mul_f32 v137, v67, v148\n"
      "v_exp_f32 v136, v136\n"
      "v_mul_f32 v138, v66, v74\n"
      "buffer_load_dwordx4 v0, s[20:23], s79 offen offset:3072 sc1 lds\n"
      "s_waitcnt lgkmcnt(3)\n"
      "v_mfma_f32_32x32x16_bf16 v[76:91], v[92:95], a[144:147], v[76:91]\n"
      "ds_read_b128 v[112:115], v0 offset:62464\n"
      "v_exp_f32 v137, v137\n"
      "v_mul_f32 v139, v67, v75\n"
      "v_fma_f32 v136, v136, v150, v150\n"
      "v_mfma_f32_32x32x16_bf16 v[76:91], v[96:99], a[148:151], v[76:91]\n"
      "ds_read_b128 v[92:95], v0 offset:63488\n"
      "v_rcp_f32 v136, v136\n"
      "v_fma_f32 v137, v137, v150, v150\n"
      "s_add_u32 s79, s67, 3548160\n"
      "s_add_u32 m0, s72, 4096\n"
      "s_nop 0\n"
      "buffer_load_dwordx4 v0, s[20:23], s79 offen offset:0 sc1 lds\n"
      "s_waitcnt lgkmcnt(3)\n"
      "v_mfma_f32_32x32x16_bf16 v[76:91], v[100:103], a[152:155], v[76:91]\n"
      "ds_read_b128 v[96:99], v0 offset:64512\n"
      "v_rcp_f32 v137, v137\n"
      "v_mul_f32 v136, v136, v138\n"
      "v_mul_f32 v137, v137, v139\n"
      "v_mfma_f32_32x32x16_bf16 v[76:91], v[104:107], a[156:159], v[76:91]\n"
      "ds_read_b128 v[100:103], v7 offset:0\n"
      "v_cvt_pk_bf16_f32 v147, v136, v137\n"
      "s_add_u32 s76, s61, 7864832\n"
      "buffer_store_dwordx4 v[144:147], v2, s[24:27], s76 offen offset:0 sc1\n"
      "s_waitcnt lgkmcnt(3)\n"
      "v_mfma_f32_32x32x16_bf16 v[44:59], v[108:111], a[0:3], 0\n"
      "ds_read_b128 v[104:107], v7 offset:1024\n"
      "buffer_load_dwordx4 v[176:179], v0, s[20:23], s78 offen offset:1024 sc1\n"
      "v_mul_f32 v132, v116, v148\n"
      "v_mul_f32 v133, v117, v148\n"
      "v_mfma_f32_32x32x16_bf16 v[44:59], v[112:115], a[4:7], v[44:59]\n"
      "ds_read_b128 v[108:111], v7 offset:2048\n"
      "v_exp_f32 v132, v132\n"
      "v_mul_f32 v134, v116, v124\n"
      "v_exp_f32 v133, v133\n"
      "s_waitcnt lgkmcnt(3)\n"
      "v_mfma_f32_32x32x16_bf16 v[44:59], v[92:95], a[8:11], v[44:59]\n"
      "ds_read_b128 v[112:115], v7 offset:3072\n"
      "v_mul_f32 v135, v117, v125\n"
      "v_fma_f32 v132, v132, v150, v150\n"
      "v_rcp_f32 v132, v132\n"
      "v_mfma_f32_32x32x16_bf16 v[44:59], v[96:99], a[12:15], v[44:59]\n"
      "ds_read_b128 v[92:95], v7 offset:4096\n"
      "v_fma_f32 v133, v133, v150, v150\n"
      "v_rcp_f32 v133, v133\n"
      "v_mul_f32 v132, v132, v134\n"
      "buffer_load_dwordx4 v[180:183], v0, s[20:23], s78 offen offset:2048 sc1\n"
      "s_waitcnt lgkmcnt(3)\n"
      "v_mfma_f32_32x32x16_bf16 v[44:59], v[100:103], a[16:19], v[44:59]\n"
      "ds_read_b128 v[96:99], v7 offset:5120\n"
      "v_mul_f32 v133, v133, v135\n"
      "v_cvt_pk_bf16_f32 v140, v132, v133\n"
      "v_mfma_f32_32x32x16_bf16 v[44:59], v[104:107], a[20:23], v[44:59]\n"
      "ds_read_b128 v[100:103], v7 offset:6144\n"
      "v_mul_f32 v136, v118, v148\n"
      "v_mul_f32 v137, v119, v148\n"
      "v_exp_f32 v136, v136\n"
      "buffer_load_dwordx4 v[184:187], v0, s[20:23], s78 offen offset:3072 sc1\n"
      "s_waitcnt lgkmcnt(3)\n"
      "v_mfma_f32_32x32x16_bf16 v[44:59], v[108:111], a[24:27], v[44:59]\n"
      "ds_read_b128 v[104:107], v7 offset:7168\n"
      "v_mul_f32 v138, v118, v126\n"
      "v_exp_f32 v137, v137\n"
      "v_mul_f32 v139, v119, v127\n"
      "v_mfma_f32_32x32x16_bf16 v[44:59], v[112:115], a[28:31], v[44:59]\n"
      "ds_read_b128 v[108:111], v7 offset:8192\n"
      "v_fma_f32 v136, v136, v150, v150\n"
      "v_rcp_f32 v136, v136\n"
      "v_fma_f32 v137, v137, v150, v150\n"
      "s_waitcnt lgkmcnt(3)\n"
      "v_mfma_f32_32x32x16_bf16 v[44:59], v[92:95], a[32:35], v[44:59]\n"
      "ds_read_b128 v[112:115], v7 offset:9216\n"
      "v_rcp_f32 v137, v137\n"
      "v_mul_f32 v136, v136, v138\n"
      "s_add_u32 s78, s69, 7898112\n"
      "buffer_load_dwordx4 v[188:191], v0, s[20:23], s78 offen offset:0 sc1\n"
      "v_mfma_f32_32x32x16_bf16 v[44:59], v[96:99], a[36:39], v[44:59]\n"
      "ds_read_b128 v[92:95], v7 offset:10240\n"
      "v_mul_f32 v137, v137, v139\n"
      "v_cvt_pk_bf16_f32 v141, v136, v137\n"
      "v_mul_f32 v132, v120, v148\n"
      "s_waitcnt lgkmcnt(3)\n"
      "v_mfma_f32_32x32x16_bf16 v[44:59], v[100:103], a[40:43], v[44:59]\n"
      "ds_read_b128 v[96:99], v7 offset:11264\n"
      "v_mul_f32 v133, v121, v148\n"
      "v_exp_f32 v132, v132\n"
      "v_mul_f32 v134, v120, v128\n"
      "buffer_load_dwordx4 v[192:195], v0, s[20:23], s78 offen offset:1024 sc1\n"
      "v_mfma_f32_32x32x16_bf16 v[44:59], v[104:107], a[44:47], v[44:59]\n"
      "ds_read_b128 v[100:103], v7 offset:12288\n"
      "v_exp_f32 v133, v133\n"
      "v_mul_f32 v135, v121, v129\n"
      "v_fma_f32 v132, v132, v150, v150\n"
      "s_waitcnt lgkmcnt(3)\n"
      "v_mfma_f32_32x32x16_bf16 v[44:59], v[108:111], a[48:51], v[44:59]\n"
      "ds_read_b128 v[104:107], v7 offset:13312\n"
      "v_rcp_f32 v132, v132\n"
      "v_fma_f32 v133, v133, v150, v150\n"
      "buffer_load_dwordx4 v[196:199], v0, s[20:23], s78 offen offset:2048 sc1\n"
      "v_mfma_f32_32x32x16_bf16 v[44:59], v[112:115], a[52:55], v[44:59]\n"
      "ds_read_b128 v[108:111], v7 offset:14336\n"
      "v_rcp_f32 v133, v133\n"
      "v_mul_f32 v132, v132, v134\n"
      "v_mul_f32 v133, v133, v135\n"
      "s_waitcnt lgkmcnt(3)\n"
      "v_mfma_f32_32x32x16_bf16 v[44:59], v[92:95], a[56:59], v[44:59]\n"
      "ds_read_b128 v[112:115], v7 offset:15360\n"
      "v_cvt_pk_bf16_f32 v142, v132, v133\n"
      "v_mul_f32 v136, v122, v148\n"
      "v_mul_f32 v137, v123, v148\n"
      "v_mfma_f32_32x32x16_bf16 v[44:59], v[96:99], a[60:63], v[44:59]\n"
      "ds_read_b128 v[92:95], v7 offset:16384\n"
      "v_exp_f32 v136, v136\n"
      "v_mul_f32 v138, v122, v130\n"
      "v_exp_f32 v137, v137\n"
      "buffer_load_dwordx4 v[200:203], v0, s[20:23], s78 offen offset:3072 sc1\n"
      "s_waitcnt lgkmcnt(3)\n"
      "v_mfma_f32_32x32x16_bf16 v[44:59], v[100:103], a[64:67], v[44:59]\n"
      "ds_read_b128 v[96:99], v7 offset:17408\n"
      "v_mul_f32 v139, v123, v131\n"
      "v_fma_f32 v136, v136, v150, v150\n"
      "v_mfma_f32_32x32x16_bf16 v[44:59], v[104:107], a[68:71], v[44:59]\n"
      "ds_read_b128 v[100:103], v7 offset:18432\n"
      "v_rcp_f32 v136, v136\n"
      "v_fma_f32 v137, v137, v150, v150\n"
      "v_rcp_f32 v137, v137\n"
      "s_add_u32 s78, s69, 7902208\n"
      "buffer_load_dwordx4 v[204:207], v0, s[20:23], s78 offen offset:0 sc1\n"
      "s_waitcnt lgkmcnt(3)\n"
      "v_mfma_f32_32x32x16_bf16 v[44:59], v[108:111], a[72:75], v[44:59]\n"
      "ds_read_b128 v[104:107], v7 offset:19456\n"
      "v_mul_f32 v136, v136, v138\n"
      "v_mul_f32 v137, v137, v139\n"
      "v_cvt_pk_bf16_f32 v143, v136, v137\n"
      "s_waitcnt vmcnt(8)\n"
      "s_lshr_b32 s80, s74, 5\n"
      "s_add_u32 s80, s80, s73\n"
      "s_barrier\n"
      "s_cmp_lg_u32 s40, 0\n"
      "s_cbranch_scc1 Lsg8_%=\n"
      "s_add_u32 s80, s80, 16023552\n"
      "s_mov_b64 exec, 1\n"
      "buffer_store_dword v10, v9, s[24:27], s80 offen sc1\n"
      "s_mov_b64 exec, -1\n"
      "Lsg8_%=:\n"
      "v_mfma_f32_32x32x16_bf16 v[44:59], v[112:115], a[76:79], v[44:59]\n"
      "ds_read_b128 v[108:111], v7 offset:20480\n"
      "s_add_u32 s76, s61, 7868416\n"
      "buffer_store_dwordx4 v[140:143], v2, s[24:27], s76 offen offset:0 sc1\n"
      "v_mul_f32 v132, v76, v148\n"
      "v_mul_f32 v133, v77, v148\n"
      "s_waitcnt lgkmcnt(3)\n"
      "v_mfma_f32_32x32x16_bf16 v[44:59], v[92:95], a[80:83], v[44:59]\n"
      "ds_read_b128 v[112:115], v7 offset:21504\n"
      "v_exp_f32 v132, v132\n"
      "v_mul_f32 v134, v76, v84\n"
      "v_mfma_f32_32x32x16_bf16 v[44:59], v[96:99], a[84:87], v[44:59]\n"
      "ds_read_b128 v[92:95], v7 offset:22528\n"
      "v_exp_f32 v133, v133\n"
      "v_mul_f32 v135, v77, v85\n"
      "v_fma_f32 v132, v132, v150, v150\n"
      "buffer_load_dwordx4 v[208:211], v0, s[20:23], s78 offen offset:1024 sc1\n"
      "s_waitcnt lgkmcnt(3)\n"
      "v_mfma_f32_32x32x16_bf16 v[44:59], v[100:103], a[88:91], v[44:59]\n"
      "ds_read_b128 v[96:99], v7 offset:23552\n"
      "v_rcp_f32 v132, v132\n"
      "v_fma_f32 v133, v133, v150, v150\n"
      "v_rcp_f32 v133, v133\n"
      "v_mfma_f32_32x32x16_bf16 v[44:59], v[104:107], a[92:95], v[44:59]\n"
      "ds_read_b128 v[100:103], v7 offset:24576\n"
      "v_mul_f32 v132, v132, v134\n"
      "v_mul_f32 v133, v133, v135\n"
      "v_cvt_pk_bf16_f32 v144, v132, v133\n"
      "buffer_load_dwordx4 v[212:215], v0, s[20:23], s78 offen offset:2048 sc1\n"
      "s_waitcnt lgkmcnt(3)\n"
      "v_mfma_f32_32x32x16_bf16 v[44:59], v[108:111], a[96:99], v[44:59]\n"
      "ds_read_b128 v[104:107], v7 offset:25600\n"
      "v_mul_f32 v136, v78, v148\n"
      "v_mul_f32 v137, v79, v148\n"
      "v_mfma_f32_32x32x16_bf16 v[44:59], v[112:115], a[100:103], v[44:59]\n"
      "ds_read_b128 v[108:111], v7 offset:26624\n"
      "v_exp_f32 v136, v136\n"
      "v_mul_f32 v138, v78, v86\n"
      "v_exp_f32 v137, v137\n"
      "s_waitcnt lgkmcnt(3)\n"
      "v_mfma_f32_32x32x16_bf16 v[44:59], v[92:95], a[104:107], v[44:59]\n"
      "ds_read_b128 v[112:115], v7 offset:27648\n"
      "v_mul_f32 v139, v79, v87\n"
      "v_fma_f32 v136, v136, v150, v150\n"
      "v_rcp_f32 v136, v136\n"
      "buffer_load_dwordx4 v[216:219], v0, s[20:23], s78 offen offset:3072 sc1\n"
      "v_mfma_f32_32x32x16_bf16 v[44:59], v[96:99], a[108:111], v[44:59]\n"
      "ds_read_b128 v[92:95], v7 offset:28672\n"
      "v_fma_f32 v137, v137, v150, v150\n"
      "v_rcp_f32 v137, v137\n"
      "v_mul_f32 v136, v136, v138\n"
      "s_waitcnt lgkmcnt(3)\n"
      "v_mfma_f32_32x32x16_bf16 v[44:59], v[100:103], a[112:115], v[44:59]\n"
      "ds_read_b128 v[96:99], v7 offset:29696\n"
      "v_mul_f32 v137, v137, v139\n"
      "v_cvt_pk_bf16_f32 v145, v136, v137\n"
      "s_add_u32 s78, s69, 7906304\n"
      "buffer_load_dwordx4 v[220:223], v0, s[20:23], s78 offen offset:0 sc1\n"
      "v_mfma_f32_32x32x16_bf16 v[44:59], v[104:107], a[116:119], v[44:59]\n"
      "ds_read_b128 v[100:103], v7 offset:30720\n"
      "v_mul_f32 v132, v80, v148\n"
      "v_mul_f32 v133, v81, v148\n"
      "v_exp_f32 v132, v132\n"
      "s_waitcnt lgkmcnt(3)\n"
      "v_mfma_f32_32x32x16_bf16 v[44:59], v[108:111], a[120:123], v[44:59]\n"
      "ds_read_b128 v[104:107], v7 offset:31744\n"
      "v_mul_f32 v134, v80, v88\n"
      "v_exp_f32 v133, v133\n"
      "v_mul_f32 v135, v81, v89\n"
      "buffer_load_dwordx4 v[224:227], v0, s[20:23], s78 offen offset:1024 sc1\n"
      "v_mfma_f32_32x32x16_bf16 v[44:59], v[112:115], a[124:127], v[44:59]\n"
      "ds_read_b128 v[108:111], v7 offset:32768\n"
      "v_fma_f32 v132, v132, v150, v150\n"
      "v_rcp_f32 v132, v132\n"
      "v_fma_f32 v133, v133, v150, v150\n"
      "s_waitcnt lgkmcnt(3)\n"
      "v_mfma_f32_32x32x16_bf16 v[44:59], v[92:95], a[128:131], v[44:59]\n"
      "ds_read_b128 v[112:115], v7 offset:33792\n"
      "v_rcp_f32 v133, v133\n"
      "v_mul_f32 v132, v132, v134\n"
      "v_mfma_f32_32x32x16_bf16 v[44:59], v[96:99], a[132:135], v[44:59]\n"
      "ds_read_b128 v[92:95], v7 offset:34816\n"
      "v_mul_f32 v133, v133, v135\n"
      "v_cvt_pk_bf16_f32 v146, v132, v133\n"
      "v_mul_f32 v136, v82, v148\n"
      "buffer_load_dwordx4 v[228:231], v0, s[20:23], s78 offen offset:2048 sc1\n"
      "s_waitcnt lgkmcnt(3)\n"
      "v_mfma_f32_32x32x16_bf16 v[44:59], v[100:103], a[136:139], v[44:59]\n"
      "ds_read_b128 v[96:99], v7 offset:35840\n"
      "v_mul_f32 v137, v83, v148\n"
      "v_exp_f32 v136, v136\n"
      "v_mul_f32 v138, v82, v90\n"
      "s_barrier\n"
      "v_mfma_f32_32x32x16_bf16 v[44:59], v[104:107], a[140:143], v[44:59]\n"
      "ds_read_b128 v[100:103], v7 offset:36864\n"
      "v_exp_f32 v137, v137\n"
      "v_mul_f32 v139, v83, v91\n"
      "v_fma_f32 v136, v136, v150, v150\n"
      "buffer_load_dwordx4 v[232:235], v0, s[20:23], s78 offen offset:3072 sc1\n"
      "s_waitcnt lgkmcnt(3)\n"
      "v_mfma_f32_32x32x16_bf16 v[44:59], v[108:111], a[144:147], v[44:59]\n"
      "ds_read_b128 v[104:107], v7 offset:37888\n"
      "v_rcp_f32 v136, v136\n"
      "v_fma_f32 v137, v137, v150, v150\n"
      "v_mfma_f32_32x32x16_bf16 v[44:59], v[112:115], a[148:151], v[44:59]\n"
      "ds_read_b128 v[108:111], v7 offset:38912\n"
      "v_rcp_f32 v137, v137\n"
      "v_mul_f32 v136, v136, v138\n"
      "v_mul_f32 v137, v137, v139\n"
      "s_waitcnt lgkmcnt(3)\n"
      "v_mfma_f32_32x32x16_bf16 v[44:59], v[92:95], a[152:155], v[44:59]\n"
      "ds_read_b128 v[112:115], v7 offset:39936\n"
      "v_cvt_pk_bf16_f32 v147, v136, v137\n"
      "s_add_u32 s76, s61, 7868928\n"
      "buffer_store_dwordx4 v[144:147], v2, s[24:27], s76 offen offset:0 sc1\n"
      "s_add_u32 s78, s69, 7910400\n"
      "buffer_load_dwordx4 v[236:239], v0, s[20:23], s78 offen offset:0 sc1\n"
      "v_mfma_f32_32x32x16_bf16 v[44:59], v[96:99], a[156:159], v[44:59]\n"
      "ds_read_b128 v[92:95], v7 offset:40960\n"
      "s_waitcnt lgkmcnt(3)\n"
      "v_mfma_f32_32x32x16_bf16 v[60:75], v[100:103], a[0:3], 0\n"
      "ds_read_b128 v[96:99], v7 offset:41984\n"
      "buffer_load_dwordx4 v[240:243], v0, s[20:23], s78 offen offset:1024 sc1\n"
      "v_mfma_f32_32x32x16_bf16 v[60:75], v[104:107], a[4:7], v[60:75]\n"
      "ds_read_b128 v[100:103], v7 offset:43008\n"
      "s_waitcnt lgkmcnt(3)\n"
      "v_mfma_f32_32x32x16_bf16 v[60:75], v[108:111], a[8:11], v[60:75]\n"
      "ds_read_b128 v[104:107], v7 offset:44032\n"
      "buffer_load_dwordx4 v[244:247], v0, s[20:23], s78 offen offset:2048 sc1\n"
      "s_nop 0\n"
      "v_mul_f32 v132, v44, v148\n"
      "v_mfma_f32_32x32x16_bf16 v[60:75], v[112:115], a[12:15], v[60:75]\n"
      "ds_read_b128 v[108:111], v7 offset:45056\n"
      "v_mul_f32 v133, v45, v148\n"
      "s_waitcnt lgkmcnt(3)\n"
      "v_mfma_f32_32x32x16_bf16 v[60:75], v[92:95], a[16:19], v[60:75]\n"
      "ds_read_b128 v[112:115], v7 offset:46080\n"
      "v_exp_f32 v132, v132\n"
      "v_mfma_f32_32x32x16_bf16 v[60:75], v[96:99], a[20:23], v[60:75]\n"
      "ds_read_b128 v[92:95], v7 offset:47104\n"
      "buffer_load_dwordx4 v[248:251], v0, s[20:23], s78 offen offset:3072 sc1\n"
      "v_mul_f32 v134, v44, v52\n"
      "s_waitcnt lgkmcnt(3)\n"
      "v_mfma_f32_32x32x16_bf16 v[60:75], v[100:103], a[24:27], v[60:75]\n"
      "ds_read_b128 v[96:99], v7 offset:48128\n"
      "v_exp_f32 v133, v133\n"
      "v_mul_f32 v135, v45, v53\n"
      "v_mfma_f32_32x32x16_bf16 v[60:75], v[104:107], a[28:31], v[60:75]\n"
      "ds_read_b128 v[100:103], v7 offset:49152\n"
      "s_add_u32 s78, s69, 7914496\n"
      "buffer_load_dwordx4 v[252:255], v0, s[20:23], s78 offen offset:0 sc1\n"
      "v_fma_f32 v132, v132, v150, v150\n"
      "s_waitcnt lgkmcnt(3)\n"
      "v_mfma_f32_32x32x16_bf16 v[60:75], v[108:111], a[32:35], v[60:75]\n"
      "ds_read_b128 v[104:107], v7 offset:50176\n"
      "v_rcp_f32 v132, v132\n"
      "v_mfma_f32_32x32x16_bf16 v[60:75], v[112:115], a[36:39], v[60:75]\n"
      "ds_read_b128 v[108:111], v7 offset:51200\n"
      "s_add_u32 s79, s65, 20480\n"
      "s_add_u32 m0, s72, 20480\n"
      "s_nop 0\n"
      "buffer_load_dwordx4 v0, s[36:39], s79 offen offset:0 sc1 lds\n"
      "v_fma_f32 v133, v133, v150, v150\n"
      "v_mov_b32 v152, v0\n"
      "s_add_u32 s98, s73, 16023552\n"
      "s_mov_b64 exec, 0xffff\n"
      "buffer_load_dword v152, v152, s[24:27], s98 offen sc1\n"
      "s_mov_b64 exec, -1\n"
      "s_waitcnt lgkmcnt(3)\n"
      "v_mfma_f32_32x32x16_bf16 v[60:75], v[92:95], a[40:43], v[60:75]\n"
      "ds_read_b128 v[112:115], v7 offset:52224\n"
      "v_rcp_f32 v133, v133\n"
      "v_mfma_f32_32x32x16_bf16 v[60:75], v[96:99], a[44:47], v[60:75]\n"
      "ds_read_b128 v[92:95], v7 offset:53248\n"
      "v_mul_f32 v132, v132, v134\n"
      "v_mul_f32 v133, v133, v135\n"
      "s_waitcnt lgkmcnt(3)\n"
      "v_mfma_f32_32x32x16_bf16 v[60:75], v[100:103], a[48:51], v[60:75]\n"
      "ds_read_b128 v[96:99], v7 offset:54272\n"
      "buffer_load_dwordx4 v0, s[36:39], s79 offen offset:1024 sc1 lds\n"
      "v_cvt_pk_bf16_f32 v140, v132, v133\n"
      "v_mfma_f32_32x32x16_bf16 v[60:75], v[104:107], a[52:55], v[60:75]\n"
      "ds_read_b128 v[100:103], v7 offset:55296\n"
      "v_mul_f32 v136, v46, v148\n"
      "s_waitcnt lgkmcnt(3)\n"
      "v_mfma_f32_32x32x16_bf16 v[60:75], v[108:111], a[56:59], v[60:75]\n"
      "ds_read_b128 v[104:107], v7 offset:56320\n"
      "buffer_load_dwordx4 v0, s[36:39], s79 offen offset:2048 sc1 lds\n"
      "v_mul_f32 v137, v47, v148\n"
      "v_mfma_f32_32x32x16_bf16 v[60:75], v[112:115], a[60:63], v[60:75]\n"
      "ds_read_b128 v[108:111], v7 offset:57344\n"
      "v_exp_f32 v136, v136\n"
      "s_waitcnt lgkmcnt(3)\n"
      "v_mfma_f32_32x32x16_bf16 v[60:75], v[92:95], a[64:67], v[60:75]\n"
      "ds_read_b128 v[112:115], v7 offset:58368\n"
      "v_mul_f32 v138, v46, v54\n"
      "v_exp_f32 v137, v137\n"
      "v_mfma_f32_32x32x16_bf16 v[60:75], v[96:99], a[68:71], v[60:75]\n"
      "ds_read_b128 v[92:95], v7 offset:59392\n"
      "buffer_load_dwordx4 v0, s[36:39], s79 offen offset:3072 sc1 lds\n"
      "v_mul_f32 v139, v47, v55\n"
      "s_waitcnt lgkmcnt(3)\n"
      "v_mfma_f32_32x32x16_bf16 v[60:75], v[100:103], a[72:75], v[60:75]\n"
      "ds_read_b128 v[96:99], v7 offset:60416\n"
      "v_fma_f32 v136, v136, v150, v150\n"
      "s_waitcnt vmcnt(10)\n"
      "s_lshr_b32 s80, s74, 5\n"
      "s_add_u32 s80, s80, s73\n"
      "s_barrier\n"
      "s_cmp_lg_u32 s40, 0\n"
      "s_cbranch_scc1 Lsg9_%=\n"
      "s_add_u32 s80, s80, 16031744\n"
      "s_mov_b64 exec, 1\n"
      "buffer_store_dword v10, v9, s[24:27], s80 offen sc1\n"
      "s_mov_b64 exec, -1\n"
      "Lsg9_%=:\n"
      "v_mfma_f32_32x32x16_bf16 v[60:75], v[104:107], a[76:79], v[60:75]\n"
      "ds_read_b128 v[100:103], v7 offset:61440\n"
      "s_add_u32 s79, s65, 24576\n"
      "s_add_u32 m0, s72, 24576\n"
      "s_nop 0\n"
      "buffer_load_dwordx4 v0, s[36:39], s79 offen offset:0 sc1 lds\n"
      "v_rcp_f32 v136, v136\n"
      "s_waitcnt lgkmcnt(3)\n"
      "v_mfma_f32_32x32x16_bf16 v[60:75], v[108:111], a[80:83], v[60:75]\n"
      "ds_read_b128 v[104:107], v7 offset:62464\n"
      "v_fma_f32 v137, v137, v150, v150\n"
      "v_mfma_f32_32x32x16_bf16 v[60:75], v[112:115], a[84:87], v[60:75]\n"
      "ds_read_b128 v[108:111], v7 offset:63488\n"
      "v_rcp_f32 v137, v137\n"
      "v_mul_f32 v136, v136, v138\n"
      "s_waitcnt lgkmcnt(3)\n"
      "v_mfma_f32_32x32x16_bf16 v[60:75], v[92:95], a[88:91], v[60:75]\n"
      "ds_read_b128 v[112:115], v8 offset:12288\n"
      "v_mul_f32 v137, v137, v139\n"
      "v_mfma_f32_32x32x16_bf16 v[60:75], v[96:99], a[92:95], v[60:75]\n"
      "ds_read_b128 v[92:95], v7 offset:64512\n"
      "v_cvt_pk_bf16_f32 v141, v136, v137\n"
      "s_waitcnt lgkmcnt(3)\n"
      "v_mfma_f32_32x32x16_bf16 v[60:75], v[100:103], a[96:99], v[60:75]\n"
      "ds_read_b128 v[96:99], v8 offset:13312\n"
      "v_mul_f32 v132, v48, v148\n"
      "v_mfma_f32_32x32x16_bf16 v[60:75], v[104:107], a[100:103], v[60:75]\n"
      "ds_read_b128 v[100:103], v8 offset:0\n"
      "v_mul_f32 v133, v49, v148\n"
      "s_waitcnt lgkmcnt(3)\n"
      "v_mfma_f32_32x32x16_bf16 v[60:75], v[108:111], a[104:107], v[60:75]\n"
      "ds_read_b128 v[104:107], v8 offset:14336\n"
      "v_exp_f32 v132, v132\n"
      "v_mul_f32 v134, v48, v56\n"
      "v_mfma_f32_32x32x16_bf16 v[116:131], v[112:115], a[0:3], 0\n"
      "ds_read_b128 v[108:111], v8 offset:1024\n"
      "v_exp_f32 v133, v133\n"
      "s_waitcnt vmcnt(4)\n"
      "v_cmp_eq_u32 vcc, 0, v152\n"
      "s_cbranch_vccz Lpd10_%=\n"
      "s_add_u32 s80, s73, 16023552\n"
      "s_mov_b32 s84, 400000\n"
      "v_mov_b32 v152, v0\n"
      "s_mov_b64 exec, 0xffff\n"
      "Lpl10_%=:\n"
      "buffer_load_dword v11, v152, s[24:27], s80 offen sc1\n"
      "s_waitcnt vmcnt(0)\n"
      "v_cmp_eq_u32 vcc, 0, v11\n"
      "s_cbranch_vccz Lpx10_%=\n"
      "s_sub_u32 s84, s84, 1\n"
      "s_cmp_lg_u32 s84, 0\n"
      "s_cbranch_scc1 Lpl10_%=\n"
      "Lpx10_%=:\n"
      "s_mov_b64 exec, -1\n"
      "Lpd10_%=:\n"
      "s_waitcnt lgkmcnt(3)\n"
      "v_mfma_f32_32x32x16_bf16 v[60:75], v[92:95], a[108:111], v[60:75]\n"
      "ds_read_b128 v[112:115], v8 offset:15360\n"
      "v_mul_f32 v135, v49, v57\n"
      "v_mfma_f32_32x32x16_bf16 v[116:131], v[96:99], a[4:7], v[116:131]\n"
      "ds_read_b128 v[92:95], v8 offset:2048\n"
      "v_fma_f32 v132, v132, v150, v150\n"
      "s_add_u32 s75, s51, 7864320\n"
      "buffer_load_dwordx4 a[0:3], v0, s[24:27], s75 offen offset:0 sc1\n"
      "s_waitcnt lgkmcnt(3)\n"
      "v_mfma_f32_32x32x16_bf16 v[60:75], v[100:103], a[112:115], v[60:75]\n"
      "ds_read_b128 v[96:99], v8 offset:16384\n"
      "v_rcp_f32 v132, v132\n"
      "v_mfma_f32_32x32x16_bf16 v[116:131], v[104:107], a[8:11], v[116:131]\n"
      "ds_read_b128 v[100:103], v8 offset:3072\n"
      "v_fma_f32 v133, v133, v150, v150\n"
      "v_rcp_f32 v133, v133\n"
      "s_add_u32 s77, s52, 7864320\n"
      "buffer_load_dwordx4 a[4:7], v0, s[24:27], s77 offen offset:0 sc1\n"
      "s_waitcnt lgkmcnt(3)\n"
      "v_mfma_f32_32x32x16_bf16 v[60:75], v[108:111], a[116:119], v[60:75]\n"
      "ds_read_b128 v[104:107], v8 offset:17408\n"
      "v_mul_f32 v132, v132, v134\n"
      "v_mfma_f32_32x32x16_bf16 v[116:131], v[112:115], a[12:15], v[116:131]\n"
      "ds_read_b128 v[108:111], v8 offset:4096\n"
      "v_mul_f32 v133, v133, v135\n"
      "s_add_u32 s90, s53, 7864320\n"
      "buffer_load_dwordx4 a[8:11], v0, s[24:27], s90 offen offset:0 sc1\n"
      "s_waitcnt lgkmcnt(3)\n"
      "v_mfma_f32_32x32x16_bf16 v[60:75], v[92:95], a[120:123], v[60:75]\n"
      "ds_read_b128 v[112:115], v8 offset:18432\n"
      "v_cvt_pk_bf16_f32 v142, v132, v133\n"
      "v_mfma_f32_32x32x16_bf16 v[116:131], v[96:99], a[16:19], v[116:131]\n"
      "ds_read_b128 v[92:95], v8 offset:5120\n"
      "v_mul_f32 v136, v50, v148\n"
      "s_add_u32 s91, s54, 7864320\n"
      "buffer_load_dwordx4 a[12:15], v0, s[24:27], s91 offen offset:0 sc1\n"
      "s_waitcnt lgkmcnt(3)\n"
      "v_mfma_f32_32x32x16_bf16 v[60:75], v[100:103], a[124:127], v[60:75]\n"
      "ds_read_b128 v[96:99], v8 offset:19456\n"
      "v_mul_f32 v137, v51, v148\n"
      "v_exp_f32 v136, v136\n"
      "v_mfma_f32_32x32x16_bf16 v[116:131], v[104:107], a[20:23], v[116:131]\n"
      "ds_read_b128 v[100:103], v8 offset:6144\n"
      "v_mul_f32 v138, v50, v58\n"
      "buffer_load_dwordx4 a[16:19], v0, s[24:27], s75 offen offset:1024 sc1\n"
      "s_waitcnt lgkmcnt(3)\n"
      "v_mfma_f32_32x32x16_bf16 v[60:75], v[108:111], a[128:131], v[60:75]\n"
      "ds_read_b128 v[104:107], v8 offset:20480\n"
      "v_exp_f32 v137, v137\n"
      "v_mfma_f32_32x32x16_bf16 v[116:131], v[112:115], a[24:27], v[116:131]\n"
      "ds_read_b128 v[108:111], v8 offset:7168\n"
      "v_mul_f32 v139, v51, v59\n"
      "buffer_load_dwordx4 a[20:23], v0, s[24:27], s77 offen offset:1024 sc1\n"
      "s_waitcnt lgkmcnt(3)\n"
      "v_mfma_f32_32x32x16_bf16 v[60:75], v[92:95], a[132:135], v[60:75]\n"
      "ds_read_b128 v[112:115], v8 offset:21504\n"
      "v_fma_f32 v136, v136, v150, v150\n"
      "v_mfma_f32_32x32x16_bf16 v[116:131], v[96:99], a[28:31], v[116:131]\n"
      "ds_read_b128 v[92:95], v8 offset:8192\n"
      "v_rcp_f32 v136, v136\n"
      "v_fma_f32 v137, v137, v150, v150\n"
      "buffer_load_dwordx4 a[24:27], v0, s[24:27], s90 offen offset:1024 sc1\n"
      "s_waitcnt lgkmcnt(3)\n"
      "v_mfma_f32_32x32x16_bf16 v[60:75], v[100:103], a[136:139], v[60:75]\n"
      "ds_read_b128 v[96:99], v8 offset:22528\n"
      "v_rcp_f32 v137, v137\n"
      "v_mfma_f32_32x32x16_bf16 v[116:131], v[104:107], a[32:35], v[116:131]\n"
      "ds_read_b128 v[100:103], v8 offset:9216\n"
      "v_mul_f32 v136, v136, v138\n"
      "buffer_load_dwordx4 a[28:31], v0, s[24:27], s91 offen offset:1024 sc1\n"
      "s_waitcnt lgkmcnt(3)\n"
      "v_mfma_f32_32x32x16_bf16 v[60:75], v[108:111], a[140:143], v[60:75]\n"
      "ds_read_b128 v[104:107], v8 offset:23552\n"
      "v_mul_f32 v137, v137, v139\n"
      "v_mfma_f32_32x32x16_bf16 v[116:131], v[112:115], a[36:39], v[116:131]\n"
      "ds_read_b128 v[108:111], v8 offset:10240\n"
      "v_cvt_pk_bf16_f32 v143, v136, v137\n"
      "buffer_load_dwordx4 a[32:35], v0, s[24:27], s75 offen offset:2048 sc1\n"
      "s_waitcnt lgkmcnt(3)\n"
      "v_mfma_f32_32x32x16_bf16 v[60:75], v[92:95], a[144:147], v[60:75]\n"
      "ds_read_b128 v[112:115], v8 offset:24576\n"
      "s_add_u32 s76, s61, 7872512\n"
      "buffer_store_dwordx4 v[140:143], v2, s[24:27], s76 offen offset:0 sc1\n"
      "v_mfma_f32_32x32x16_bf16 v[116:131], v[96:99], a[40:43], v[116:131]\n"
      "ds_read_b128 v[92:95], v8 offset:11264\n"
      "buffer_load_dwordx4 a[36:39], v0, s[24:27], s77 offen offset:2048 sc1\n"
      "s_waitcnt lgkmcnt(3)\n"
      "v_mfma_f32_32x32x16_bf16 v[60:75], v[100:103], a[148:151], v[60:75]\n"
      "ds_read_b128 v[96:99], v8 offset:25600\n"
      "v_mov_b32 v153, v0\n"
      "s_add_u32 s98, s73, 16031744\n"
      "s_mov_b64 exec, 0xffff\n"
      "buffer_load_dword v153, v153, s[24:27], s98 offen sc1\n"
      "s_mov_b64 exec, -1\n"
      "v_mfma_f32_32x32x16_bf16 v[116:131], v[104:107], a[44:47], v[116:131]\n"
      "ds_read_b128 v[100:103], v8 offset:26624\n"
      "buffer_load_dwordx4 a[40:43], v0, s[24:27], s90 offen offset:2048 sc1\n"
      "s_waitcnt lgkmcnt(3)\n"
      "v_mfma_f32_32x32x16_bf16 v[60:75], v[108:111], a[152:155], v[60:75]\n"
      "ds_read_b128 v[104:107], v8 offset:27648\n"
      "v_mfma_f32_32x32x16_bf16 v[116:131], v[112:115], a[48:51], v[116:131]\n"
      "ds_read_b128 v[108:111], v8 offset:28672\n"
      "buffer_load_dwordx4 a[44:47], v0, s[24:27], s91 offen offset:2048 sc1\n"
      "s_waitcnt lgkmcnt(3)\n"
      "v_mfma_f32_32x32x16_bf16 v[60:75], v[92:95], a[156:159], v[60:75]\n"
      "ds_read_b128 v[112:115], v8 offset:29696\n"
      "v_mfma_f32_32x32x16_bf16 v[116:131], v[96:99], a[52:55], v[116:131]\n"
      "ds_read_b128 v[92:95], v8 offset:30720\n"
      "buffer_load_dwordx4 a[48:51], v0, s[24:27], s75 offen offset:3072 sc1\n"
      "s_waitcnt lgkmcnt(3)\n"
      "v_mfma_f32_32x32x16_bf16 v[116:131], v[100:103], a[56:59], v[116:131]\n"
      "ds_read_b128 v[96:99], v8 offset:31744\n"
      "v_mfma_f32_32x32x16_bf16 v[116:131], v[104:107], a[60:63], v[116:131]\n"
      "ds_read_b128 v[100:103], v0 offset:0\n"
      "s_nop 2\n"
      "v_mul_f32 v132, v60, v148\n"
      "v_mul_f32 v133, v61, v148\n"
      "buffer_load_dwordx4 a[52:55], v0, s[24:27], s77 offen offset:3072 sc1\n"
      "s_waitcnt lgkmcnt(3)\n"
      "v_mfma_f32_32x32x16_bf16 v[116:131], v[108:111], a[64:67], v[116:131]\n"
      "ds_read_b128 v[104:107], v0 offset:1024\n"
      "v_exp_f32 v132, v132\n"
      "v_mul_f32 v134, v60, v68\n"
      "v_exp_f32 v133, v133\n"
      "v_mfma_f32_32x32x16_bf16 v[116:131], v[112:115], a[68:71], v[116:131]\n"
      "ds_read_b128 v[108:111], v0 offset:2048\n"
      "v_mul_f32 v135, v61, v69\n"
      "v_fma_f32 v132, v132, v150, v150\n"
      "v_rcp_f32 v132, v132\n"
      "buffer_load_dwordx4 a[56:59], v0, s[24:27], s90 offen offset:3072 sc1\n"
      "s_waitcnt lgkmcnt(3)\n"
      "v_mfma_f32_32x32x16_bf16 v[116:131], v[92:95], a[72:75], v[116:131]\n"
      "ds_read_b128 v[112:115], v0 offset:3072\n"
      "v_fma_f32 v133, v133, v150, v150\n"
      "v_rcp_f32 v133, v133\n"
      "v_mfma_f32_32x32x16_bf16 v[116:131], v[96:99], a[76:79], v[116:131]\n"
      "ds_read_b128 v[92:95], v0 offset:4096\n"
      "v_mul_f32 v132, v132, v134\n"
      "v_mul_f32 v133, v133, v135\n"
      "v_cvt_pk_bf16_f32 v144, v132, v133\n"
      "buffer_load_dwordx4 a[60:63], v0, s[24:27], s91 offen offset:3072 sc1\n"
      "s_waitcnt lgkmcnt(3)\n"
      "v_mfma_f32_32x32x16_bf16 v[116:131], v[100:103], a[80:83], v[116:131]\n"
      "ds_read_b128 v[96:99], v0 offset:5120\n"
      "v_mul_f32 v136, v62, v148\n"
      "v_mul_f32 v137, v63, v148\n"
      "v_exp_f32 v136, v136\n"
      "v_mfma_f32_32x32x16_bf16 v[116:131], v[104:107], a[84:87], v[116:131]\n"
      "ds_read_b128 v[100:103], v0 offset:6144\n"
      "v_mul_f32 v138, v62, v70\n"
      "v_exp_f32 v137, v137\n"
      "buffer_load_dwordx4 a[64:67], v0, s[20:23], s78 offen offset:1024 sc1\n"
      "s_waitcnt lgkmcnt(3)\n"
      "v_mfma_f32_32x32x16_bf16 v[116:131], v[108:111], a[88:91], v[116:131]\n"
      "ds_read_b128 v[104:107], v0 offset:7168\n"
      "v_mul_f32 v139, v63, v71\n"
      "v_fma_f32 v136, v136, v150, v150\n"
      "v_rcp_f32 v136, v136\n"
      "v_mfma_f32_32x32x16_bf16 v[116:131], v[112:115], a[92:95], v[116:131]\n"
      "ds_read_b128 v[108:111], v0 offset:8192\n"
      "v_fma_f32 v137, v137, v150, v150\n"
      "v_rcp_f32 v137, v137\n"
      "v_mul_f32 v136, v136, v138\n"
      "buffer_load_dwordx4 a[68:71], v0, s[20:23], s78 offen offset:2048 sc1\n"
      "s_waitcnt lgkmcnt(3)\n"
      "v_mfma_f32_32x32x16_bf16 v[116:131], v[92:95], a[96:99], v[116:131]\n"
      "ds_read_b128 v[112:115], v0 offset:9216\n"
      "v_mul_f32 v137, v137, v139\n"
      "v_cvt_pk_bf16_f32 v145, v136, v137\n"
      "v_mul_f32 v132, v64, v148\n"
      "v_mfma_f32_32x32x16_bf16 v[116:131], v[96:99], a[100:103], v[116:131]\n"
      "ds_read_b128 v[92:95], v0 offset:10240\n"
      "v_mul_f32 v133, v65, v148\n"
      "v_exp_f32 v132, v132\n"
      "s_waitcnt lgkmcnt(3)\n"
      "v_mfma_f32_32x32x16_bf16 v[116:131], v[100:103], a[104:107], v[116:131]\n"
      "ds_read_b128 v[96:99], v0 offset:11264\n"
      "v_mul_f32 v134, v64, v72\n"
      "v_exp_f32 v133, v133\n"
      "v_mul_f32 v135, v65, v73\n"
      "s_waitcnt vmcnt(8)\n"
      "v_cmp_eq_u32 vcc, 0, v153\n"
      "s_cbranch_vccz Lpd11_%=\n"
      "s_add_u32 s80, s73, 16031744\n"
      "s_mov_b32 s84, 400000\n"
      "v_mov_b32 v153, v0\n"
      "s_mov_b64 exec, 0xffff\n"
      "Lpl11_%=:\n"
      "buffer_load_dword v11, v153, s[24:27], s80 offen sc1\n"
      "s_waitcnt vmcnt(0)\n"
      "v_cmp_eq_u32 vcc, 0, v11\n"
      "s_cbranch_vccz Lpx11_%=\n"
      "s_sub_u32 s84, s84, 1\n"
      "s_cmp_lg_u32 s84, 0\n"
      "s_cbranch_scc1 Lpl11_%=\n"
      "Lpx11_%=:\n"
      "s_mov_b64 exec, -1\n"
      "Lpd11_%=:\n"
      "v_mfma_f32_32x32x16_bf16 v[116:131], v[104:107], a[108:111], v[116:131]\n"
      "ds_read_b128 v[100:103], v0 offset:12288\n"
      "v_fma_f32 v132, v132, v150, v150\n"
      "v_rcp_f32 v132, v132\n"
      "v_fma_f32 v133, v133, v150, v150\n"
      "s_waitcnt lgkmcnt(3)\n"
      "v_mfma_f32_32x32x16_bf16 v[116:131], v[108:111], a[112:115], v[116:131]\n"
      "ds_read_b128 v[104:107], v0 offset:13312\n"
      "v_rcp_f32 v133, v133\n"
      "v_mul_f32 v132, v132, v134\n"
      "s_add_u32 s75, s51, 7868416\n"
      "buffer_load_dwordx4 a[72:75], v0, s[24:27], s75 offen offset:0 sc1\n"
      "v_mfma_f32_32x32x16_bf16 v[116:131], v[112:115], a[116:119], v[116:131]\n"
      "ds_read_b128 v[108:111], v0 offset:14336\n"
      "v_mul_f32 v133, v133, v135\n"
      "v_cvt_pk_bf16_f32 v146, v132, v133\n"
      "v_mul_f32 v136, v66, v148\n"
      "s_waitcnt lgkmcnt(3)\n"
      "v_mfma_f32_32x32x16_bf16 v[116:131], v[92:95], a[120:123], v[116:131]\n"
      "ds_read_b128 v[112:115], v0 offset:15360\n"
      "v_mul_f32 v137, v67, v148\n"
      "v_exp_f32 v136, v136\n"
      "v_mul_f32 v138, v66, v74\n"
      "s_add_u32 s77, s52, 7868416\n"
      "buffer_load_dwordx4 a[76:79], v0, s[24:27], s77 offen offset:0 sc1\n"
      "v_mfma_f32_32x32x16_bf16 v[116:131], v[96:99], a[124:127], v[116:131]\n"
      "ds_read_b128 v[92:95], v0 offset:16384\n"
      "v_exp_f32 v137, v137\n"
      "v_mul_f32 v139, v67, v75\n"
      "s_waitcnt lgkmcnt(3)\n"
      "v_mfma_f32_32x32x16_bf16 v[116:131], v[100:103], a[128:131], v[116:131]\n"
      "ds_read_b128 v[96:99], v0 offset:17408\n"
      "v_fma_f32 v136, v136, v150, v150\n"
      "v_rcp_f32 v136, v136\n"
      "v_fma_f32 v137, v137, v150, v150\n"
      "s_add_u32 s90, s53, 7868416\n"
      "buffer_load_dwordx4 a[80:83], v0, s[24:27], s90 offen offset:0 sc1\n"
      "v_mfma_f32_32x32x16_bf16 v[116:131], v[104:107], a[132:135], v[116:131]\n"
      "ds_read_b128 v[100:103], v0 offset:18432\n"
      "v_rcp_f32 v137, v137\n"
      "v_mul_f32 v136, v136, v138\n"
      "v_mul_f32 v137, v137, v139\n"
      "s_waitcnt lgkmcnt(3)\n"
      "v_mfma_f32_32x32x16_bf16 v[116:131], v[108:111], a[136:139], v[116:131]\n"
      "ds_read_b128 v[104:107], v0 offset:19456\n"
      "v_cvt_pk_bf16_f32 v147, v136, v137\n"
      "s_add_u32 s76, s61, 7873024\n"
      "buffer_store_dwordx4 v[144:147], v2, s[24:27], s76 offen offset:0 sc1\n"
      "s_add_u32 s91, s54, 7868416\n"
      "buffer_load_dwordx4 a[84:87], v0, s[24:27], s91 offen offset:0 sc1\n"
      "v_mfma_f32_32x32x16_bf16 v[116:131], v[112:115], a[140:143], v[116:131]\n"
      "s_waitcnt lgkmcnt(2)\n"
      "v_mfma_f32_32x32x16_bf16 v[116:131], v[92:95], a[144:147], v[116:131]\n"
      "buffer_load_dwordx4 a[88:91], v0, s[24:27], s75 offen offset:1024 sc1\n"
      "v_mfma_f32_32x32x16_bf16 v[116:131], v[96:99], a[148:151], v[116:131]\n"
      "s_waitcnt lgkmcnt(0)\n"
      "v_mfma_f32_32x32x16_bf16 v[116:131], v[100:103], a[152:155], v[116:131]\n"
      "buffer_load_dwordx4 a[92:95], v0, s[24:27], s77 offen offset:1024 sc1\n"
      "v_mfma_f32_32x32x16_bf16 v[116:131], v[104:107], a[156:159], v[116:131]\n"
      "v_mfma_f32_16x16x32_bf16 v[44:47], a[160:163], a[0:3], 0\n"
      "v_mfma_f32_16x16x32_bf16 v[48:51], a[164:167], a[0:3], 0\n"
      "v_mfma_f32_16x16x32_bf16 v[52:55], a[168:171], a[0:3], 0\n"
      "s_nop 8\n"
      "v_mul_f32 v132, v116, v148\n"
      "v_mul_f32 v133, v117, v148\n"
      "v_mfma_f32_16x16x32_bf16 v[56:59], a[172:175], a[0:3], 0\n"
      "v_exp_f32 v132, v132\n"
      "v_mul_f32 v134, v116, v124\n"
      "v_mfma_f32_16x16x32_bf16 v[60:63], a[176:179], a[0:3], 0\n"
      "v_exp_f32 v133, v133\n"
      "v_mul_f32 v135, v117, v125\n"
      "buffer_load_dwordx4 a[96:99], v0, s[24:27], s90 offen offset:1024 sc1\n"
      "v_mfma_f32_16x16x32_bf16 v[64:67], a[160:163], a[4:7], 0\n"
      "v_fma_f32 v132, v132, v150, v150\n"
      "v_rcp_f32 v132, v132\n"
      "v_mfma_f32_16x16x32_bf16 v[68:71], a[164:167], a[4:7], 0\n"
      "v_fma_f32 v133, v133, v150, v150\n"
      "v_rcp_f32 v133, v133\n"
      "v_mfma_f32_16x16x32_bf16 v[72:75], a[168:171], a[4:7], 0\n"
      "v_mul_f32 v132, v132, v134\n"
      "v_mul_f32 v133, v133, v135\n"
      "v_mfma_f32_16x16x32_bf16 v[76:79], a[172:175], a[4:7], 0\n"
      "v_cvt_pk_bf16_f32 v140, v132, v133\n"
      "v_mul_f32 v136, v118, v148\n"
      "buffer_load_dwordx4 a[100:103], v0, s[24:27], s91 offen offset:1024 sc1\n"
      "v_mfma_f32_16x16x32_bf16 v[80:83], a[176:179], a[4:7], 0\n"
      "v_mul_f32 v137, v119, v148\n"
      "v_exp_f32 v136, v136\n"
      "v_mfma_f32_16x16x32_bf16 v[84:87], a[160:163], a[8:11], 0\n"
      "v_mul_f32 v138, v118, v126\n"
      "v_exp_f32 v137, v137\n"
      "v_mfma_f32_16x16x32_bf16 v[88:91], a[164:167], a[8:11], 0\n"
      "v_mul_f32 v139, v119, v127\n"
      "v_fma_f32 v136, v136, v150, v150\n"
      "v_mfma_f32_16x16x32_bf16 v[92:95], a[168:171], a[8:11], 0\n"
      "v_rcp_f32 v136, v136\n"
      "v_fma_f32 v137, v137, v150, v150\n"
      "buffer_load_dwordx4 a[104:107], v0, s[24:27], s75 offen offset:2048 sc1\n"
      "v_mfma_f32_16x16x32_bf16 v[96:99], a[172:175], a[8:11], 0\n"
      "v_rcp_f32 v137, v137\n"
      "v_mul_f32 v136, v136, v138\n"
      "v_mfma_f32_16x16x32_bf16 v[100:103], a[176:179], a[8:11], 0\n"
      "v_mul_f32 v137, v137, v139\n"
      "v_cvt_pk_bf16_f32 v141, v136, v137\n"
      "v_mfma_f32_16x16x32_bf16 v[12:15], a[160:163], a[12:15], v[12:15]\n"
      "v_mul_f32 v132, v120, v148\n"
      "v_mul_f32 v133, v121, v148\n"
      "v_mfma_f32_16x16x32_bf16 v[16:19], a[164:167], a[12:15], v[16:19]\n"
      "v_exp_f32 v132, v132\n"
      "v_mul_f32 v134, v120, v128\n"
      "buffer_load_dwordx4 a[108:111], v0, s[24:27], s77 offen offset:2048 sc1\n"
      "v_mfma_f32_16x16x32_bf16 v[20:23], a[168:171], a[12:15], v[20:23]\n"
      "v_exp_f32 v133, v133\n"
      "v_mul_f32 v135, v121, v129\n"
      "v_mfma_f32_16x16x32_bf16 v[24:27], a[172:175], a[12:15], v[24:27]\n"
      "v_fma_f32 v132, v132, v150, v150\n"
      "v_rcp_f32 v132, v132\n"
      "v_mfma_f32_16x16x32_bf16 v[28:31], a[176:179], a[12:15], v[28:31]\n"
      "v_fma_f32 v133, v133, v150, v150\n"
      "v_rcp_f32 v133, v133\n"
      "v_mfma_f32_16x16x32_bf16 v[44:47], a[180:183], a[16:19], v[44:47]\n"
      "v_mul_f32 v132, v132, v134\n"
      "v_mul_f32 v133, v133, v135\n"
      "buffer_load_dwordx4 a[112:115], v0, s[24:27], s90 offen offset:2048 sc1\n"
      "v_mfma_f32_16x16x32_bf16 v[48:51], a[184:187], a[16:19], v[48:51]\n"
      "v_cvt_pk_bf16_f32 v142, v132, v133\n"
      "v_mul_f32 v136, v122, v148\n"
      "v_mfma_f32_16x16x32_bf16 v[52:55], a[188:191], a[16:19], v[52:55]\n"
      "v_mul_f32 v137, v123, v148\n"
      "v_exp_f32 v136, v136\n"
      "v_mfma_f32_16x16x32_bf16 v[56:59], a[192:195], a[16:19], v[56:59]\n"
      "v_mul_f32 v138, v122, v130\n"
      "v_exp_f32 v137, v137\n"
      "v_mfma_f32_16x16x32_bf16 v[60:63], a[196:199], a[16:19], v[60:63]\n"
      "v_mul_f32 v139, v123, v131\n"
      "v_fma_f32 v136, v136, v150, v150\n"
      "buffer_load_dwordx4 a[116:119], v0, s[24:27], s91 offen offset:2048 sc1\n"
      "v_mfma_f32_16x16x32_bf16 v[64:67], a[180:183], a[20:23], v[64:67]\n"
      "v_rcp_f32 v136, v136\n"
      "v_fma_f32 v137, v137, v150, v150\n"
      "v_mfma_f32_16x16x32_bf16 v[68:71], a[184:187], a[20:23], v[68:71]\n"
      "v_rcp_f32 v137, v137\n"
      "v_mul_f32 v136, v136, v138\n"
      "v_mfma_f32_16x16x32_bf16 v[72:75], a[188:191], a[20:23], v[72:75]\n"
      "v_mul_f32 v137, v137, v139\n"
      "v_cvt_pk_bf16_f32 v143, v136, v137\n"
      "v_mfma_f32_16x16x32_bf16 v[76:79], a[192:195], a[20:23], v[76:79]\n"
      "s_add_u32 s76, s62, 7876608\n"
      "buffer_store_dwordx4 v[140:143], v2, s[24:27], s76 offen offset:0 sc1\n"
      "s_waitcnt vmcnt(10)\n"
      "s_lshr_b32 s80, s74, 5\n"
      "s_add_u32 s80, s80, s73\n"
      "s_barrier\n"
      "s_cmp_lg_u32 s40, 0\n"
      "s_cbranch_scc1 Lsg12_%=\n"
      "s_add_u32 s80, s80, 16039936\n"
      "s_mov_b64 exec, 1\n"
      "buffer_store_dword v10, v9, s[24:27], s80 offen sc1\n"
      "s_mov_b64 exec, -1\n"
      "Lsg12_%=:\n"
      "v_mfma_f32_16x16x32_bf16 v[80:83], a[196:199], a[20:23], v[80:83]\n"
      "buffer_load_dwordx4 a[120:123], v0, s[24:27], s75 offen offset:3072 sc1\n"
      "v_mfma_f32_16x16x32_bf16 v[84:87], a[180:183], a[24:27], v[84:87]\n"
      "v_mfma_f32_16x16x32_bf16 v[88:91], a[184:187], a[24:27], v[88:91]\n"
      "v_mfma_f32_16x16x32_bf16 v[92:95], a[188:191], a[24:27], v[92:95]\n"
      "v_mfma_f32_16x16x32_bf16 v[96:99], a[192:195], a[24:27], v[96:99]\n"
      "buffer_load_dwordx4 a[124:127], v0, s[24:27], s77 offen offset:3072 sc1\n"
      "v_mfma_f32_16x16x32_bf16 v[100:103], a[196:199], a[24:27], v[100:103]\n"
      "v_mfma_f32_16x16x32_bf16 v[12:15], a[180:183], a[28:31], v[12:15]\n"
      "v_mfma_f32_16x16x32_bf16 v[16:19], a[184:187], a[28:31], v[16:19]\n"
      "v_mfma_f32_16x16x32_bf16 v[20:23], a[188:191], a[28:31], v[20:23]\n"
      "buffer_load_dwordx4 a[128:131], v0, s[24:27], s90 offen offset:3072 sc1\n"
      "v_mfma_f32_16x16x32_bf16 v[24:27], a[192:195], a[28:31], v[24:27]\n"
      "v_mfma_f32_16x16x32_bf16 v[28:31], a[196:199], a[28:31], v[28:31]\n"
      "v_mfma_f32_16x16x32_bf16 v[44:47], a[200:203], a[32:35], v[44:47]\n"
      "v_mfma_f32_16x16x32_bf16 v[48:51], a[204:207], a[32:35], v[48:51]\n"
      "buffer_load_dwordx4 a[132:135], v0, s[24:27], s91 offen offset:3072 sc1\n"
      "v_mfma_f32_16x16x32_bf16 v[52:55], a[208:211], a[32:35], v[52:55]\n"
      "v_mfma_f32_16x16x32_bf16 v[56:59], a[212:215], a[32:35], v[56:59]\n"
      "v_mfma_f32_16x16x32_bf16 v[60:63], a[216:219], a[32:35], v[60:63]\n"
      "v_mfma_f32_16x16x32_bf16 v[64:67], a[200:203], a[36:39], v[64:67]\n"
      "buffer_load_dwordx4 a[136:139], v0, s[20:23], s78 offen offset:3072 sc1\n"
      "v_mfma_f32_16x16x32_bf16 v[68:71], a[204:207], a[36:39], v[68:71]\n"
      "v_mfma_f32_16x16x32_bf16 v[72:75], a[208:211], a[36:39], v[72:75]\n"
      "v_mfma_f32_16x16x32_bf16 v[76:79], a[212:215], a[36:39], v[76:79]\n"
      "v_mfma_f32_16x16x32_bf16 v[80:83], a[216:219], a[36:39], v[80:83]\n"
      "s_add_u32 s78, s69, 7918592\n"
      "buffer_load_dwordx4 a[140:143], v0, s[20:23], s78 offen offset:0 sc1\n"
      "v_mfma_f32_16x16x32_bf16 v[84:87], a[200:203], a[40:43], v[84:87]\n"
      "v_mfma_f32_16x16x32_bf16 v[88:91], a[204:207], a[40:43], v[88:91]\n"
      "v_mfma_f32_16x16x32_bf16 v[92:95], a[208:211], a[40:43], v[92:95]\n"
      "v_mfma_f32_16x16x32_bf16 v[96:99], a[212:215], a[40:43], v[96:99]\n"
      "v_mfma_f32_16x16x32_bf16 v[100:103], a[216:219], a[40:43], v[100:103]\n"
      "buffer_load_dwordx4 a[144:147], v0, s[20:23], s78 offen offset:1024 sc1\n"
      "v_mfma_f32_16x16x32_bf16 v[12:15], a[200:203], a[44:47], v[12:15]\n"
      "v_mfma_f32_16x16x32_bf16 v[16:19], a[204:207], a[44:47], v[16:19]\n"
      "v_mfma_f32_16x16x32_bf16 v[20:23], a[208:211], a[44:47], v[20:23]\n"
      "v_mfma_f32_16x16x32_bf16 v[24:27], a[212:215], a[44:47], v[24:27]\n"
      "buffer_load_dwordx4 a[148:151], v0, s[20:23], s78 offen offset:2048 sc1\n"
      "v_mfma_f32_16x16x32_bf16 v[28:31], a[216:219], a[44:47], v[28:31]\n"
      "v_mfma_f32_16x16x32_bf16 v[44:47], a[220:223], a[48:51], v[44:47]\n"
      "v_mfma_f32_16x16x32_bf16 v[48:51], a[224:227], a[48:51], v[48:51]\n"
      "v_mfma_f32_16x16x32_bf16 v[52:55], a[228:231], a[48:51], v[52:55]\n"
      "v_mfma_f32_16x16x32_bf16 v[56:59], a[232:235], a[48:51], v[56:59]\n"
      "v_mfma_f32_16x16x32_bf16 v[60:63], a[236:239], a[48:51], v[60:63]\n"
      "buffer_load_dwordx4 a[152:155], v0, s[20:23], s78 offen offset:3072 sc1\n"
      "v_mfma_f32_16x16x32_bf16 v[64:67], a[220:223], a[52:55], v[64:67]\n"
      "v_mfma_f32_16x16x32_bf16 v[68:71], a[224:227], a[52:55], v[68:71]\n"
      "v_mfma_f32_16x16x32_bf16 v[72:75], a[228:231], a[52:55], v[72:75]\n"
      "v_mfma_f32_16x16x32_bf16 v[76:79], a[232:235], a[52:55], v[76:79]\n"
      "s_add_u32 s78, s69, 7922688\n"
      "buffer_load_dwordx4 a[156:159], v0, s[20:23], s78 offen offset:0 sc1\n"
      "v_mfma_f32_16x16x32_bf16 v[80:83], a[236:239], a[52:55], v[80:83]\n"
      "v_mfma_f32_16x16x32_bf16 v[84:87], a[220:223], a[56:59], v[84:87]\n"
      "s_waitcnt vmcnt(10)\n"
      "s_lshr_b32 s80, s74, 5\n"
      "s_add_u32 s80, s80, s73\n"
      "s_barrier\n"
      "s_cmp_lg_u32 s40, 0\n"
      "s_cbranch_scc1 Lsg13_%=\n"
      "s_add_u32 s80, s80, 16048128\n"
      "s_mov_b64 exec, 1\n"
      "buffer_store_dword v10, v9, s[24:27], s80 offen sc1\n"
      "s_mov_b64 exec, -1\n"
      "Lsg13_%=:\n"
      "v_mfma_f32_16x16x32_bf16 v[88:91], a[224:227], a[56:59], v[88:91]\n"
      "v_mfma_f32_16x16x32_bf16 v[92:95], a[228:231], a[56:59], v[92:95]\n"
      "buffer_load_dwordx4 a[160:163], v0, s[20:23], s78 offen offset:1024 sc1\n"
      "v_mfma_f32_16x16x32_bf16 v[96:99], a[232:235], a[56:59], v[96:99]\n"
      "v_mfma_f32_16x16x32_bf16 v[100:103], a[236:239], a[56:59], v[100:103]\n"
      "v_mfma_f32_16x16x32_bf16 v[12:15], a[220:223], a[60:63], v[12:15]\n"
      "v_mfma_f32_16x16x32_bf16 v[16:19], a[224:227], a[60:63], v[16:19]\n"
      "buffer_load_dwordx4 a[164:167], v0, s[20:23], s78 offen offset:2048 sc1\n"
      "v_mfma_f32_16x16x32_bf16 v[20:23], a[228:231], a[60:63], v[20:23]\n"
      "v_mfma_f32_16x16x32_bf16 v[24:27], a[232:235], a[60:63], v[24:27]\n"
      "v_mfma_f32_16x16x32_bf16 v[28:31], a[236:239], a[60:63], v[28:31]\n"
      "v_mfma_f32_16x16x32_bf16 v[44:47], a[240:243], a[72:75], v[44:47]\n"
      "buffer_load_dwordx4 a[168:171], v0, s[20:23], s78 offen offset:3072 sc1\n"
      "v_mfma_f32_16x16x32_bf16 v[48:51], a[244:247], a[72:75], v[48:51]\n"
      "v_mfma_f32_16x16x32_bf16 v[52:55], a[248:251], a[72:75], v[52:55]\n"
      "v_mfma_f32_16x16x32_bf16 v[56:59], a[252:255], a[72:75], v[56:59]\n"
      "v_mfma_f32_16x16x32_bf16 v[60:63], v[156:159], a[72:75], v[60:63]\n"
      "v_mov_b32 v152, v0\n"
      "s_add_u32 s98, s73, 16039936\n"
      "s_mov_b64 exec, 0xffff\n"
      "buffer_load_dword v152, v152, s[24:27], s98 offen sc1\n"
      "s_mov_b64 exec, -1\n"
      "s_add_u32 s78, s69, 7926784\n"
      "buffer_load_dwordx4 a[172:175], v0, s[20:23], s78 offen offset:0 sc1\n"
      "v_mfma_f32_16x16x32_bf16 v[64:67], a[240:243], a[76:79], v[64:67]\n"
      "v_mfma_f32_16x16x32_bf16 v[68:71], a[244:247], a[76:79], v[68:71]\n"
      "v_mfma_f32_16x16x32_bf16 v[72:75], a[248:251], a[76:79], v[72:75]\n"
      "v_mfma_f32_16x16x32_bf16 v[76:79], a[252:255], a[76:79], v[76:79]\n"
      "v_mfma_f32_16x16x32_bf16 v[80:83], v[156:159], a[76:79], v[80:83]\n"
      "buffer_load_dwordx4 a[176:179], v0, s[20:23], s78 offen offset:1024 sc1\n"
      "v_mfma_f32_16x16x32_bf16 v[84:87], a[240:243], a[80:83], v[84:87]\n"
      "v_mfma_f32_16x16x32_bf16 v[88:91], a[244:247], a[80:83], v[88:91]\n"
      "v_mfma_f32_16x16x32_bf16 v[92:95], a[248:251], a[80:83], v[92:95]\n"
      "v_mfma_f32_16x16x32_bf16 v[96:99], a[252:255], a[80:83], v[96:99]\n"
      "buffer_load_dwordx4 a[180:183], v0, s[20:23], s78 offen offset:2048 sc1\n"
      "v_mfma_f32_16x16x32_bf16 v[100:103], v[156:159], a[80:83], v[100:103]\n"
      "v_mfma_f32_16x16x32_bf16 v[12:15], a[240:243], a[84:87], v[12:15]\n"
      "v_mfma_f32_16x16x32_bf16 v[16:19], a[244:247], a[84:87], v[16:19]\n"
      "v_mfma_f32_16x16x32_bf16 v[20:23], a[248:251], a[84:87], v[20:23]\n"
      "buffer_load_dwordx4 a[184:187], v0, s[20:23], s78 offen offset:3072 sc1\n"
      "v_mfma_f32_16x16x32_bf16 v[24:27], a[252:255], a[84:87], v[24:27]\n"
      "v_mfma_f32_16x16x32_bf16 v[28:31], v[156:159], a[84:87], v[28:31]\n"
      "v_mfma_f32_16x16x32_bf16 v[44:47], v[160:163], a[88:91], v[44:47]\n"
      "v_mfma_f32_16x16x32_bf16 v[48:51], v[164:167], a[88:91], v[48:51]\n"
      "s_add_u32 s78, s69, 7930880\n"
      "buffer_load_dwordx4 a[188:191], v0, s[20:23], s78 offen offset:0 sc1\n"
      "v_mfma_f32_16x16x32_bf16 v[52:55], v[168:171], a[88:91], v[52:55]\n"
      "v_mfma_f32_16x16x32_bf16 v[56:59], v[172:175], a[88:91], v[56:59]\n"
      "v_mfma_f32_16x16x32_bf16 v[60:63], v[176:179], a[88:91], v[60:63]\n"
      "v_mfma_f32_16x16x32_bf16 v[64:67], v[160:163], a[92:95], v[64:67]\n"
      "buffer_load_dwordx4 a[192:195], v0, s[20:23], s78 offen offset:1024 sc1\n"
      "v_mfma_f32_16x16x32_bf16 v[68:71], v[164:167], a[92:95], v[68:71]\n"
      "v_mfma_f32_16x16x32_bf16 v[72:75], v[168:171], a[92:95], v[72:75]\n"
      "v_mfma_f32_16x16x32_bf16 v[76:79], v[172:175], a[92:95], v[76:79]\n"
      "v_mfma_f32_16x16x32_bf16 v[80:83], v[176:179], a[92:95], v[80:83]\n"
      "buffer_load_dwordx4 a[196:199], v0, s[20:23], s78 offen offset:2048 sc1\n"
      "v_mfma_f32_16x16x32_bf16 v[84:87], v[160:163], a[96:99], v[84:87]\n"
      "v_mfma_f32_16x16x32_bf16 v[88:91], v[164:167], a[96:99], v[88:91]\n"
      "v_mfma_f32_16x16x32_bf16 v[92:95], v[168:171], a[96:99], v[92:95]\n"
      "v_mfma_f32_16x16x32_bf16 v[96:99], v[172:175], a[96:99], v[96:99]\n"
      "v_mfma_f32_16x16x32_bf16 v[100:103], v[176:179], a[96:99], v[100:103]\n"
      "buffer_load_dwordx4 a[200:203], v0, s[20:23], s78 offen offset:3072 sc1\n"
      "v_mfma_f32_16x16x32_bf16 v[12:15], v[160:163], a[100:103], v[12:15]\n"
      "v_mfma_f32_16x16x32_bf16 v[16:19], v[164:167], a[100:103], v[16:19]\n"
      "v_mfma_f32_16x16x32_bf16 v[20:23], v[168:171], a[100:103], v[20:23]\n"
      "v_mfma_f32_16x16x32_bf16 v[24:27], v[172:175], a[100:103], v[24:27]\n"
      "s_add_u32 s78, s69, 7934976\n"
      "buffer_load_dwordx4 a[204:207], v0, s[20:23], s78 offen offset:0 sc1\n"
      "v_mfma_f32_16x16x32_bf16 v[28:31], v[176:179], a[100:103], v[28:31]\n"
      "v_mfma_f32_16x16x32_bf16 v[44:47], v[180:183], a[104:107], v[44:47]\n"
      "s_waitcnt vmcnt(9)\n"
      "v_cmp_eq_u32 vcc, 0, v152\n"
      "s_cbranch_vccz Lpd14_%=\n"
      "s_add_u32 s80, s73, 16039936\n"
      "s_mov_b32 s84, 400000\n"
      "v_mov_b32 v152, v0\n"
      "s_mov_b64 exec, 0xffff\n"
      "Lpl14_%=:\n"
      "buffer_load_dword v11, v152, s[24:27], s80 offen sc1\n"
      "s_waitcnt vmcnt(0)\n"
      "v_cmp_eq_u32 vcc, 0, v11\n"
      "s_cbranch_vccz Lpx14_%=\n"
      "s_sub_u32 s84, s84, 1\n"
      "s_cmp_lg_u32 s84, 0\n"
      "s_cbranch_scc1 Lpl14_%=\n"
      "Lpx14_%=:\n"
      "s_mov_b64 exec, -1\n"
      "Lpd14_%=:\n"
      "v_mfma_f32_16x16x32_bf16 v[48:51], v[184:187], a[104:107], v[48:51]\n"
      "v_mfma_f32_16x16x32_bf16 v[52:55], v[188:191], a[104:107], v[52:55]\n"
      "v_mfma_f32_16x16x32_bf16 v[56:59], v[192:195], a[104:107], v[56:59]\n"
      "v_mfma_f32_16x16x32_bf16 v[60:63], v[196:199], a[104:107], v[60:63]\n"
      "v_mfma_f32_16x16x32_bf16 v[64:67], v[180:183], a[108:111], v[64:67]\n"
      "s_add_u32 s75, s51, 7872512\n"
      "buffer_load_dwordx4 a[0:3], v0, s[24:27], s75 offen offset:0 sc1\n"
      "v_mfma_f32_16x16x32_bf16 v[68:71], v[184:187], a[108:111], v[68:71]\n"
      "v_mfma_f32_16x16x32_bf16 v[72:75], v[188:191], a[108:111], v[72:75]\n"
      "v_mfma_f32_16x16x32_bf16 v[76:79], v[192:195], a[108:111], v[76:79]\n"
      "v_mfma_f32_16x16x32_bf16 v[80:83], v[196:199], a[108:111], v[80:83]\n"
      "buffer_load_dwordx4 a[16:19], v0, s[24:27], s75 offen offset:1024 sc1\n"
      "v_mfma_f32_16x16x32_bf16 v[84:87], v[180:183], a[112:115], v[84:87]\n"
      "v_mfma_f32_16x16x32_bf16 v[88:91], v[184:187], a[112:115], v[88:91]\n"
      "v_mfma_f32_16x16x32_bf16 v[92:95], v[188:191], a[112:115], v[92:95]\n"
      "v_mfma_f32_16x16x32_bf16 v[96:99], v[192:195], a[112:115], v[96:99]\n"
      "buffer_load_dwordx4 a[32:35], v0, s[24:27], s75 offen offset:2048 sc1\n"
      "v_mfma_f32_16x16x32_bf16 v[100:103], v[196:199], a[112:115], v[100:103]\n"
      "v_mfma_f32_16x16x32_bf16 v[12:15], v[180:183], a[116:119], v[12:15]\n"
      "v_mfma_f32_16x16x32_bf16 v[16:19], v[184:187], a[116:119], v[16:19]\n"
      "v_mov_b32 v153, v0\n"
      "s_add_u32 s98, s73, 16048128\n"
      "s_mov_b64 exec, 0xffff\n"
      "buffer_load_dword v153, v153, s[24:27], s98 offen sc1\n"
      "s_mov_b64 exec, -1\n"
      "v_mfma_f32_16x16x32_bf16 v[20:23], v[188:191], a[116:119], v[20:23]\n"
      "buffer_load_dwordx4 a[48:51], v0, s[24:27], s75 offen offset:3072 sc1\n"
      "v_mfma_f32_16x16x32_bf16 v[24:27], v[192:195], a[116:119], v[24:27]\n"
      "v_mfma_f32_16x16x32_bf16 v[28:31], v[196:199], a[116:119], v[28:31]\n"
      "v_mfma_f32_16x16x32_bf16 v[44:47], v[200:203], a[120:123], v[44:47]\n"
      "v_mfma_f32_16x16x32_bf16 v[48:51], v[204:207], a[120:123], v[48:51]\n"
      "buffer_load_dwordx4 a[208:211], v0, s[20:23], s78 offen offset:1024 sc1\n"
      "v_mfma_f32_16x16x32_bf16 v[52:55], v[208:211], a[120:123], v[52:55]\n"
      "v_mfma_f32_16x16x32_bf16 v[56:59], v[212:215], a[120:123], v[56:59]\n"
      "v_mfma_f32_16x16x32_bf16 v[60:63], v[216:219], a[120:123], v[60:63]\n"
      "v_mfma_f32_16x16x32_bf16 v[64:67], v[200:203], a[124:127], v[64:67]\n"
      "s_add_u32 s77, s52, 7872512\n"
      "buffer_load_dwordx4 a[4:7], v0, s[24:27], s77 offen offset:0 sc1\n"
      "v_mfma_f32_16x16x32_bf16 v[68:71], v[204:207], a[124:127], v[68:71]\n"
      "v_mfma_f32_16x16x32_bf16 v[72:75], v[208:211], a[124:127], v[72:75]\n"
      "v_mfma_f32_16x16x32_bf16 v[76:79], v[212:215], a[124:127], v[76:79]\n"
      "v_mfma_f32_16x16x32_bf16 v[80:83], v[216:219], a[124:127], v[80:83]\n"
      "v_mfma_f32_16x16x32_bf16 v[84:87], v[200:203], a[128:131], v[84:87]\n"
      "buffer_load_dwordx4 a[20:23], v0, s[24:27], s77 offen offset:1024 sc1\n"
      "v_mfma_f32_16x16x32_bf16 v[88:91], v[204:207], a[128:131], v[88:91]\n"
      "v_mfma_f32_16x16x32_bf16 v[92:95], v[208:211], a[128:131], v[92:95]\n"
      "v_mfma_f32_16x16x32_bf16 v[96:99], v[212:215], a[128:131], v[96:99]\n"
      "v_mfma_f32_16x16x32_bf16 v[100:103], v[216:219], a[128:131], v[100:103]\n"
      "buffer_load_dwordx4 a[36:39], v0, s[24:27], s77 offen offset:2048 sc1\n"
      "v_mfma_f32_16x16x32_bf16 v[12:15], v[200:203], a[132:135], v[12:15]\n"
      "v_mfma_f32_16x16x32_bf16 v[16:19], v[204:207], a[132:135], v[16:19]\n"
      "v_mfma_f32_16x16x32_bf16 v[20:23], v[208:211], a[132:135], v[20:23]\n"
      "v_mfma_f32_16x16x32_bf16 v[24:27], v[212:215], a[132:135], v[24:27]\n"
      "buffer_load_dwordx4 a[52:55], v0, s[24:27], s77 offen offset:3072 sc1\n"
      "v_mfma_f32_16x16x32_bf16 v[28:31], v[216:219], a[132:135], v[28:31]\n"
      "s_waitcnt vmcnt(9)\n"
      "v_mfma_f32_16x16x32_bf16 v[44:47], v[220:223], a[0:3], v[44:47]\n"
      "v_mfma_f32_16x16x32_bf16 v[48:51], v[224:227], a[0:3], v[48:51]\n"
      "v_mfma_f32_16x16x32_bf16 v[52:55], v[228:231], a[0:3], v[52:55]\n"
      "s_add_u32 s90, s53, 7872512\n"
      "buffer_load_dwordx4 a[8:11], v0, s[24:27], s90 offen offset:0 sc1\n"
      "v_mfma_f32_16x16x32_bf16 v[56:59], v[232:235], a[0:3], v[56:59]\n"
      "v_mfma_f32_16x16x32_bf16 v[60:63], v[236:239], a[0:3], v[60:63]\n"
      "s_waitcnt vmcnt(9)\n"
      "v_mfma_f32_16x16x32_bf16 v[44:47], v[240:243], a[16:19], v[44:47]\n"
      "v_mfma_f32_16x16x32_bf16 v[48:51], v[244:247], a[16:19], v[48:51]\n"
      "buffer_load_dwordx4 a[24:27], v0, s[24:27], s90 offen offset:1024 sc1\n"
      "v_mfma_f32_16x16x32_bf16 v[52:55], v[248:251], a[16:19], v[52:55]\n"
      "v_mfma_f32_16x16x32_bf16 v[56:59], v[252:255], a[16:19], v[56:59]\n"
      "v_mfma_f32_16x16x32_bf16 v[60:63], a[64:67], a[16:19], v[60:63]\n"
      "s_waitcnt vmcnt(9)\n"
      "v_mfma_f32_16x16x32_bf16 v[44:47], a[68:71], a[32:35], v[44:47]\n"
      "buffer_load_dwordx4 a[40:43], v0, s[24:27], s90 offen offset:2048 sc1\n"
      "v_mfma_f32_16x16x32_bf16 v[48:51], a[136:139], a[32:35], v[48:51]\n"
      "v_mfma_f32_16x16x32_bf16 v[52:55], a[140:143], a[32:35], v[52:55]\n"
      "s_waitcnt vmcnt(9)\n"
      "v_cmp_eq_u32 vcc, 0, v153\n"
      "s_cbranch_vccz Lpd15_%=\n"
      "s_add_u32 s80, s73, 16048128\n"
      "s_mov_b32 s84, 400000\n"
      "v_mov_b32 v153, v0\n"
      "s_mov_b64 exec, 0xffff\n"
      "Lpl15_%=:\n"
      "buffer_load_dword v11, v153, s[24:27], s80 offen sc1\n"
      "s_waitcnt vmcnt(0)\n"
      "v_cmp_eq_u32 vcc, 0, v11\n"
      "s_cbranch_vccz Lpx15_%=\n"
      "s_sub_u32 s84, s84, 1\n"
      "s_cmp_lg_u32 s84, 0\n"
      "s_cbranch_scc1 Lpl15_%=\n"
      "Lpx15_%=:\n"
      "s_mov_b64 exec, -1\n"
      "Lpd15_%=:\n"
      "v_mfma_f32_16x16x32_bf16 v[56:59], a[144:147], a[32:35], v[56:59]\n"
      "v_mfma_f32_16x16x32_bf16 v[60:63], a[148:151], a[32:35], v[60:63]\n"
      "s_waitcnt vmcnt(8)\n"
      "v_mfma_f32_16x16x32_bf16 v[44:47], a[152:155], a[48:51], v[44:47]\n"
      "v_mfma_f32_16x16x32_bf16 v[48:51], a[156:159], a[48:51], v[48:51]\n"
      "v_mfma_f32_16x16x32_bf16 v[52:55], a[160:163], a[48:51], v[52:55]\n"
      "s_add_u32 s75, s51, 7876608\n"
      "buffer_load_dwordx4 a[72:75], v0, s[24:27], s75 offen offset:0 sc1\n"
      "v_mfma_f32_16x16x32_bf16 v[56:59], a[164:167], a[48:51], v[56:59]\n"
      "v_mfma_f32_16x16x32_bf16 v[60:63], a[168:171], a[48:51], v[60:63]\n"
      "s_waitcnt vmcnt(7)\n"
      "v_mfma_f32_16x16x32_bf16 v[64:67], v[220:223], a[4:7], v[64:67]\n"
      "v_mfma_f32_16x16x32_bf16 v[68:71], v[224:227], a[4:7], v[68:71]\n"
      "buffer_load_dwordx4 a[88:91], v0, s[24:27], s75 offen offset:1024 sc1\n"
      "v_mfma_f32_16x16x32_bf16 v[72:75], v[228:231], a[4:7], v[72:75]\n"
      "v_mfma_f32_16x16x32_bf16 v[76:79], v[232:235], a[4:7], v[76:79]\n"
      "v_mfma_f32_16x16x32_bf16 v[80:83], v[236:239], a[4:7], v[80:83]\n"
      "s_waitcnt vmcnt(7)\n"
      "v_mfma_f32_16x16x32_bf16 v[64:67], v[240:243], a[20:23], v[64:67]\n"
      "s_add_u32 s77, s52, 7876608\n"
      "buffer_load_dwordx4 a[76:79], v0, s[24:27], s77 offen offset:0 sc1\n"
      "v_mfma_f32_16x16x32_bf16 v[68:71], v[244:247], a[20:23], v[68:71]\n"
      "v_mfma_f32_16x16x32_bf16 v[72:75], v[248:251], a[20:23], v[72:75]\n"
      "v_mfma_f32_16x16x32_bf16 v[76:79], v[252:255], a[20:23], v[76:79]\n"
      "v_mfma_f32_16x16x32_bf16 v[80:83], a[64:67], a[20:23], v[80:83]\n"
      "buffer_load_dwordx4 a[92:95], v0, s[24:27], s77 offen offset:1024 sc1\n"
      "s_waitcnt vmcnt(8)\n"
      "v_mfma_f32_16x16x32_bf16 v[64:67], a[68:71], a[36:39], v[64:67]\n"
      "v_mfma_f32_16x16x32_bf16 v[68:71], a[136:139], a[36:39], v[68:71]\n"
      "v_mfma_f32_16x16x32_bf16 v[72:75], a[140:143], a[36:39], v[72:75]\n"
      "v_mfma_f32_16x16x32_bf16 v[76:79], a[144:147], a[36:39], v[76:79]\n"
      "s_add_u32 s90, s53, 7876608\n"
      "buffer_load_dwordx4 a[80:83], v0, s[24:27], s90 offen offset:0 sc1\n"
      "v_mfma_f32_16x16x32_bf16 v[80:83], a[148:151], a[36:39], v[80:83]\n"
      "s_waitcnt vmcnt(8)\n"
      "v_mfma_f32_16x16x32_bf16 v[64:67], a[152:155], a[52:55], v[64:67]\n"
      "v_mfma_f32_16x16x32_bf16 v[68:71], a[156:159], a[52:55], v[68:71]\n"
      "v_mfma_f32_16x16x32_bf16 v[72:75], a[160:163], a[52:55], v[72:75]\n"
      "buffer_load_dwordx4 a[96:99], v0, s[24:27], s90 offen offset:1024 sc1\n"
      "v_mfma_f32_16x16x32_bf16 v[76:79], a[164:167], a[52:55], v[76:79]\n"
      "v_mfma_f32_16x16x32_bf16 v[80:83], a[168:171], a[52:55], v[80:83]\n"
      "s_waitcnt vmcnt(5)\n"
      "v_mfma_f32_16x16x32_bf16 v[44:47], a[172:175], a[72:75], v[44:47]\n"
      "v_mfma_f32_16x16x32_bf16 v[48:51], a[176:179], a[72:75], v[48:51]\n"
      "v_mfma_f32_16x16x32_bf16 v[52:55], a[180:183], a[72:75], v[52:55]\n"
      "s_add_u32 s90, s53, 7872512\n"
      "buffer_load_dwordx4 a[56:59], v0, s[24:27], s90 offen offset:3072 sc1\n"
      "v_mfma_f32_16x16x32_bf16 v[56:59], a[184:187], a[72:75], v[56:59]\n"
      "v_mfma_f32_16x16x32_bf16 v[60:63], a[188:191], a[72:75], v[60:63]\n"
      "s_waitcnt vmcnt(5)\n"
      "v_mfma_f32_16x16x32_bf16 v[44:47], a[192:195], a[88:91], v[44:47]\n"
      "v_mfma_f32_16x16x32_bf16 v[48:51], a[196:199], a[88:91], v[48:51]\n"
      "s_add_u32 s91, s54, 7872512\n"
      "buffer_load_dwordx4 a[12:15], v0, s[24:27], s91 offen offset:0 sc1\n"
      "v_mfma_f32_16x16x32_bf16 v[52:55], a[200:203], a[88:91], v[52:55]\n"
      "v_mfma_f32_16x16x32_bf16 v[56:59], a[204:207], a[88:91], v[56:59]\n"
      "v_mfma_f32_16x16x32_bf16 v[60:63], a[208:211], a[88:91], v[60:63]\n"
      "v_mfma_f32_16x16x32_bf16 v[84:87], v[220:223], a[8:11], v[84:87]\n"
      "buffer_load_dwordx4 a[28:31], v0, s[24:27], s91 offen offset:1024 sc1\n"
      "s_add_u32 s80, s58, 102400\n"
      "v_add_u32 v154, s80, v0\n"
      "s_add_u32 s80, s55, 102400\n"
      "v_add_u32 v152, s80, v0\n"
      "v_mfma_f32_16x16x32_bf16 v[88:91], v[224:227], a[8:11], v[88:91]\n"
      "ds_write_b128 v152, v[44:47] offset:0\n"
      "v_mfma_f32_16x16x32_bf16 v[92:95], v[228:231], a[8:11], v[92:95]\n"
      "v_mfma_f32_16x16x32_bf16 v[96:99], v[232:235], a[8:11], v[96:99]\n"
      "v_mfma_f32_16x16x32_bf16 v[100:103], v[236:239], a[8:11], v[100:103]\n"
      "buffer_load_dwordx4 a[44:47], v0, s[24:27], s91 offen offset:2048 sc1\n"
      "ds_write_b128 v152, v[48:51] offset:1024\n"
      "v_mfma_f32_16x16x32_bf16 v[84:87], v[240:243], a[24:27], v[84:87]\n"
      "v_mfma_f32_16x16x32_bf16 v[88:91], v[244:247], a[24:27], v[88:91]\n"
      "v_mfma_f32_16x16x32_bf16 v[92:95], v[248:251], a[24:27], v[92:95]\n"
      "ds_write_b128 v152, v[52:55] offset:2048\n"
      "v_mfma_f32_16x16x32_bf16 v[96:99], v[252:255], a[24:27], v[96:99]\n"
      "buffer_load_dwordx4 a[60:63], v0, s[24:27], s91 offen offset:3072 sc1\n"
      "v_mfma_f32_16x16x32_bf16 v[100:103], a[64:67], a[24:27], v[100:103]\n"
      "s_waitcnt vmcnt(8)\n"
      "v_mfma_f32_16x16x32_bf16 v[64:67], a[172:175], a[76:79], v[64:67]\n"
      "v_mfma_f32_16x16x32_bf16 v[68:71], a[176:179], a[76:79], v[68:71]\n"
      "ds_write_b128 v152, v[56:59] offset:3072\n"
      "v_mfma_f32_16x16x32_bf16 v[72:75], a[180:183], a[76:79], v[72:75]\n"
      "s_add_u32 s91, s54, 7876608\n"
      "buffer_load_dwordx4 a[84:87], v0, s[24:27], s91 offen offset:0 sc1\n"
      "v_mfma_f32_16x16x32_bf16 v[76:79], a[184:187], a[76:79], v[76:79]\n"
      "v_mfma_f32_16x16x32_bf16 v[80:83], a[188:191], a[76:79], v[80:83]\n"
      "ds_write_b128 v152, v[60:63] offset:4096\n"
      "s_waitcnt vmcnt(8)\n"
      "v_mfma_f32_16x16x32_bf16 v[64:67], a[192:195], a[92:95], v[64:67]\n"
      "v_mfma_f32_16x16x32_bf16 v[68:71], a[196:199], a[92:95], v[68:71]\n"
      "v_mfma_f32_16x16x32_bf16 v[72:75], a[200:203], a[92:95], v[72:75]\n"
      "buffer_load_dwordx4 a[100:103], v0, s[24:27], s91 offen offset:1024 sc1\n"
      "v_mfma_f32_16x16x32_bf16 v[76:79], a[204:207], a[92:95], v[76:79]\n"
      "v_mfma_f32_16x16x32_bf16 v[80:83], a[208:211], a[92:95], v[80:83]\n"
      "v_mfma_f32_16x16x32_bf16 v[84:87], a[68:71], a[40:43], v[84:87]\n"
      "s_add_u32 s80, s56, 102400\n"
      "v_add_u32 v153, s80, v0\n"
      "v_mfma_f32_16x16x32_bf16 v[88:91], a[136:139], a[40:43], v[88:91]\n"
      "s_add_u32 s79, s65, 0\n"
      "s_add_u32 m0, s72, 0\n"
      "s_nop 0\n"
      "buffer_load_dwordx4 v0, s[36:39], s79 offen offset:0 sc1 lds\n"
      "ds_write_b128 v153, v[64:67] offset:0\n"
      "v_mfma_f32_16x16x32_bf16 v[92:95], a[140:143], a[40:43], v[92:95]\n"
      "v_mfma_f32_16x16x32_bf16 v[96:99], a[144:147], a[40:43], v[96:99]\n"
      "v_mfma_f32_16x16x32_bf16 v[100:103], a[148:151], a[40:43], v[100:103]\n"
      "ds_write_b128 v153, v[68:71] offset:1024\n"
      "s_waitcnt vmcnt(7)\n"
      "v_mfma_f32_16x16x32_bf16 v[84:87], a[152:155], a[56:59], v[84:87]\n"
      "buffer_load_dwordx4 v0, s[36:39], s79 offen offset:1024 sc1 lds\n"
      "v_mfma_f32_16x16x32_bf16 v[88:91], a[156:159], a[56:59], v[88:91]\n"
      "v_mfma_f32_16x16x32_bf16 v[92:95], a[160:163], a[56:59], v[92:95]\n"
      "ds_write_b128 v153, v[72:75] offset:2048\n"
      "v_mfma_f32_16x16x32_bf16 v[96:99], a[164:167], a[56:59], v[96:99]\n"
      "v_mfma_f32_16x16x32_bf16 v[100:103], a[168:171], a[56:59], v[100:103]\n"
      "buffer_load_dwordx4 v0, s[36:39], s79 offen offset:2048 sc1 lds\n"
      "v_mfma_f32_16x16x32_bf16 v[84:87], a[172:175], a[80:83], v[84:87]\n"
      "v_mfma_f32_16x16x32_bf16 v[88:91], a[176:179], a[80:83], v[88:91]\n"
      "ds_write_b128 v153, v[76:79] offset:3072\n"
      "v_mfma_f32_16x16x32_bf16 v[92:95], a[180:183], a[80:83], v[92:95]\n"
      "v_mfma_f32_16x16x32_bf16 v[96:99], a[184:187], a[80:83], v[96:99]\n"
      "buffer_load_dwordx4 v0, s[36:39], s79 offen offset:3072 sc1 lds\n"
      "v_mfma_f32_16x16x32_bf16 v[100:103], a[188:191], a[80:83], v[100:103]\n"
      "ds_write_b128 v153, v[80:83] offset:4096\n"
      "v_mfma_f32_16x16x32_bf16 v[84:87], a[192:195], a[96:99], v[84:87]\n"
      "v_mfma_f32_16x16x32_bf16 v[88:91], a[196:199], a[96:99], v[88:91]\n"
      "v_mfma_f32_16x16x32_bf16 v[92:95], a[200:203], a[96:99], v[92:95]\n"
      "s_add_u32 s79, s65, 4096\n"
      "s_add_u32 m0, s72, 4096\n"
      "s_nop 0\n"
      "buffer_load_dwordx4 v0, s[36:39], s79 offen offset:0 sc1 lds\n"
      "v_mfma_f32_16x16x32_bf16 v[96:99], a[204:207], a[96:99], v[96:99]\n"
      "v_mfma_f32_16x16x32_bf16 v[100:103], a[208:211], a[96:99], v[100:103]\n"
      "s_waitcnt vmcnt(10)\n"
      "v_mfma_f32_16x16x32_bf16 v[12:15], v[220:223], a[12:15], v[12:15]\n"
      "s_add_u32 s80, s57, 102400\n"
      "v_add_u32 v152, s80, v0\n"
      "v_mfma_f32_16x16x32_bf16 v[16:19], v[224:227], a[12:15], v[16:19]\n"
      "ds_write_b128 v152, v[84:87] offset:0\n"
      "v_mfma_f32_16x16x32_bf16 v[20:23], v[228:231], a[12:15], v[20:23]\n"
      "v_mfma_f32_16x16x32_bf16 v[24:27], v[232:235], a[12:15], v[24:27]\n"
      "v_mfma_f32_16x16x32_bf16 v[28:31], v[236:239], a[12:15], v[28:31]\n"
      "ds_write_b128 v152, v[88:91] offset:1024\n"
      "s_waitcnt vmcnt(9)\n"
      "v_mfma_f32_16x16x32_bf16 v[12:15], v[240:243], a[28:31], v[12:15]\n"
      "v_mfma_f32_16x16x32_bf16 v[16:19], v[244:247], a[28:31], v[16:19]\n"
      "v_mfma_f32_16x16x32_bf16 v[20:23], v[248:251], a[28:31], v[20:23]\n"
      "ds_write_b128 v152, v[92:95] offset:2048\n"
      "v_mfma_f32_16x16x32_bf16 v[24:27], v[252:255], a[28:31], v[24:27]\n"
      "v_mfma_f32_16x16x32_bf16 v[28:31], a[64:67], a[28:31], v[28:31]\n"
      "s_waitcnt vmcnt(8)\n"
      "v_mfma_f32_16x16x32_bf16 v[12:15], a[68:71], a[44:47], v[12:15]\n"
      "v_mfma_f32_16x16x32_bf16 v[16:19], a[136:139], a[44:47], v[16:19]\n"
      "ds_write_b128 v152, v[96:99] offset:3072\n"
      "v_mfma_f32_16x16x32_bf16 v[20:23], a[140:143], a[44:47], v[20:23]\n"
      "v_mfma_f32_16x16x32_bf16 v[24:27], a[144:147], a[44:47], v[24:27]\n"
      "v_mfma_f32_16x16x32_bf16 v[28:31], a[148:151], a[44:47], v[28:31]\n"
      "ds_write_b128 v152, v[100:103] offset:4096\n"
      "s_waitcnt vmcnt(7)\n"
      "v_mfma_f32_16x16x32_bf16 v[12:15], a[152:155], a[60:63], v[12:15]\n"
      "v_mfma_f32_16x16x32_bf16 v[16:19], a[156:159], a[60:63], v[16:19]\n"
      "v_mfma_f32_16x16x32_bf16 v[20:23], a[160:163], a[60:63], v[20:23]\n"
      "s_waitcnt lgkmcnt(0)\n"
      "s_barrier\n"
      "ds_read_b128 v[156:159], v154 offset:0\n"
      "v_mfma_f32_16x16x32_bf16 v[24:27], a[164:167], a[60:63], v[24:27]\n"
      "ds_read_b128 v[160:163], v154 offset:1024\n"
      "v_mfma_f32_16x16x32_bf16 v[28:31], a[168:171], a[60:63], v[28:31]\n"
      "ds_read_b128 v[164:167], v154 offset:2048\n"
      "s_waitcnt vmcnt(6)\n"
      "v_mfma_f32_16x16x32_bf16 v[12:15], a[172:175], a[84:87], v[12:15]\n"
      "ds_read_b128 v[168:171], v154 offset:3072\n"
      "v_mfma_f32_16x16x32_bf16 v[16:19], a[176:179], a[84:87], v[16:19]\n"
      "ds_read_b128 v[172:175], v154 offset:4096\n"
      "v_mfma_f32_16x16x32_bf16 v[20:23], a[180:183], a[84:87], v[20:23]\n"
      "ds_read_b128 v[176:179], v154 offset:5120\n"
      "v_mfma_f32_16x16x32_bf16 v[24:27], a[184:187], a[84:87], v[24:27]\n"
      "ds_read_b128 v[180:183], v154 offset:6144\n"
      "v_mfma_f32_16x16x32_bf16 v[28:31], a[188:191], a[84:87], v[28:31]\n"
      "ds_read_b128 v[184:187], v154 offset:7168\n"
      "s_waitcnt vmcnt(5)\n"
      "v_mfma_f32_16x16x32_bf16 v[12:15], a[192:195], a[100:103], v[12:15]\n"
      "ds_read_b128 v[188:191], v154 offset:8192\n"
      "v_mfma_f32_16x16x32_bf16 v[16:19], a[196:199], a[100:103], v[16:19]\n"
      "ds_read_b128 v[192:195], v154 offset:9216\n"
      "v_mfma_f32_16x16x32_bf16 v[20:23], a[200:203], a[100:103], v[20:23]\n"
      "ds_read_b128 v[196:199], v154 offset:10240\n"
      "v_mfma_f32_16x16x32_bf16 v[24:27], a[204:207], a[100:103], v[24:27]\n"
      "ds_read_b128 v[200:203], v154 offset:11264\n"
      "v_mfma_f32_16x16x32_bf16 v[28:31], a[208:211], a[100:103], v[28:31]\n"
      "ds_read_b128 v[204:207], v154 offset:12288\n"
      "ds_read_b128 v[208:211], v154 offset:13312\n"
      "ds_read_b128 v[212:215], v154 offset:14336\n"
      "s_waitcnt lgkmcnt(9)\n"
      "v_pk_add_f32 v[156:157], v[156:157], v[176:177]\n"
      "v_pk_add_f32 v[158:159], v[158:159], v[178:179]\n"
      "s_waitcnt lgkmcnt(8)\n"
      "v_pk_add_f32 v[160:161], v[160:161], v[180:181]\n"
      "v_pk_add_f32 v[162:163], v[162:163], v[182:183]\n"
      "s_waitcnt lgkmcnt(7)\n"
      "v_pk_add_f32 v[164:165], v[164:165], v[184:185]\n"
      "v_pk_add_f32 v[166:167], v[166:167], v[186:187]\n"
      "s_waitcnt lgkmcnt(6)\n"
      "v_pk_add_f32 v[168:169], v[168:169], v[188:189]\n"
      "v_pk_add_f32 v[170:171], v[170:171], v[190:191]\n"
      "s_waitcnt lgkmcnt(5)\n"
      "v_pk_add_f32 v[172:173], v[172:173], v[192:193]\n"
      "v_pk_add_f32 v[174:175], v[174:175], v[194:195]\n"
      "v_pk_add_f32 v[12:13], v[12:13], v[156:157]\n"
      "v_pk_add_f32 v[14:15], v[14:15], v[158:159]\n"
      "v_pk_add_f32 v[16:17], v[16:17], v[160:161]\n"
      "v_pk_add_f32 v[18:19], v[18:19], v[162:163]\n"
      "v_pk_add_f32 v[20:21], v[20:21], v[164:165]\n"
      "v_pk_add_f32 v[22:23], v[22:23], v[166:167]\n"
      "v_pk_add_f32 v[24:25], v[24:25], v[168:169]\n"
      "v_pk_add_f32 v[26:27], v[26:27], v[170:171]\n"
      "v_pk_add_f32 v[28:29], v[28:29], v[172:173]\n"
      "v_pk_add_f32 v[30:31], v[30:31], v[174:175]\n"
      "s_waitcnt lgkmcnt(4)\n"
      "v_pk_add_f32 v[12:13], v[12:13], v[196:197]\n"
      "v_pk_add_f32 v[14:15], v[14:15], v[198:199]\n"
      "s_waitcnt lgkmcnt(3)\n"
      "v_pk_add_f32 v[16:17], v[16:17], v[200:201]\n"
      "v_pk_add_f32 v[18:19], v[18:19], v[202:203]\n"
      "s_waitcnt lgkmcnt(2)\n"
      "v_pk_add_f32 v[20:21], v[20:21], v[204:205]\n"
      "v_pk_add_f32 v[22:23], v[22:23], v[206:207]\n"
      "s_waitcnt lgkmcnt(1)\n"
      "v_pk_add_f32 v[24:25], v[24:25], v[208:209]\n"
      "v_pk_add_f32 v[26:27], v[26:27], v[210:211]\n"
      "s_waitcnt lgkmcnt(0)\n"
      "v_pk_add_f32 v[28:29], v[28:29], v[212:213]\n"
      "v_pk_add_f32 v[30:31], v[30:31], v[214:215]\n"
      "v_cvt_pk_bf16_f32 v44, v12, v13\n"
      "v_cvt_pk_bf16_f32 v45, v14, v15\n"
      "v_cvt_pk_bf16_f32 v46, v16, v17\n"
      "v_cvt_pk_bf16_f32 v47, v18, v19\n"
      "buffer_store_dwordx4 v[44:47], v32, s[28:31], s81 offen sc1\n"
      "v_cvt_pk_bf16_f32 v48, v20, v21\n"
      "v_cvt_pk_bf16_f32 v49, v22, v23\n"
      "v_cvt_pk_bf16_f32 v50, v24, v25\n"
      "v_cvt_pk_bf16_f32 v51, v26, v27\n"
      "buffer_store_dwordx4 v[48:51], v32, s[28:31], s82 offen sc1\n"
      "v_cvt_pk_bf16_f32 v52, v28, v29\n"
      "v_cvt_pk_bf16_f32 v53, v30, v31\n"
      "buffer_store_dwordx2 v[52:53], v33, s[28:31], s83 offen sc1\n"
      "v_pk_add_f32 v[56:57], v[12:13], v[14:15]\n"
      "v_pk_mul_f32 v[58:59], v[12:13], v[12:13]\n"
      "v_pk_fma_f32 v[58:59], v[14:15], v[14:15], v[58:59]\n"
      "v_pk_add_f32 v[56:57], v[56:57], v[16:17]\n"
      "v_pk_fma_f32 v[58:59], v[16:17], v[16:17], v[58:59]\n"
      "v_pk_add_f32 v[56:57], v[56:57], v[18:19]\n"
      "v_pk_fma_f32 v[58:59], v[18:19], v[18:19], v[58:59]\n"
      "v_pk_add_f32 v[56:57], v[56:57], v[20:21]\n"
      "v_pk_fma_f32 v[58:59], v[20:21], v[20:21], v[58:59]\n"
      "v_pk_add_f32 v[56:57], v[56:57], v[22:23]\n"
      "v_pk_fma_f32 v[58:59], v[22:23], v[22:23], v[58:59]\n"
      "v_pk_add_f32 v[56:57], v[56:57], v[24:25]\n"
      "v_pk_fma_f32 v[58:59], v[24:25], v[24:25], v[58:59]\n"
      "v_pk_add_f32 v[56:57], v[56:57], v[26:27]\n"
      "v_pk_fma_f32 v[58:59], v[26:27], v[26:27], v[58:59]\n"
      "v_pk_add_f32 v[56:57], v[56:57], v[28:29]\n"
      "v_pk_fma_f32 v[58:59], v[28:29], v[28:29], v[58:59]\n"
      "v_pk_add_f32 v[56:57], v[56:57], v[30:31]\n"
      "v_pk_fma_f32 v[58:59], v[30:31], v[30:31], v[58:59]\n"
      "v_add_f32 v56, v56, v57\n"
      "v_add_f32 v58, v58, v59\n"
      "s_nop 1\n"
      "v_permlane32_swap_b32 v56, v58\n"
      "v_add_f32 v56, v56, v58\n"
      "v_mov_b32 v58, v56\n"
      "s_nop 1\n"
      "v_permlane16_swap_b32 v56, v58\n"
      "v_add_f32 v56, v56, v58\n"
      "s_add_u32 s76, s70, 15728640\n"
      "buffer_store_dword v56, v34, s[24:27], s76 offen offset:0 sc1\n"
      "v_mov_b32 v36, s34\n"
      "v_mov_b32 v37, s35\n"
      "v_mov_b32 v38, %[sl]\n"
      "s_mov_b64 exec, 1\n"
      "buffer_store_dwordx2 v[36:37], v38, s[36:39], 0 offen sc1\n"
      "s_mov_b64 exec, -1\n"
      "s_waitcnt vmcnt(0)\n"
      "s_cmp_lg_u32 s41, 0\n"
      "s_cbranch_scc1 Lend_%=\n"
      "s_bfe_u32 s80, s74, 262153\n"
      "s_mul_i32 s80, s80, 64\n"
      "s_lshl2_add_u32 s80, s73, s80\n"
      "s_add_u32 s80, s80, 32505856\n"
      "s_barrier\n"
      "s_cmp_lg_u32 s40, 0\n"
      "s_cbranch_scc1 Lsg16_%=\n"
      "s_mov_b64 exec, 1\n"
      "buffer_store_dword v10, v9, s[24:27], s80 offen sc1\n"
      "s_mov_b64 exec, -1\n"
      "Lsg16_%=:\n"
      "s_mov_b32 s41, 1\n"
      "s_mov_b64 s[28:29], %[o]\n"
      "s_mov_b32 s30, 2621440\n"
      "s_mov_b32 s81, s46\n"
      "s_add_u32 s82, s46, 64\n"
      "s_add_u32 s83, s46, 128\n"
      "v_bfe_u32 v36, v0, 4, 4\n"
      "v_mul_u32_u24 v36, 1280, v36\n"
      "v_bfe_u32 v37, v0, 8, 2\n"
      "v_lshl_add_u32 v32, v37, 4, v36\n"
      "v_and_b32 v38, 1, v37\n"
      "v_lshl_add_u32 v36, v38, 4, v36\n"
      "v_lshrrev_b32 v37, 1, v37\n"
      "v_lshl_add_u32 v33, v37, 3, v36\n"
      "s_mov_b32 s70, s45\n"
      "s_cmp_lg_u32 s40, 0\n"
      "s_cbranch_scc1 Lsd17_%=\n"
      "s_lshl2_add_u32 s80, s73, 32505856\n"
      "v_mul_u32_u24 v43, 16, v0\n"
      "v_lshrrev_b32 v43, 2, v43\n"
      "s_sleep 6\n"
      "s_mov_b32 s84, 400000\n"
      "s_mov_b64 exec, 65535\n"
      "Lpl17_%=:\n"
      "buffer_load_dword v40, v43, s[24:27], s80 offen sc1\n"
      "s_waitcnt vmcnt(0)\n"
      "v_cmp_gt_u32 vcc, 1, v40\n"
      "s_cbranch_vccz Lpx17_%=\n"
      "s_sub_u32 s84, s84, 1\n"
      "s_cmp_lg_u32 s84, 0\n"
      "s_cbranch_scc1 Lpl17_%=\n"
      "Lpx17_%=:\n"
      "s_mov_b64 exec, -1\n"
      "s_branch Lpd17_%=\n"
      "Lsd17_%=:\n"
      "s_mov_b32 s79, 61440\n"
      "s_cmp_eq_u32 s40, 1\n"
      "s_cselect_b32 s79, 40960, s79\n"
      "s_cmp_eq_u32 s40, 3\n"
      "s_cselect_b32 s79, 81920, s79\n"
      "s_add_u32 s79, s79, s65\n"
      "s_sub_u32 s79, s79, s72\n"
      "s_add_u32 s84, s40, 1\n"
      "s_mul_i32 m0, s84, 20480\n"
      "s_mov_b32 s84, 5\n"
      "Lsl17_%=:\n"
      "buffer_load_dwordx4 v0, s[20:23], s79 offen offset:0 sc1 lds\n"
      "buffer_load_dwordx4 v0, s[20:23], s79 offen offset:1024 sc1 lds\n"
      "buffer_load_dwordx4 v0, s[20:23], s79 offen offset:2048 sc1 lds\n"
      "buffer_load_dwordx4 v0, s[20:23], s79 offen offset:3072 sc1 lds\n"
      "s_add_u32 s79, s79, 4096\n"
      "s_add_u32 m0, m0, 4096\n"
      "s_sub_u32 s84, s84, 1\n"
      "s_cmp_lg_u32 s84, 0\n"
      "s_cbranch_scc1 Lsl17_%=\n"
      "Lpd17_%=:\n"
      "s_mov_b32 s38, 0\n"
      "s_add_u32 s24, s24, 16449536\n"
      "s_addc_u32 s25, s25, 0\n"
      "s_barrier\n"
      "s_branch Lloop_%=\n"
      "Lend_%=:\n"
      "Lkend:\n"
      "s_waitcnt vmcnt(0)\n"
      : "+{v[200:203]}"(e0), "+{v[204:207]}"(e1), "+{v[208:211]}"(e2), "+{v[212:215]}"(e3), "+{v[216:219]}"(src),
        "+{v[220:223]}"(dst), "+{v[224:227]}"(par), "+{s[20:21]}"(pw), "+{s[28:29]}"(px), "+{s[36:37]}"(pm)
      : [o] "{s[18:19]}"(out), [b] "{s17}"(b), [w] "{s16}"(w), [sl] "{s15}"(__builtin_amdgcn_readfirstlane(slot)), [lp] "s"(lp)
      : "v0", "v1", "v2", "v3", "v4", "v5", "v6", "v7", "v8", "v9", "v10", "v11", "v12", "v13", "v14", "v15", "v16", "v17", "v18", "v19", "v20", "v21", "v22", "v23", "v24", "v25", "v26", "v27", "v28", "v29", "v30", "v31", "v32", "v33", "v34", "v35", "v36", "v37", "v38", "v39", "v40", "v41", "v42", "v43", "v44", "v45", "v46", "v47", "v48", "v49", "v50", "v51", "v52", "v53", "v54", "v55", "v56", "v57", "v58", "v59", "v60", "v61", "v62", "v63", "v64", "v65", "v66", "v67", "v68", "v69", "v70", "v71", "v72", "v73", "v74", "v75", "v76", "v77", "v78", "v79", "v80", "v81", "v82", "v83", "v84", "v85", "v86", "v87", "v88", "v89", "v90", "v91", "v92", "v93", "v94", "v95", "v96", "v97", "v98", "v99", "v100", "v101", "v102", "v103", "v104", "v105", "v106", "v107", "v108", "v109", "v110", "v111", "v112", "v113", "v114", "v115", "v116", "v117", "v118", "v119", "v120", "v121", "v122", "v123", "v124", "v125", "v126", "v127", "v128", "v129", "v130", "v131", "v132", "v133", "v134", "v135", "v136", "v137", "v138", "v139", "v140", "v141", "v142", "v143", "v144", "v145", "v146", "v147", "v148", "v149", "v150", "v151", "v152", "v153", "v154", "v155", "v156", "v157", "v158", "v159", "v160", "v161", "v162", "v163", "v164", "v165", "v166", "v167", "v168", "v169", "v170", "v171", "v172", "v173", "v174", "v175", "v176", "v177", "v178", "v179", "v180", "v181", "v182", "v183", "v184", "v185", "v186", "v187", "v188", "v189", "v190", "v191", "v192", "v193", "v194", "v195", "v196", "v197", "v198", "v199", "v228", "v229", "v230", "v231", "v232", "v233", "v234", "v235", "v236", "v237", "v238", "v239", "v240", "v241", "v242", "v243", "v244", "v245", "v246", "v247", "v248", "v249", "v250", "v251", "v252", "v253", "v254", "v255", "a0", "a1", "a2", "a3", "a4", "a5", "a6", "a7", "a8", "a9", "a10", "a11", "a12", "a13", "a14", "a15", "a16", "a17", "a18", "a19", "a20", "a21", "a22", "a23", "a24", "a25", "a26", "a27", "a28", "a29", "a30", "a31", "a32", "a33", "a34", "a35", "a36", "a37", "a38", "a39", "a40", "a41", "a42", "a43", "a44", "a45", "a46", "a47", "a48", "a49", "a50", "a51", "a52", "a53", "a54", "a55", "a56", "a57", "a58", "a59", "a60", "a61", "a62", "a63", "a64", "a65", "a66", "a67", "a68", "a69", "a70", "a71", "a72", "a73", "a74", "a75", "a76", "a77", "a78", "a79", "a80", "a81", "a82", "a83", "a84", "a85", "a86", "a87", "a88", "a89", "a90", "a91", "a92", "a93", "a94", "a95", "a96", "a97", "a98", "a99", "a100", "a101", "a102", "a103", "a104", "a105", "a106", "a107", "a108", "a109", "a110", "a111", "a112", "a113", "a114", "a115", "a116", "a117", "a118", "a119", "a120", "a121", "a122", "a123", "a124", "a125", "a126", "a127", "a128", "a129", "a130", "a131", "a132", "a133", "a134", "a135", "a136", "a137", "a138", "a139", "a140", "a141", "a142", "a143", "a144", "a145", "a146", "a147", "a148", "a149", "a150", "a151", "a152", "a153", "a154", "a155", "a156", "a157", "a158", "a159", "a160", "a161", "a162", "a163", "a164", "a165", "a166", "a167", "a168", "a169", "a170", "a171", "a172", "a173", "a174", "a175", "a176", "a177", "a178", "a179", "a180", "a181", "a182", "a183", "a184", "a185", "a186", "a187", "a188", "a189", "a190", "a191", "a192", "a193", "a194", "a195", "a196", "a197", "a198", "a199", "a200", "a201", "a202", "a203", "a204", "a205", "a206", "a207", "a208", "a209", "a210", "a211", "a212", "a213", "a214", "a215", "a216", "a217", "a218", "a219", "a220", "a221", "a222", "a223", "a224", "a225", "a226", "a227", "a228", "a229", "a230", "a231", "a232", "a233", "a234", "a235", "a236", "a237", "a238", "a239", "a240", "a241", "a242", "a243", "a244", "a245", "a246", "a247", "a248", "a249", "a250", "a251", "a252", "a253", "a254", "a255", "s22", "s23", "s24", "s25", "s26", "s27", "s30", "s31", "s34", "s35", "s38", "s39", "s40", "s41", "s42", "s43", "s44", "s45", "s46", "s47", "s48", "s49", "s50", "s51", "s52", "s53", "s54", "s55", "s56", "s57", "s58", "s59", "s60", "s61", "s62", "s63", "s64", "s65", "s66", "s67", "s68", "s69", "s70", "s71", "s72", "s73", "s74", "s75", "s76", "s77", "s78", "s79", "s80", "s81", "s82", "s83", "s84", "s85", "s86", "s87", "s88", "s89", "s90", "s91", "s92", "s93", "s94", "s95", "s96", "s97", "s98", "s99", "s14", "vcc", "scc", "memory");
}

extern "C" void kernel_launch(void* const* d_in, const int* in_sizes, int n_in, void* d_out, int out_size, void* d_ws,
                              size_t ws_size, hipStream_t stream) {
  k_main<<<256, 256, 0, stream>>>((W)d_in[0], (W)d_in[1], (W)d_in[2], (W)d_in[4], (W)d_in[5], (W)d_in[6], (char*)d_ws, (char*)d_out);
}
```
